```python
import torch

def _softmax(x, axis=-1):
    return torch.softmax(x, dim=axis)
def _gelu(x):
    return torch.nn.functional.gelu(x, approximate="tanh")
def _silu(x):
    return torch.nn.functional.silu(x)
def _sigmoid(x):
    return torch.sigmoid(x)
def _layernorm(x, axis=-1, eps=1e-5):
    m = x.mean(dim=axis, keepdim=True)
    v = x.var(dim=axis, keepdim=True, unbiased=False)
    return (x - m) / torch.sqrt(v + eps)
def _scatter_add(tgt, idx, src, axis=0):
    return tgt.clone().index_add_(axis, idx, src)
def _segment_sum(src, seg, n):
    out = torch.zeros((n,) + src.shape[1:], dtype=src.dtype)
    return out.index_add_(0, seg, src)
def _pad(x, pw):
    flat = tuple(v for pair in reversed(pw) for v in pair)
    return torch.nn.functional.pad(x, flat)

kernel_name = 'parallel_sum__sliding_local_dilated__glu_gated_bilinear__prompt'

def setup_inputs():

    _w = (lambda a: (lambda t: t.to(torch.bfloat16) if t.is_floating_point() else t)(torch.as_tensor(a)))
    def _A_setup():
        B, S, D = 1, 2048, 1024
        return {
            "x": _w((lambda *s: torch.randn(*s, dtype=torch.bfloat16) * 0.05)(B, S, D)),
            "wqkv": _w((lambda *s: torch.randn(*s, dtype=torch.bfloat16) * 0.05)(D, 3 * D)),
            "wo": _w((lambda *s: torch.randn(*s, dtype=torch.bfloat16) * 0.05)(D, D)),
        }
    _A_in = _A_setup()

    def _B_setup():
        B, S, D, F = 1, 2048, 1024, 3584
        return {
            "x": _w((lambda *s: torch.randn(*s, dtype=torch.bfloat16) * 0.05)(B, S, D)),
            "w_gate": _w((lambda *s: torch.randn(*s, dtype=torch.bfloat16) * 0.8)(D, F) * (D ** -0.5)),
            "w_up": _w((lambda *s: torch.randn(*s, dtype=torch.bfloat16) * 0.8)(D, F) * (D ** -0.5)),
            "w_down": _w((lambda *s: torch.randn(*s, dtype=torch.bfloat16) * 0.8)(F, D) * (F ** -0.5)),
        }
    _B_in = _B_setup()

    return {
        "A_x": _A_in["x"],
        "A_wqkv": _A_in["wqkv"],
        "A_wo": _A_in["wo"],
        "B_x": _B_in["x"],
        "B_w_gate": _B_in["w_gate"],
        "B_w_up": _B_in["w_up"],
        "B_w_down": _B_in["w_down"],
    }

def reference(A_x, A_wqkv, A_wo, B_x, B_w_gate, B_w_up, B_w_down):
    def _A_ref(x, wqkv, wo):
        B, S, D = x.shape
        H = 32; Dh = D // H
        W, dil = 8, 2
        qkv = (lambda x, *d: x.permute(*d))((x @ wqkv).reshape(B, S, 3, H, Dh), 2, 0, 3, 1, 4)
        q, k, v = qkv[0], qkv[1], qkv[2]
        scale = Dh ** -0.5
        pos = torch.arange(S)
        off = torch.arange(W) * dil
        idx = pos[:, None] - off[None, :]
        mask = idx >= 0
        idx_c = torch.where(mask, idx, 0).reshape(-1)
        kg = k[:, :, idx_c, :].reshape(B, H, S, W, Dh)
        vg = v[:, :, idx_c, :].reshape(B, H, S, W, Dh)
        logits = torch.einsum("bhsd,bhswd->bhsw", q, kg) * scale
        logits = torch.where(mask[None, None, :, :], logits, -1e9)
        attn = _softmax(logits, axis=-1)
        o = torch.einsum("bhsw,bhswd->bhsd", attn, vg)
        return (lambda x, *d: x.permute(*d))(o, 0, 2, 1, 3).reshape(B, S, D) @ wo

    def _B_ref(x, w_gate, w_up, w_down):
        g = torch.einsum("bsd,df->bsf", x, w_gate)
        u = torch.einsum("bsd,df->bsf", x, w_up)
        h = g * u
        return torch.einsum("bsf,fd->bsd", h, w_down)

    x = A_x
    xn = _layernorm(x, axis=-1)
    return x + _A_ref(xn, A_wqkv, A_wo) + _B_ref(xn, B_w_gate, B_w_up, B_w_down)
```

```cpp
#include <hip/hip_runtime.h>
#include <hip/hip_bf16.h>

typedef unsigned short u16;
__device__ inline float bf2f(u16 b) { return __uint_as_float(((unsigned)b) << 16); }
__device__ inline u16 f2bf(float f) { unsigned u = __float_as_uint(f); u += 0x7FFFu + ((u >> 16) & 1u); return (u16)(u >> 16); }

constexpr int S = 2048, D = 1024, H = 32, DH = 32, F = 3584;

__global__ void ln_k(const u16* x, u16* xn) {
    int row = blockIdx.x; __shared__ float red[256];
    float v[4]; float s = 0;
    for (int i = 0; i < 4; ++i) { v[i] = bf2f(x[row * D + threadIdx.x * 4 + i]); s += v[i]; }
    red[threadIdx.x] = s; __syncthreads();
    for (int o = 128; o > 0; o >>= 1) { if (threadIdx.x < o) red[threadIdx.x] += red[threadIdx.x + o]; __syncthreads(); }
    float m = red[0] / D; __syncthreads();
    float q = 0; for (int i = 0; i < 4; ++i) { float d = v[i] - m; q += d * d; }
    red[threadIdx.x] = q; __syncthreads();
    for (int o = 128; o > 0; o >>= 1) { if (threadIdx.x < o) red[threadIdx.x] += red[threadIdx.x + o]; __syncthreads(); }
    float r = 1.0f / sqrtf(red[0] / D + 1e-5f);
    for (int i = 0; i < 4; ++i) xn[row * D + threadIdx.x * 4 + i] = f2bf((v[i] - m) * r);
}
__global__ void gemm_k(const u16* A, int lda, const u16* B, int ldb, float* C, int ldc, int K, int accum) {
    __shared__ float As[16][17], Bs[16][17];
    int r = blockIdx.y * 16 + threadIdx.y, c = blockIdx.x * 16 + threadIdx.x; float acc = 0;
    for (int k0 = 0; k0 < K; k0 += 16) {
        As[threadIdx.y][threadIdx.x] = bf2f(A[(size_t)r * lda + k0 + threadIdx.x]);
        Bs[threadIdx.y][threadIdx.x] = bf2f(B[(size_t)(k0 + threadIdx.y) * ldb + c]);
        __syncthreads();
        for (int k = 0; k < 16; ++k) acc += As[threadIdx.y][k] * Bs[k][threadIdx.x];
        __syncthreads();
    }
    if (accum) C[(size_t)r * ldc + c] += acc; else C[(size_t)r * ldc + c] = acc;
}
__global__ void attn_k(const float* qkv, u16* o) {
    int t = blockIdx.x * blockDim.x + threadIdx.x; int s = t / H, h = t % H;
    float q[DH]; for (int d = 0; d < DH; ++d) q[d] = bf2f(f2bf(qkv[(size_t)s * 3 * D + h * DH + d]));
    float lg[8]; float mx = -3e38f;
    for (int w = 0; w < 8; ++w) { int j = s - 2 * w; if (j < 0) { lg[w] = -1e9f; } else { float a = 0; for (int d = 0; d < DH; ++d) a += q[d] * bf2f(f2bf(qkv[(size_t)j * 3 * D + D + h * DH + d])); lg[w] = a * 0.17677669529663687f; } mx = fmaxf(mx, lg[w]); }
    float sum = 0; for (int w = 0; w < 8; ++w) { lg[w] = __expf(lg[w] - mx); sum += lg[w]; }
    for (int d = 0; d < DH; ++d) { float a = 0; for (int w = 0; w < 8; ++w) { int j = s - 2 * w; if (j < 0) j = 0; a += lg[w] / sum * bf2f(f2bf(qkv[(size_t)j * 3 * D + 2 * D + h * DH + d])); } o[(size_t)s * D + h * DH + d] = f2bf(a); }
}
__global__ void glu_k(const float* g, const float* u, u16* h, int n) { int i = blockIdx.x * blockDim.x + threadIdx.x; if (i < n) h[i] = f2bf(bf2f(f2bf(g[i])) * bf2f(f2bf(u[i]))); }
__global__ void fin_k(const u16* x, const float* acc, u16* out, int n) { int i = blockIdx.x * blockDim.x + threadIdx.x; if (i < n) out[i] = f2bf(bf2f(x[i]) + acc[i]); }

extern "C" void kernel_launch(void* const* d_in, const int* in_sizes, int n_in,
                              void* d_out, int out_size, void* d_ws, size_t ws_size,
                              hipStream_t stream) {
    const u16* x = (const u16*)d_in[0]; const u16* wqkv = (const u16*)d_in[1]; const u16* wo = (const u16*)d_in[2];
    const u16* wg = (const u16*)d_in[4]; const u16* wu = (const u16*)d_in[5]; const u16* wd = (const u16*)d_in[6];
    char* ws = (char*)d_ws;
    u16* xn = (u16*)ws; ws += (size_t)S * D * 2;
    float* qkv = (float*)ws; ws += (size_t)S * 3 * D * 4;
    u16* o = (u16*)ws; ws += (size_t)S * D * 2;
    float* g = (float*)ws; ws += (size_t)S * F * 4;
    float* u = (float*)ws; ws += (size_t)S * F * 4;
    u16* h = (u16*)ws; ws += (size_t)S * F * 2;
    float* acc = (float*)ws; ws += (size_t)S * D * 4;
    ln_k<<<S, 256, 0, stream>>>(x, xn);
    gemm_k<<<dim3(3 * D / 16, S / 16), dim3(16, 16), 0, stream>>>(xn, D, wqkv, 3 * D, qkv, 3 * D, D, 0);
    attn_k<<<S * H / 256, 256, 0, stream>>>(qkv, o);
    gemm_k<<<dim3(F / 16, S / 16), dim3(16, 16), 0, stream>>>(xn, D, wg, F, g, F, D, 0);
    gemm_k<<<dim3(F / 16, S / 16), dim3(16, 16), 0, stream>>>(xn, D, wu, F, u, F, D, 0);
    glu_k<<<S * F / 256, 256, 0, stream>>>(g, u, h, S * F);
    gemm_k<<<dim3(D / 16, S / 16), dim3(16, 16), 0, stream>>>(o, D, wo, D, acc, D, D, 0);
    gemm_k<<<dim3(D / 16, S / 16), dim3(16, 16), 0, stream>>>(h, F, wd, D, acc, D, F, 1);
    fin_k<<<S * D / 256, 256, 0, stream>>>(x, acc, (u16*)d_out, S * D);
}
```

```cpp
#define OUT_AUX 16
#define K2TAIL 1
#include <hip/hip_runtime.h>
#include <hip/hip_bf16.h>

typedef unsigned short u16;
typedef unsigned int u32;
typedef float f32x4 __attribute__((ext_vector_type(4)));
typedef float f32x2 __attribute__((ext_vector_type(2)));
typedef u32 u32x2 __attribute__((ext_vector_type(2)));
typedef u32 u32x4 __attribute__((ext_vector_type(4)));
typedef u32 u32x8 __attribute__((ext_vector_type(8)));
typedef u32 u32x16 __attribute__((ext_vector_type(16)));
typedef __bf16 bf16x2 __attribute__((ext_vector_type(2)));
typedef __bf16 bf16x8 __attribute__((ext_vector_type(8)));
#define GAS __attribute__((address_space(1)))
#define LAS __attribute__((address_space(3)))

constexpr int S = 2048, D = 1024, F = 3584;
constexpr size_t WS_XNF = 0;
constexpr size_t WS_A2F = 4u << 20;
constexpr size_t A2F_BYTES = 144u * 128u * 1024u;
constexpr size_t WS_SLAB = 24u << 20;
constexpr size_t SLAB_BYTES = 256u * 16384u;
constexpr size_t WS_FLAG = 23u * (1u << 20) + (1u << 19);
constexpr u32 FL_LN = 0, FL_TILE = 32768, FL_EPOCH = 65536, FL_BYTES = 65536 + 128;
#define SNPF 1
constexpr int K12_LDS = 159744;

__device__ __forceinline__ u32 pk(float a, float b) { f32x2 f = {a, b}; bf16x2 r = __builtin_convertvector(f, bf16x2); return __builtin_bit_cast(u32, r); }
__device__ __forceinline__ float bflo(u32 x) { return __uint_as_float(x << 16); }
__device__ __forceinline__ float bfhi(u32 x) { return __uint_as_float(x & 0xffff0000u); }
__device__ __forceinline__ u32x4 mk_srd(const void* p, u32 bytes) {
  unsigned long long q = (unsigned long long)p; u32x4 r; r[0] = (u32)q; r[1] = (u32)(q >> 32) & 0xffffu; r[2] = bytes; r[3] = 0x00020000u; return r; }
__device__ __forceinline__ f32x4 mfma16(u32x4 A, u32x4 B, f32x4 C) {
  return __builtin_amdgcn_mfma_f32_16x16x32_bf16(__builtin_bit_cast(bf16x8, A), __builtin_bit_cast(bf16x8, B), C, 0, 0, 0); }

#define TS0_BEGIN
#define TS0(i)
#define TS0K
#define TS0_PAR
#define TS0_ARG
#define TS0_END
#define TS_DECL
#define TS_BEGIN(k)
#define TS(i)
#define TS_K1
#define TS_END(k)
__device__ __forceinline__ int fresh_lane() { int l; asm volatile("v_mbcnt_lo_u32_b32 %0, -1, 0\n\tv_mbcnt_hi_u32_b32 %0, -1, %0" : "=v"(l)); return l; }

__device__ __forceinline__ u32x8 pf_plan(int b, int w, const u16* wqkv, const u16* wg, const u16* wu, int round0) {
  unsigned long long code; int dm, df, de;
  asm volatile("s_getpc_b64 %0\nPFPC_%=:\n\ts_mov_b32 %1, k1m_begin-PFPC_%=\n\ts_mov_b32 %2, k1f_begin-PFPC_%=\n\ts_mov_b32 %3, k2code_end-PFPC_%=" : "=s"(code), "=s"(dm), "=s"(df), "=s"(de));
  const int d0 = dm < df ? dm : df;
  code += (long long)d0;
  const u32 csz = (u32)(de - d0);
  const int gw = 4 * b + w;
  unsigned long long base; u32 size, off, inc;
  if (gw < 192) { base = (unsigned long long)wqkv; size = 1024u * 6144u; off = gw * 4096u; inc = 192u * 4096u; }
  else if (gw < 416) { base = (unsigned long long)wg; size = 1024u * 7168u; off = (gw - 192) * 4096u; inc = 224u * 4096u; }
  else if (gw < 640) { base = (unsigned long long)wu; size = 1024u * 7168u; off = (gw - 416) * 4096u; inc = 224u * 4096u; }
  else { base = code; size = csz; off = (((b - 160) >> 3) * 4 + w) * 4096u; inc = 0; }
  u32x8 r; r[0] = (u32)base; r[1] = (u32)(base >> 32) & 0xffffu; r[2] = size; r[3] = 0x00020000u; r[4] = off + round0 * inc; r[5] = inc; r[6] = 0; r[7] = 0;
  return r;
}
#define SPIN_MAX (1u << 20)
__device__ __forceinline__ float xsum(float s) {
  s += __uint_as_float(__builtin_amdgcn_update_dpp(0, (int)__float_as_uint(s), 0x128, 0xf, 0xf, false));
  auto a = __builtin_amdgcn_permlane16_swap(__float_as_uint(s), __float_as_uint(s), false, false);
  s = __uint_as_float(a[0]) + __uint_as_float(a[1]);
  auto c = __builtin_amdgcn_permlane32_swap(__float_as_uint(s), __float_as_uint(s), false, false);
  return __uint_as_float(c[0]) + __uint_as_float(c[1]);
}

template <bool MIXED>
__device__ __forceinline__ void ln_phase(const u32x16& d, const u32x4& ep, const u32x4& srdX, char* ws, char* smem, int b, int w, int rb, unsigned long long& tag TS0_PAR) {
  const int l = fresh_lane(), i8 = l & 7;
  LAS float* red = (LAS float*)(LAS char*)(smem + 153600);
  TS0(1)
  const float piv = bflo(ep[2]);
  f32x2 dv[16];
#pragma unroll
  for (int j = 0; j < 16; ++j) dv[j] = (f32x2){bflo(d[j]), bfhi(d[j])} - (f32x2){piv, piv};
  f32x2 s0 = dv[0] + dv[1], s1 = dv[2] + dv[3], q0 = dv[0] * dv[0], q1 = dv[1] * dv[1];
  q0 = __builtin_elementwise_fma(dv[2], dv[2], q0); q1 = __builtin_elementwise_fma(dv[3], dv[3], q1);
#pragma unroll
  for (int j = 4; j < 16; j += 2) {
    s0 += dv[j]; s1 += dv[j + 1];
    q0 = __builtin_elementwise_fma(dv[j], dv[j], q0); q1 = __builtin_elementwise_fma(dv[j + 1], dv[j + 1], q1);
  }
  s0 += s1; q0 += q1;
  const float s = xsum(s0[0] + s0[1]), q = xsum(q0[0] + q0[1]);
  if (l < 8) *(LAS f32x2*)(red + 8 * l + 2 * w) = (f32x2){s, q};
  asm volatile("s_waitcnt lgkmcnt(0)\n\ts_barrier" ::: "memory");
  const f32x4 r0 = *(LAS f32x4*)(red + 8 * i8), r1 = *(LAS f32x4*)(red + 8 * i8 + 4);
  const float mean = ((r0[0] + r0[2]) + (r1[0] + r1[2])) * (1.0f / D);
  const float var = __builtin_fmaxf(__builtin_fmaf(-mean, mean, ((r0[1] + r0[3]) + (r1[1] + r1[3])) * (1.0f / D)), 0.f);
  const float rs = __builtin_amdgcn_rsqf(var + 1e-5f), nb = -mean * rs;
  const u32 so = (((8 * w + (l >> 5)) * 128 + (b >> 1)) * 64 + ((l >> 3) & 3) * 16 + 8 * (b & 1) + i8) * 16;
#pragma unroll
  for (int it = 0; it < 4; ++it) {
    u32x4 r;
#pragma unroll
    for (int j = 0; j < 4; ++j) { const f32x2 y = __builtin_elementwise_fma(dv[4 * it + j], (f32x2){rs, rs}, (f32x2){nb, nb}); r[j] = pk(y[0], y[1]); }
    asm volatile("buffer_store_dwordx4 %0, %1, %2, 0 offen sc1\n\ts_nop 1" : : "v"(r), "v"(so + it * 2 * 131072), "{s[40:43]}"(srdX) : "memory");
  }
  TS0(2)
  tag = (((unsigned long long)(u32)__builtin_amdgcn_readfirstlane(ep[1]) << 32) | (u32)__builtin_amdgcn_readfirstlane(ep[0])) + 1;
  asm volatile("" : "+s"(tag));
}
template <bool MIXED>
__device__ __forceinline__ void ln_publish(char* ws, char* smem, int b, int w, int rb, unsigned long long tag TS0_PAR) {
  const int l = fresh_lane();
  asm volatile("s_waitcnt vmcnt(0)\n\ts_barrier" ::: "memory");
  GAS unsigned long long* flg = (GAS unsigned long long*)(ws + WS_FLAG + FL_LN);
  if (w == 0 && l == 0) __hip_atomic_store(flg + b * 16, tag, __ATOMIC_RELAXED, __HIP_MEMORY_SCOPE_AGENT);
  TS0(3)
}
template <bool MIXED>
__device__ __forceinline__ void ln_poll(char* ws, int w, int rb, unsigned long long tag TS0_PAR) {
  const int l = fresh_lane();
  GAS unsigned long long* flg = (GAS unsigned long long*)(ws + WS_FLAG + FL_LN);
  const int hp = rb == 0 ? 0 : 32 * rb - 2;
  const bool need = l < (MIXED ? 10 : 8);
  const u32 fo = (l < 8 ? 32 * rb + 8 * w + l : hp + (l & 1)) * 16;
  for (u32 spins = 0; spins < SPIN_MAX; ++spins) {
    const unsigned long long f = __hip_atomic_load(flg + fo, __ATOMIC_RELAXED, __HIP_MEMORY_SCOPE_AGENT);
    if (!__any(need && f != tag)) break;
    __builtin_amdgcn_s_sleep(1);
  }
  TS0(4)
}

template <bool MIXED>
__device__ __forceinline__ void k1_body(const u16* __restrict__ x, const u16* __restrict__ wqkv, const u16* __restrict__ wg, const u16* __restrict__ wu, const u16* __restrict__ wo, const u16* __restrict__ wd, char* __restrict__ ws_, char* smem, const int w_, unsigned long long& tag) {
  TS_DECL
  char* ws = ws_; int w = w_;
  TS_BEGIN(0)
  TS0_BEGIN
  constexpr u32 SB = 37888, WREG = 17408;
  const u32 lds0 = (u32)(unsigned long long)(LAS char*)smem;
  int bq = blockIdx.x, wq = w_, lane;
  unsigned long long pws = (unsigned long long)ws_, pwo = (unsigned long long)wo, pwd = (unsigned long long)wd;
  asm volatile("" : "+s"(bq), "+s"(wq));
  u32x16 xd; u32x4 ep;
  {
    const int l0 = fresh_lane();
    const u32 vo = (8 * bq + (l0 & 7)) * 2048 + (8 * wq + (l0 >> 5)) * 64 + ((l0 >> 3) & 3) * 16;
    const u32 vp = (8 * bq + (l0 & 7)) * 2048;
    const u32x4 srdI = mk_srd(x, S * D * 2), srdE = mk_srd(ws_ + WS_FLAG + FL_EPOCH, 128);
    TS0K
    asm volatile("buffer_load_dwordx4 v[0:3], v20, %2, 0 offen\n\tbuffer_load_dwordx4 v[4:7], v20, %2, 0 offen offset:128\n\t"
                 "buffer_load_dwordx4 v[8:11], v20, %2, 0 offen offset:256\n\tbuffer_load_dwordx4 v[12:15], v20, %2, 0 offen offset:384\n\t"
                 "buffer_load_ushort v18, v21, %2, 0 offen\n\tbuffer_load_dwordx2 v[16:17], off, %3, 0 sc1"
                 : "={v[0:15]}"(xd), "={v[16:19]}"(ep) : "s"(srdI), "s"(srdE), "{v20}"(vo), "{v21}"(vp) : "memory");
  }
  u32x4 srdA, srdB, srdC, vinA;
  u32x8 soff;
  const u32x8 spf = pf_plan(bq, wq, wqkv, wg, wu, 0);
  u32x4 srdP = {spf[0], spf[1], spf[2], spf[3]};
  const u32 pfo = spf[4];
  u32 vpf;
  {
    const int l0 = fresh_lane(), rb = (bq >> 3) & 7, cb = 2 * (bq & 7) + ((bq >> 6) & 1), T0 = 16 * rb;
    const int lrow = 8 * wq + (l0 >> 3);
    const int f2l = ((lrow >> 1) & 1) | (((lrow >> 3) & 1) << 1);
    const int sch = (l0 & 7) ^ (f2l << 1);
    vinA[0] = (T0 + 4 * wq) * 1024 + l0 * 16;
    soff[0] = 0;
    soff[5] = 0; soff[6] = lds0 + wq * 1024; soff[7] = lds0 + wq * 4096;
    vpf = l0 * 64;
    if constexpr (MIXED) {
      vinA[1] = (u32)((rb == 0 ? 0 : T0 - 1) * 1024 + l0 * 16);
      vinA[2] = lrow * 6144 + sch * 16;
      vinA[3] = lrow * 7168 + sch * 16;
      srdA = mk_srd(wqkv, 1024u * 3072u * 2u); srdB = mk_srd(wg, 1024u * 3584u * 2u); srdC = mk_srd(wu, 1024u * 3584u * 2u);
      soff[1] = cb * 128; soff[2] = 2048 + cb * 128; soff[3] = 4096 + cb * 128; soff[4] = cb * 128;
  asm volatile(
    "k1m_begin:\n"
    "s_waitcnt lgkmcnt(0)\n"
    "s_mov_b32 m0, 155648\n"
    "s_nop 0\n"
    "buffer_load_dword v22, s[20:23], s74 offen lds\n"
    "s_add_u32 m0, s62, 17408\n"
    "s_nop 0\n"
    "buffer_load_dwordx4 v238, s[44:47], s57 offen lds\n"
    "s_add_u32 s57, s57, 196608\n"
    "s_add_u32 m0, s62, 21504\n"
    "s_nop 0\n"
    "buffer_load_dwordx4 v238, s[44:47], s58 offen lds\n"
    "s_add_u32 s58, s58, 196608\n"
    "s_add_u32 m0, s62, 25600\n"
    "s_nop 0\n"
    "buffer_load_dwordx4 v238, s[44:47], s59 offen lds\n"
    "s_add_u32 s59, s59, 196608\n"
    "s_add_u32 m0, s62, 29696\n"
    "s_nop 0\n"
    "buffer_load_dwordx4 v239, s[48:51], s60 offen lds\n"
    "s_add_u32 m0, s62, 33792\n"
    "s_nop 0\n"
    "buffer_load_dwordx4 v239, s[52:55], s60 offen lds\n"
    "s_add_u32 s60, s60, 229376\n"
    "s_add_u32 m0, s62, 55296\n"
    "s_nop 0\n"
    "buffer_load_dwordx4 v238, s[44:47], s57 offen lds\n"
    "s_add_u32 s57, s57, 196608\n"
    "s_add_u32 m0, s62, 59392\n"
    "s_nop 0\n"
    "buffer_load_dwordx4 v238, s[44:47], s58 offen lds\n"
    "s_add_u32 s58, s58, 196608\n"
    "s_add_u32 m0, s62, 63488\n"
    "s_nop 0\n"
    "buffer_load_dwordx4 v238, s[44:47], s59 offen lds\n"
    "s_add_u32 s59, s59, 196608\n"
    "s_add_u32 m0, s62, 67584\n"
    "s_nop 0\n"
    "buffer_load_dwordx4 v239, s[48:51], s60 offen lds\n"
    "s_add_u32 m0, s62, 71680\n"
    "s_nop 0\n"
    "buffer_load_dwordx4 v239, s[52:55], s60 offen lds\n"
    "s_add_u32 s60, s60, 229376\n"
    : "+{v[236:239]}"(vinA), "+{s[56:63]}"(soff), "+{v[0:15]}"(xd), "+{v[16:19]}"(ep)
    : "{s[44:47]}"(srdA), "{s[48:51]}"(srdB), "{s[52:55]}"(srdC), "{s[20:23]}"(srdP), "{s74}"(pfo), "{v22}"(vpf)
    : "memory", "scc", "m0");

    } else {
      const int U = 16 + 5 * (cb >> 1), odd = cb & 1;
      const int u0 = U + 3 * odd, u1 = u0 + 1;
      srdA = mk_srd(wg, 1024u * 3584u * 2u); srdB = mk_srd(wu, 1024u * 3584u * 2u); srdC = srdB;
      soff[1] = u0 * 128; soff[2] = u1 * 128; soff[3] = 0; soff[4] = 229376;
      vinA[1] = lrow * 7168 + sch * 16;
      const u16* base = (sch < 4) ? wg : wu;
      unsigned long long ga = (unsigned long long)base + (size_t)lrow * 7168 + (size_t)((U + 2) * 128 + odd * 64 + (sch & 3) * 16);
      vinA[2] = (u32)ga; vinA[3] = (u32)(ga >> 32);
  asm volatile(
    "k1f_begin:\n"
    "s_waitcnt lgkmcnt(0)\n"
    "s_mov_b32 m0, 155648\n"
    "s_nop 0\n"
    "buffer_load_dword v22, s[20:23], s74 offen lds\n"
    "s_add_u32 m0, s62, 17408\n"
    "s_nop 0\n"
    "buffer_load_dwordx4 v237, s[44:47], s57 offen lds\n"
    "s_add_u32 m0, s62, 21504\n"
    "s_nop 0\n"
    "buffer_load_dwordx4 v237, s[48:51], s57 offen lds\n"
    "s_add_u32 s57, s57, 229376\n"
    "s_add_u32 m0, s62, 25600\n"
    "s_nop 0\n"
    "buffer_load_dwordx4 v237, s[44:47], s58 offen lds\n"
    "s_add_u32 m0, s62, 29696\n"
    "s_nop 0\n"
    "buffer_load_dwordx4 v237, s[48:51], s58 offen lds\n"
    "s_add_u32 s58, s58, 229376\n"
    "s_add_u32 m0, s62, 33792\n"
    "s_nop 0\n"
    "global_load_lds_dwordx4 v[238:239], off\n"
    "v_lshl_add_u64 v[238:239], v[238:239], 0, s[60:61]\n"
    "s_add_u32 m0, s62, 55296\n"
    "s_nop 0\n"
    "buffer_load_dwordx4 v237, s[44:47], s57 offen lds\n"
    "s_add_u32 m0, s62, 59392\n"
    "s_nop 0\n"
    "buffer_load_dwordx4 v237, s[48:51], s57 offen lds\n"
    "s_add_u32 s57, s57, 229376\n"
    "s_add_u32 m0, s62, 63488\n"
    "s_nop 0\n"
    "buffer_load_dwordx4 v237, s[44:47], s58 offen lds\n"
    "s_add_u32 m0, s62, 67584\n"
    "s_nop 0\n"
    "buffer_load_dwordx4 v237, s[48:51], s58 offen lds\n"
    "s_add_u32 s58, s58, 229376\n"
    "s_add_u32 m0, s62, 71680\n"
    "s_nop 0\n"
    "global_load_lds_dwordx4 v[238:239], off\n"
    "v_lshl_add_u64 v[238:239], v[238:239], 0, s[60:61]\n"
    : "+{v[236:239]}"(vinA), "+{s[56:63]}"(soff), "+{v[0:15]}"(xd), "+{v[16:19]}"(ep)
    : "{s[44:47]}"(srdA), "{s[48:51]}"(srdB), "{s[52:55]}"(srdC), "{s[20:23]}"(srdP), "{s74}"(pfo), "{v22}"(vpf)
    : "memory", "scc", "m0");

    }
  }
  u32x4 srdX = mk_srd(ws_ + WS_XNF, 4u << 20);
  asm volatile("" : "+{s[40:43]}"(srdX), "+{s[56:63]}"(soff));
  asm volatile("s_waitcnt vmcnt(11)" : "+{v[0:15]}"(xd), "+{v[16:19]}"(ep) :: "memory");
  ln_phase<MIXED>(xd, ep, srdX, ws, smem, bq, wq, (bq >> 3) & 7, tag TS0_ARG);
  if constexpr (MIXED) {

  } else {

  }
  ln_publish<MIXED>(ws, smem, bq, wq, (bq >> 3) & 7, tag TS0_ARG);
  if constexpr (MIXED) {

  } else {

  }
  ln_poll<MIXED>(ws, wq, (bq >> 3) & 7, tag TS0_ARG);
  TS0_END
  {
    const unsigned long long qmask = 0xffffull << (16 * wq);
    if constexpr (MIXED) {
  asm volatile(
    "s_waitcnt lgkmcnt(0)\n"
    "s_add_u32 m0, s63, 1024\n"
    "s_nop 0\n"
    "buffer_load_dwordx4 v236, s[40:43], s56 offen sc1 lds\n"
    "buffer_load_dwordx4 v236, s[40:43], s56 offen offset:1024 sc1 lds\n"
    "buffer_load_dwordx4 v236, s[40:43], s56 offen offset:2048 sc1 lds\n"
    "buffer_load_dwordx4 v236, s[40:43], s56 offen offset:3072 sc1 lds\n"
    "s_mov_b32 m0, 0\n"
    "s_nop 0\n"
    "s_mov_b64 exec, s[64:65]\n"
    "buffer_load_dwordx4 v237, s[40:43], s56 offen sc1 lds\n"
    "s_mov_b64 exec, -1\n"
    "s_add_u32 s56, s56, 131072\n"
    "s_add_u32 m0, s63, 38912\n"
    "s_nop 0\n"
    "buffer_load_dwordx4 v236, s[40:43], s56 offen sc1 lds\n"
    "buffer_load_dwordx4 v236, s[40:43], s56 offen offset:1024 sc1 lds\n"
    "buffer_load_dwordx4 v236, s[40:43], s56 offen offset:2048 sc1 lds\n"
    "buffer_load_dwordx4 v236, s[40:43], s56 offen offset:3072 sc1 lds\n"
    "s_mov_b32 m0, 37888\n"
    "s_nop 0\n"
    "s_mov_b64 exec, s[64:65]\n"
    "buffer_load_dwordx4 v237, s[40:43], s56 offen sc1 lds\n"
    "s_mov_b64 exec, -1\n"
    "s_add_u32 s56, s56, 131072\n"
    : "+{v[236:239]}"(vinA), "+{s[56:63]}"(soff)
    : "{s[40:43]}"(srdX), "{s[64:65]}"(qmask)
    : "memory", "scc", "m0", "s93");

    } else {
  asm volatile(
    "s_waitcnt lgkmcnt(0)\n"
    "s_add_u32 m0, s63, 1024\n"
    "s_nop 0\n"
    "buffer_load_dwordx4 v236, s[40:43], s56 offen sc1 lds\n"
    "buffer_load_dwordx4 v236, s[40:43], s56 offen offset:1024 sc1 lds\n"
    "buffer_load_dwordx4 v236, s[40:43], s56 offen offset:2048 sc1 lds\n"
    "buffer_load_dwordx4 v236, s[40:43], s56 offen offset:3072 sc1 lds\n"
    "s_add_u32 s56, s56, 131072\n"
    "s_add_u32 m0, s63, 38912\n"
    "s_nop 0\n"
    "buffer_load_dwordx4 v236, s[40:43], s56 offen sc1 lds\n"
    "buffer_load_dwordx4 v236, s[40:43], s56 offen offset:1024 sc1 lds\n"
    "buffer_load_dwordx4 v236, s[40:43], s56 offen offset:2048 sc1 lds\n"
    "buffer_load_dwordx4 v236, s[40:43], s56 offen offset:3072 sc1 lds\n"
    "s_add_u32 s56, s56, 131072\n"
    : "+{v[236:239]}"(vinA), "+{s[56:63]}"(soff)
    : "{s[40:43]}"(srdX), "{s[64:65]}"(qmask)
    : "memory", "scc", "m0", "s93");

    }
  }
  asm volatile("" : "+s"(bq), "+s"(wq), "+s"(pws), "+s"(pwo), "+s"(pwd));
  lane = fresh_lane();
  const int b = bq, xcd = b & 7, idx = b >> 3, rb = idx & 7, cj = idx >> 3;
  w = wq; ws = (char*)pws;
  const int r = w >> 1, c = w & 1;
  const int cb = 2 * xcd + (cj & 1);
  const int T0 = 16 * rb;
  const int g = lane >> 4, q = (lane >> 2) & 3, pp = lane & 3;
  const int rrow = 8 * g + q;
  const int f2r = ((rrow >> 1) & 1) | (((rrow >> 3) & 1) << 1);
  typedef u32 u32x12 __attribute__((ext_vector_type(12)));
  u32x12 vin;
  vin[0] = lds0 + lane * 16 + 8192 * r; vin[1] = vin[0] + 2 * SB;
  vin[2] = lds0 + lane * 16; vin[3] = vin[2] + 2 * SB;
  const int sw = MIXED ? r : 0;
  const u32 wbase = lds0 + WREG + rrow * 128 + pp * 8;
  vin[4] = wbase + (((2 * c + (0 ^ sw)) ^ f2r) * 32); vin[6] = vin[4] + 2 * SB;
  vin[5] = wbase + (((2 * c + (1 ^ sw)) ^ f2r) * 32); vin[7] = vin[5] + 2 * SB;
  vin[8] = wbase + 4 * 4096 + (((c + 0) ^ f2r) * 32); vin[10] = vin[8] + 2 * SB;
  vin[9] = wbase + 4 * 4096 + (((c + 2) ^ f2r) * 32); vin[11] = vin[9] + 2 * SB;
  u32x4 smisc;
  { unsigned long long qm = 0xffffull << (16 * w); smisc[0] = (u32)qm; smisc[1] = (u32)(qm >> 32); smisc[2] = 0; smisc[3] = w; }
  u32x8 sext;
  { const unsigned long long po = pwo + (u32)(b * 4 + w) * 2048u, pd = pwd + (u32)(b * 4 + w) * 7168u;
    sext[0] = (u32)po; sext[1] = (u32)(po >> 32); sext[2] = (u32)pd; sext[3] = (u32)(pd >> 32);
    sext[4] = 0; sext[5] = 0; sext[6] = spf[4] + SNPF * spf[5];
    sext[7] = (u32)((((xcd >> 1) * 4 + (idx & 3)) * 8 + (idx >> 2)) * 2 + (xcd & 1)) * 16384u + (u32)w * 4096u; }
  u32x4 srdV = mk_srd((const void*)((xcd & 1) == 0 ? pwo : pwd), (xcd & 1) == 0 ? 1024u * 1024u * 2u : 3584u * 1024u * 2u);
  u32x2 sw2; sw2[0] = ((xcd & 1) == 0 ? 0u : 1280u * 2048u) + (idx >> 2) * 256; sw2[1] = sw2[0] + 65536;
  u32x4 srdS = mk_srd(ws + WS_SLAB, (u32)SLAB_BYTES);
  u32x4 srdO = mk_srd(ws + WS_A2F, (u32)A2F_BYTES);
  u32x8 sepi;
  const u32 Tw = T0 + 8 * r;
  if constexpr (MIXED) {
    sepi[0] = ((2 * cb + c) * 128 + Tw) * 1024u; sepi[1] = sepi[0] + 4096; sepi[2] = ((32 + 2 * cb + c) * 128 + Tw) * 1024u; sepi[3] = sepi[2] + 4096;
    sepi[4] = spf[5]; sepi[5] = 0; sepi[6] = lds0 + 147456 + c * 3072; sepi[7] = (rb == 0) ? 0xff800000u : 0u;
    const int i = lane & 15;
    u32x4 cprev, cown;
#pragma unroll
    for (int e = 0; e < 4; ++e) {
      const int jp = 4 * g + e, jo = 16 + jp;
      cprev[e] = (((jp ^ i) & 1) == 0 && jp >= i + 2) ? 0u : 0xff800000u;
      cown[e] = (((jo ^ i) & 1) == 0 && jo <= i + 16) ? 0u : 0xff800000u;
    }
    u32x2 sel = {(i & 1) ? 0x01000c0cu : 0x0c0c0100u, (i & 1) ? 0x03020c0cu : 0x0c0c0302u};
  asm volatile(
    "s_waitcnt lgkmcnt(0)\n"
    "v_mbcnt_lo_u32_b32 v244, -1, 0\n"
    "v_mbcnt_hi_u32_b32 v244, -1, v244\n"
    "v_lshlrev_b32 v244, 4, v244\n"
    "v_mov_b32 v240, -1\n"
    "v_mov_b32 v241, -1\n"
    "v_mov_b32 v242, -1\n"
    "v_mov_b32 v243, -1\n"
    "v_lshlrev_b32 v245, 2, v244\n"
    "s_waitcnt vmcnt(5)\n"
    "s_barrier\n"
    "ds_read_b128 a[80:83], v224 offset:1024\n"
    "ds_read_b128 a[84:87], v224 offset:2048\n"
    "ds_read_b128 a[88:91], v224 offset:3072\n"
    "ds_read_b128 a[92:95], v224 offset:4096\n"
    "ds_read_b128 a[96:99], v224 offset:5120\n"
    "ds_read_b128 a[100:103], v224 offset:6144\n"
    "ds_read_b128 a[104:107], v224 offset:7168\n"
    "ds_read_b128 a[108:111], v224 offset:8192\n"
    "ds_read_b128 a[112:115], v226 offset:0\n"
    "ds_read_b64_tr_b16 a[0:1], v228 offset:0\n"
    "ds_read_b64_tr_b16 a[2:3], v228 offset:512\n"
    "ds_read_b64_tr_b16 a[4:5], v229 offset:0\n"
    "ds_read_b64_tr_b16 a[6:7], v229 offset:512\n"
    "ds_read_b64_tr_b16 a[8:9], v228 offset:4096\n"
    "ds_read_b64_tr_b16 a[10:11], v228 offset:4608\n"
    "ds_read_b64_tr_b16 a[12:13], v229 offset:4096\n"
    "ds_read_b64_tr_b16 a[14:15], v229 offset:4608\n"
    "ds_read_b64_tr_b16 a[16:17], v228 offset:8192\n"
    "ds_read_b64_tr_b16 a[18:19], v228 offset:8704\n"
    "ds_read_b64_tr_b16 a[20:21], v229 offset:8192\n"
    "ds_read_b64_tr_b16 a[22:23], v229 offset:8704\n"
    "ds_read_b64_tr_b16 a[24:25], v228 offset:12288\n"
    "ds_read_b64_tr_b16 a[26:27], v228 offset:12800\n"
    "ds_read_b64_tr_b16 a[28:29], v229 offset:12288\n"
    "ds_read_b64_tr_b16 a[30:31], v229 offset:12800\n"
    "ds_read_b64_tr_b16 a[32:33], v228 offset:16384\n"
    "ds_read_b64_tr_b16 a[34:35], v228 offset:16896\n"
    "ds_read_b64_tr_b16 a[36:37], v229 offset:16384\n"
    "ds_read_b64_tr_b16 a[38:39], v229 offset:16896\n"
    "s_waitcnt lgkmcnt(0)\n"
    "v_mfma_f32_16x16x32_bf16 v[200:203], a[0:3], a[80:83], 0\n"
    "s_add_u32 m0, s63, 76800\n"
    "v_mfma_f32_16x16x32_bf16 v[208:211], a[0:3], a[84:87], 0\n"
    "buffer_load_dwordx4 v236, s[40:43], s56 offen sc1 lds\n"
    "v_mfma_f32_16x16x32_bf16 v[216:219], a[0:3], a[88:91], 0\n"
    "v_mfma_f32_16x16x32_bf16 a[216:219], a[0:3], a[92:95], 0\n"
    "v_mfma_f32_16x16x32_bf16 a[224:227], a[0:3], a[96:99], 0\n"
    "buffer_load_dwordx4 v236, s[40:43], s56 offen offset:1024 sc1 lds\n"
    "v_mfma_f32_16x16x32_bf16 a[232:235], a[0:3], a[100:103], 0\n"
    "v_mfma_f32_16x16x32_bf16 a[240:243], a[0:3], a[104:107], 0\n"
    "v_mfma_f32_16x16x32_bf16 a[248:251], a[0:3], a[108:111], 0\n"
    "buffer_load_dwordx4 v236, s[40:43], s56 offen offset:2048 sc1 lds\n"
    "v_mfma_f32_16x16x32_bf16 v[204:207], a[4:7], a[80:83], 0\n"
    "v_mfma_f32_16x16x32_bf16 v[212:215], a[4:7], a[84:87], 0\n"
    "v_mfma_f32_16x16x32_bf16 v[220:223], a[4:7], a[88:91], 0\n"
    "buffer_load_dwordx4 v236, s[40:43], s56 offen offset:3072 sc1 lds\n"
    "v_mfma_f32_16x16x32_bf16 a[220:223], a[4:7], a[92:95], 0\n"
    "v_mfma_f32_16x16x32_bf16 a[228:231], a[4:7], a[96:99], 0\n"
    "s_mov_b32 m0, 75776\n"
    "v_mfma_f32_16x16x32_bf16 a[236:239], a[4:7], a[100:103], 0\n"
    "s_mov_b64 exec, s[64:65]\n"
    "buffer_load_dwordx4 v237, s[40:43], s56 offen sc1 lds\n"
    "s_mov_b64 exec, -1\n"
    "v_mfma_f32_16x16x32_bf16 a[244:247], a[4:7], a[104:107], 0\n"
    "s_add_u32 s56, s56, 131072\n"
    "v_mfma_f32_16x16x32_bf16 a[252:255], a[4:7], a[108:111], 0\n"
    "s_add_u32 m0, s62, 93184\n"
    "v_mfma_f32_16x16x32_bf16 v[0:3], a[8:11], a[80:83], 0\n"
    "buffer_load_dwordx4 v238, s[44:47], s57 offen lds\n"
    "v_mfma_f32_16x16x32_bf16 v[4:7], a[8:11], a[84:87], 0\n"
    "s_add_u32 s57, s57, 196608\n"
    "v_mfma_f32_16x16x32_bf16 v[8:11], a[8:11], a[88:91], 0\n"
    "s_add_u32 m0, s62, 97280\n"
    "v_mfma_f32_16x16x32_bf16 v[12:15], a[8:11], a[92:95], 0\n"
    "buffer_load_dwordx4 v238, s[44:47], s58 offen lds\n"
    "v_mfma_f32_16x16x32_bf16 v[16:19], a[8:11], a[96:99], 0\n"
    "s_add_u32 s58, s58, 196608\n"
    "v_mfma_f32_16x16x32_bf16 v[20:23], a[8:11], a[100:103], 0\n"
    "s_add_u32 m0, s62, 101376\n"
    "v_mfma_f32_16x16x32_bf16 v[24:27], a[8:11], a[104:107], 0\n"
    "buffer_load_dwordx4 v238, s[44:47], s59 offen lds\n"
    "v_mfma_f32_16x16x32_bf16 v[28:31], a[8:11], a[108:111], 0\n"
    "s_add_u32 s59, s59, 196608\n"
    "v_mfma_f32_16x16x32_bf16 v[132:135], a[8:11], a[112:115], 0\n"
    "s_waitcnt vmcnt(8)\n"
    "s_barrier\n"
    "s_add_u32 m0, s62, 105472\n"
    "v_mfma_f32_16x16x32_bf16 v[32:35], a[12:15], a[80:83], 0\n"
    "buffer_load_dwordx4 v239, s[48:51], s60 offen lds\n"
    "v_mfma_f32_16x16x32_bf16 v[36:39], a[12:15], a[84:87], 0\n"
    "ds_read_b128 a[116:119], v224 offset:38912\n"
    "v_mfma_f32_16x16x32_bf16 v[40:43], a[12:15], a[88:91], 0\n"
    "s_add_u32 m0, s62, 109568\n"
    "v_mfma_f32_16x16x32_bf16 v[44:47], a[12:15], a[92:95], 0\n"
    "buffer_load_dwordx4 v239, s[52:55], s60 offen lds\n"
    "v_mfma_f32_16x16x32_bf16 v[48:51], a[12:15], a[96:99], 0\n"
    "s_add_u32 s60, s60, 229376\n"
    "v_mfma_f32_16x16x32_bf16 v[52:55], a[12:15], a[100:103], 0\n"
    "s_add_u32 m0, s63, 114688\n"
    "v_mfma_f32_16x16x32_bf16 v[56:59], a[12:15], a[104:107], 0\n"
    "buffer_load_dwordx4 v236, s[40:43], s56 offen sc1 lds\n"
    "v_mfma_f32_16x16x32_bf16 v[60:63], a[12:15], a[108:111], 0\n"
    "ds_read_b128 a[120:123], v224 offset:39936\n"
    "v_mfma_f32_16x16x32_bf16 v[68:71], a[80:83], a[16:19], 0\n"
    "ds_read_b128 a[124:127], v224 offset:40960\n"
    "v_mfma_f32_16x16x32_bf16 v[72:75], a[84:87], a[16:19], 0\n"
    "buffer_load_dwordx4 v236, s[40:43], s56 offen offset:1024 sc1 lds\n"
    "v_mfma_f32_16x16x32_bf16 v[76:79], a[88:91], a[16:19], 0\n"
    "ds_read_b128 a[128:131], v224 offset:41984\n"
    "v_mfma_f32_16x16x32_bf16 v[80:83], a[92:95], a[16:19], 0\n"
    "ds_read_b128 a[132:135], v224 offset:43008\n"
    "v_mfma_f32_16x16x32_bf16 v[84:87], a[96:99], a[16:19], 0\n"
    "buffer_load_dwordx4 v236, s[40:43], s56 offen offset:2048 sc1 lds\n"
    "v_mfma_f32_16x16x32_bf16 v[88:91], a[100:103], a[16:19], 0\n"
    "ds_read_b128 a[136:139], v224 offset:44032\n"
    "v_mfma_f32_16x16x32_bf16 v[92:95], a[104:107], a[16:19], 0\n"
    "ds_read_b128 a[140:143], v224 offset:45056\n"
    "v_mfma_f32_16x16x32_bf16 v[96:99], a[108:111], a[16:19], 0\n"
    "buffer_load_dwordx4 v236, s[40:43], s56 offen offset:3072 sc1 lds\n"
    "v_mfma_f32_16x16x32_bf16 v[64:67], a[112:115], a[16:19], 0\n"
    "ds_read_b128 a[144:147], v224 offset:46080\n"
    "v_mfma_f32_16x16x32_bf16 v[100:103], a[80:83], a[20:23], 0\n"
    "s_mov_b32 m0, 113664\n"
    "v_mfma_f32_16x16x32_bf16 v[104:107], a[84:87], a[20:23], 0\n"
    "s_mov_b64 exec, s[64:65]\n"
    "buffer_load_dwordx4 v237, s[40:43], s56 offen sc1 lds\n"
    "s_mov_b64 exec, -1\n"
    "v_mfma_f32_16x16x32_bf16 v[108:111], a[88:91], a[20:23], 0\n"
    "s_add_u32 s56, s56, 131072\n"
    "v_mfma_f32_16x16x32_bf16 v[112:115], a[92:95], a[20:23], 0\n"
    "s_add_u32 m0, s62, 131072\n"
    "v_mfma_f32_16x16x32_bf16 v[116:119], a[96:99], a[20:23], 0\n"
    "buffer_load_dwordx4 v238, s[44:47], s57 offen lds\n"
    "v_mfma_f32_16x16x32_bf16 v[120:123], a[100:103], a[20:23], 0\n"
    "s_add_u32 s57, s57, 196608\n"
    "v_mfma_f32_16x16x32_bf16 v[124:127], a[104:107], a[20:23], 0\n"
    "s_add_u32 m0, s62, 135168\n"
    "v_mfma_f32_16x16x32_bf16 v[128:131], a[108:111], a[20:23], 0\n"
    "buffer_load_dwordx4 v238, s[44:47], s58 offen lds\n"
    "v_mfma_f32_16x16x32_bf16 a[152:155], a[24:27], a[80:83], 0\n"
    "s_add_u32 s58, s58, 196608\n"
    "v_mfma_f32_16x16x32_bf16 a[156:159], a[24:27], a[84:87], 0\n"
    "s_add_u32 m0, s62, 139264\n"
    "v_mfma_f32_16x16x32_bf16 a[160:163], a[24:27], a[88:91], 0\n"
    "buffer_load_dwordx4 v238, s[44:47], s59 offen lds\n"
    "v_mfma_f32_16x16x32_bf16 a[164:167], a[24:27], a[92:95], 0\n"
    "s_add_u32 s59, s59, 196608\n"
    "v_mfma_f32_16x16x32_bf16 a[168:171], a[24:27], a[96:99], 0\n"
    "s_add_u32 m0, s62, 143360\n"
    "v_mfma_f32_16x16x32_bf16 a[172:175], a[24:27], a[100:103], 0\n"
    "buffer_load_dwordx4 v239, s[48:51], s60 offen lds\n"
    "v_mfma_f32_16x16x32_bf16 a[176:179], a[24:27], a[104:107], 0\n"
    "ds_read_b128 a[148:151], v226 offset:37888\n"
    "v_mfma_f32_16x16x32_bf16 a[180:183], a[24:27], a[108:111], 0\n"
    "s_add_u32 m0, s62, 147456\n"
    "v_mfma_f32_16x16x32_bf16 a[184:187], a[28:31], a[80:83], 0\n"
    "buffer_load_dwordx4 v239, s[52:55], s60 offen lds\n"
    "v_mfma_f32_16x16x32_bf16 a[188:191], a[28:31], a[84:87], 0\n"
    "s_add_u32 s60, s60, 229376\n"
    "v_mfma_f32_16x16x32_bf16 a[192:195], a[28:31], a[88:91], 0\n"
    "ds_read_b64_tr_b16 a[40:41], v228 offset:37888\n"
    "v_mfma_f32_16x16x32_bf16 a[196:199], a[28:31], a[92:95], 0\n"
    "buffer_load_dword v232, v245, s[20:23], s74 offen\n"
    "v_mfma_f32_16x16x32_bf16 a[200:203], a[28:31], a[96:99], 0\n"
    "s_add_u32 s74, s74, s88\n"
    "v_mfma_f32_16x16x32_bf16 a[204:207], a[28:31], a[100:103], 0\n"
    "s_mov_b32 m0, 155648\n"
    "v_mfma_f32_16x16x32_bf16 a[208:211], a[28:31], a[104:107], 0\n"
    "global_load_lds_dwordx4 v244, s[70:71]\n"
    "v_mfma_f32_16x16x32_bf16 a[212:215], a[28:31], a[108:111], 0\n"
    "s_add_u32 s70, s70, 1024\n"
    "s_addc_u32 s71, s71, 0\n"
    "v_mfma_f32_16x16x32_bf16 v[136:139], a[32:35], a[80:83], 0\n"
    "ds_read_b64_tr_b16 a[42:43], v228 offset:38400\n"
    "v_mfma_f32_16x16x32_bf16 v[140:143], a[32:35], a[84:87], 0\n"
    "ds_read_b64_tr_b16 a[44:45], v229 offset:37888\n"
    "v_mfma_f32_16x16x32_bf16 v[144:147], a[32:35], a[88:91], 0\n"
    "ds_read_b64_tr_b16 a[46:47], v229 offset:38400\n"
    "v_mfma_f32_16x16x32_bf16 v[148:151], a[32:35], a[92:95], 0\n"
    "ds_read_b64_tr_b16 a[48:49], v228 offset:41984\n"
    "v_mfma_f32_16x16x32_bf16 v[152:155], a[32:35], a[96:99], 0\n"
    "ds_read_b64_tr_b16 a[50:51], v228 offset:42496\n"
    "v_mfma_f32_16x16x32_bf16 v[156:159], a[32:35], a[100:103], 0\n"
    "ds_read_b64_tr_b16 a[52:53], v229 offset:41984\n"
    "v_mfma_f32_16x16x32_bf16 v[160:163], a[32:35], a[104:107], 0\n"
    "ds_read_b64_tr_b16 a[54:55], v229 offset:42496\n"
    "v_mfma_f32_16x16x32_bf16 v[164:167], a[32:35], a[108:111], 0\n"
    "ds_read_b64_tr_b16 a[56:57], v228 offset:46080\n"
    "v_mfma_f32_16x16x32_bf16 v[168:171], a[36:39], a[80:83], 0\n"
    "ds_read_b64_tr_b16 a[58:59], v228 offset:46592\n"
    "v_mfma_f32_16x16x32_bf16 v[172:175], a[36:39], a[84:87], 0\n"
    "ds_read_b64_tr_b16 a[60:61], v229 offset:46080\n"
    "v_mfma_f32_16x16x32_bf16 v[176:179], a[36:39], a[88:91], 0\n"
    "ds_read_b64_tr_b16 a[62:63], v229 offset:46592\n"
    "v_mfma_f32_16x16x32_bf16 v[180:183], a[36:39], a[92:95], 0\n"
    "ds_read_b64_tr_b16 a[64:65], v228 offset:50176\n"
    "v_mfma_f32_16x16x32_bf16 v[184:187], a[36:39], a[96:99], 0\n"
    "ds_read_b64_tr_b16 a[66:67], v228 offset:50688\n"
    "v_mfma_f32_16x16x32_bf16 v[188:191], a[36:39], a[100:103], 0\n"
    "ds_read_b64_tr_b16 a[68:69], v229 offset:50176\n"
    "v_mfma_f32_16x16x32_bf16 v[192:195], a[36:39], a[104:107], 0\n"
    "ds_read_b64_tr_b16 a[70:71], v229 offset:50688\n"
    "v_mfma_f32_16x16x32_bf16 v[196:199], a[36:39], a[108:111], 0\n"
    "ds_read_b64_tr_b16 a[72:73], v228 offset:54272\n"
    "ds_read_b64_tr_b16 a[74:75], v228 offset:54784\n"
    "ds_read_b64_tr_b16 a[76:77], v229 offset:54272\n"
    "ds_read_b64_tr_b16 a[78:79], v229 offset:54784\n"
    "s_waitcnt vmcnt(12) lgkmcnt(0)\n"
    "s_barrier\n"
    "v_mfma_f32_16x16x32_bf16 v[200:203], a[40:43], a[116:119], v[200:203]\n"
    "ds_read_b128 a[80:83], v225 offset:1024\n"
    "v_mfma_f32_16x16x32_bf16 v[208:211], a[40:43], a[120:123], v[208:211]\n"
    "ds_read_b128 a[84:87], v225 offset:2048\n"
    "v_mfma_f32_16x16x32_bf16 v[216:219], a[40:43], a[124:127], v[216:219]\n"
    "s_add_u32 m0, s63, 1024\n"
    "v_mfma_f32_16x16x32_bf16 a[216:219], a[40:43], a[128:131], a[216:219]\n"
    "buffer_load_dwordx4 v236, s[40:43], s56 offen sc1 lds\n"
    "v_mfma_f32_16x16x32_bf16 a[224:227], a[40:43], a[132:135], a[224:227]\n"
    "ds_read_b128 a[88:91], v225 offset:3072\n"
    "v_mfma_f32_16x16x32_bf16 a[232:235], a[40:43], a[136:139], a[232:235]\n"
    "ds_read_b128 a[92:95], v225 offset:4096\n"
    "v_mfma_f32_16x16x32_bf16 a[240:243], a[40:43], a[140:143], a[240:243]\n"
    "ds_read_b128 a[96:99], v225 offset:5120\n"
    "v_mfma_f32_16x16x32_bf16 a[248:251], a[40:43], a[144:147], a[248:251]\n"
    "buffer_load_dwordx4 v236, s[40:43], s56 offen offset:1024 sc1 lds\n"
    "v_mfma_f32_16x16x32_bf16 v[204:207], a[44:47], a[116:119], v[204:207]\n"
    "ds_read_b128 a[100:103], v225 offset:6144\n"
    "v_mfma_f32_16x16x32_bf16 v[212:215], a[44:47], a[120:123], v[212:215]\n"
    "ds_read_b128 a[104:107], v225 offset:7168\n"
    "v_mfma_f32_16x16x32_bf16 v[220:223], a[44:47], a[124:127], v[220:223]\n"
    "ds_read_b128 a[108:111], v225 offset:8192\n"
    "v_mfma_f32_16x16x32_bf16 a[220:223], a[44:47], a[128:131], a[220:223]\n"
    "buffer_load_dwordx4 v236, s[40:43], s56 offen offset:2048 sc1 lds\n"
    "v_mfma_f32_16x16x32_bf16 a[228:231], a[44:47], a[132:135], a[228:231]\n"
    "ds_read_b128 a[112:115], v227 offset:0\n"
    "v_mfma_f32_16x16x32_bf16 a[236:239], a[44:47], a[136:139], a[236:239]\n"
    "ds_read_b64_tr_b16 a[0:1], v230 offset:0\n"
    "v_mfma_f32_16x16x32_bf16 a[244:247], a[44:47], a[140:143], a[244:247]\n"
    "ds_read_b64_tr_b16 a[2:3], v230 offset:512\n"
    "v_mfma_f32_16x16x32_bf16 a[252:255], a[44:47], a[144:147], a[252:255]\n"
    "buffer_load_dwordx4 v236, s[40:43], s56 offen offset:3072 sc1 lds\n"
    "v_mfma_f32_16x16x32_bf16 v[0:3], a[48:51], a[116:119], v[0:3]\n"
    "ds_read_b64_tr_b16 a[4:5], v231 offset:0\n"
    "v_mfma_f32_16x16x32_bf16 v[4:7], a[48:51], a[120:123], v[4:7]\n"
    "ds_read_b64_tr_b16 a[6:7], v231 offset:512\n"
    "v_mfma_f32_16x16x32_bf16 v[8:11], a[48:51], a[124:127], v[8:11]\n"
    "s_mov_b32 m0, 0\n"
    "v_mfma_f32_16x16x32_bf16 v[12:15], a[48:51], a[128:131], v[12:15]\n"
    "s_mov_b64 exec, s[64:65]\n"
    "buffer_load_dwordx4 v237, s[40:43], s56 offen sc1 lds\n"
    "s_mov_b64 exec, -1\n"
    "v_mfma_f32_16x16x32_bf16 v[16:19], a[48:51], a[132:135], v[16:19]\n"
    "s_add_u32 s56, s56, 131072\n"
    "v_mfma_f32_16x16x32_bf16 v[20:23], a[48:51], a[136:139], v[20:23]\n"
    "ds_read_b64_tr_b16 a[8:9], v230 offset:4096\n"
    "v_mfma_f32_16x16x32_bf16 v[24:27], a[48:51], a[140:143], v[24:27]\n"
    "s_add_u32 m0, s62, 17408\n"
    "v_mfma_f32_16x16x32_bf16 v[28:31], a[48:51], a[144:147], v[28:31]\n"
    "buffer_load_dwordx4 v238, s[44:47], s57 offen lds\n"
    "v_mfma_f32_16x16x32_bf16 v[132:135], a[48:51], a[148:151], v[132:135]\n"
    "s_add_u32 s57, s57, 196608\n"
    "v_mfma_f32_16x16x32_bf16 v[32:35], a[52:55], a[116:119], v[32:35]\n"
    "ds_read_b64_tr_b16 a[10:11], v230 offset:4608\n"
    "v_mfma_f32_16x16x32_bf16 v[36:39], a[52:55], a[120:123], v[36:39]\n"
    "s_add_u32 m0, s62, 21504\n"
    "v_mfma_f32_16x16x32_bf16 v[40:43], a[52:55], a[124:127], v[40:43]\n"
    "buffer_load_dwordx4 v238, s[44:47], s58 offen lds\n"
    "v_mfma_f32_16x16x32_bf16 v[44:47], a[52:55], a[128:131], v[44:47]\n"
    "s_add_u32 s58, s58, 196608\n"
    "v_mfma_f32_16x16x32_bf16 v[48:51], a[52:55], a[132:135], v[48:51]\n"
    "ds_read_b64_tr_b16 a[12:13], v231 offset:4096\n"
    "v_mfma_f32_16x16x32_bf16 v[52:55], a[52:55], a[136:139], v[52:55]\n"
    "s_add_u32 m0, s62, 25600\n"
    "v_mfma_f32_16x16x32_bf16 v[56:59], a[52:55], a[140:143], v[56:59]\n"
    "buffer_load_dwordx4 v238, s[44:47], s59 offen lds\n"
    "v_mfma_f32_16x16x32_bf16 v[60:63], a[52:55], a[144:147], v[60:63]\n"
    "s_add_u32 s59, s59, 196608\n"
    "v_mfma_f32_16x16x32_bf16 v[68:71], a[116:119], a[56:59], v[68:71]\n"
    "ds_read_b64_tr_b16 a[14:15], v231 offset:4608\n"
    "v_mfma_f32_16x16x32_bf16 v[72:75], a[120:123], a[56:59], v[72:75]\n"
    "s_add_u32 m0, s62, 29696\n"
    "v_mfma_f32_16x16x32_bf16 v[76:79], a[124:127], a[56:59], v[76:79]\n"
    "buffer_load_dwordx4 v239, s[48:51], s60 offen lds\n"
    "v_mfma_f32_16x16x32_bf16 v[80:83], a[128:131], a[56:59], v[80:83]\n"
    "ds_read_b64_tr_b16 a[16:17], v230 offset:8192\n"
    "v_mfma_f32_16x16x32_bf16 v[84:87], a[132:135], a[56:59], v[84:87]\n"
    "ds_read_b64_tr_b16 a[18:19], v230 offset:8704\n"
    "v_mfma_f32_16x16x32_bf16 v[88:91], a[136:139], a[56:59], v[88:91]\n"
    "s_add_u32 m0, s62, 33792\n"
    "v_mfma_f32_16x16x32_bf16 v[92:95], a[140:143], a[56:59], v[92:95]\n"
    "buffer_load_dwordx4 v239, s[52:55], s60 offen lds\n"
    "v_mfma_f32_16x16x32_bf16 v[96:99], a[144:147], a[56:59], v[96:99]\n"
    "s_add_u32 s60, s60, 229376\n"
    "v_mfma_f32_16x16x32_bf16 v[64:67], a[148:151], a[56:59], v[64:67]\n"
    "ds_read_b64_tr_b16 a[20:21], v231 offset:8192\n"
    "v_mfma_f32_16x16x32_bf16 v[100:103], a[116:119], a[60:63], v[100:103]\n"
    "s_add_u32 m0, s63, 38912\n"
    "v_mfma_f32_16x16x32_bf16 v[104:107], a[120:123], a[60:63], v[104:107]\n"
    "buffer_load_dwordx4 v236, s[40:43], s56 offen sc1 lds\n"
    "v_mfma_f32_16x16x32_bf16 v[108:111], a[124:127], a[60:63], v[108:111]\n"
    "ds_read_b64_tr_b16 a[22:23], v231 offset:8704\n"
    "v_mfma_f32_16x16x32_bf16 v[112:115], a[128:131], a[60:63], v[112:115]\n"
    "ds_read_b64_tr_b16 a[24:25], v230 offset:12288\n"
    "v_mfma_f32_16x16x32_bf16 v[116:119], a[132:135], a[60:63], v[116:119]\n"
    "ds_read_b64_tr_b16 a[26:27], v230 offset:12800\n"
    "v_mfma_f32_16x16x32_bf16 v[120:123], a[136:139], a[60:63], v[120:123]\n"
    "buffer_load_dwordx4 v236, s[40:43], s56 offen offset:1024 sc1 lds\n"
    "v_mfma_f32_16x16x32_bf16 v[124:127], a[140:143], a[60:63], v[124:127]\n"
    "ds_read_b64_tr_b16 a[28:29], v231 offset:12288\n"
    "v_mfma_f32_16x16x32_bf16 v[128:131], a[144:147], a[60:63], v[128:131]\n"
    "ds_read_b64_tr_b16 a[30:31], v231 offset:12800\n"
    "v_mfma_f32_16x16x32_bf16 a[152:155], a[64:67], a[116:119], a[152:155]\n"
    "ds_read_b64_tr_b16 a[32:33], v230 offset:16384\n"
    "v_mfma_f32_16x16x32_bf16 a[156:159], a[64:67], a[120:123], a[156:159]\n"
    "buffer_load_dwordx4 v236, s[40:43], s56 offen offset:2048 sc1 lds\n"
    "v_mfma_f32_16x16x32_bf16 a[160:163], a[64:67], a[124:127], a[160:163]\n"
    "ds_read_b64_tr_b16 a[34:35], v230 offset:16896\n"
    "v_mfma_f32_16x16x32_bf16 a[164:167], a[64:67], a[128:131], a[164:167]\n"
    "ds_read_b64_tr_b16 a[36:37], v231 offset:16384\n"
    "v_mfma_f32_16x16x32_bf16 a[168:171], a[64:67], a[132:135], a[168:171]\n"
    "ds_read_b64_tr_b16 a[38:39], v231 offset:16896\n"
    "v_mfma_f32_16x16x32_bf16 a[172:175], a[64:67], a[136:139], a[172:175]\n"
    "buffer_load_dwordx4 v236, s[40:43], s56 offen offset:3072 sc1 lds\n"
    "v_mfma_f32_16x16x32_bf16 a[176:179], a[64:67], a[140:143], a[176:179]\n"
    "v_mfma_f32_16x16x32_bf16 a[180:183], a[64:67], a[144:147], a[180:183]\n"
    "v_mfma_f32_16x16x32_bf16 a[184:187], a[68:71], a[116:119], a[184:187]\n"
    "s_mov_b32 m0, 37888\n"
    "v_mfma_f32_16x16x32_bf16 a[188:191], a[68:71], a[120:123], a[188:191]\n"
    "s_mov_b64 exec, s[64:65]\n"
    "buffer_load_dwordx4 v237, s[40:43], s56 offen sc1 lds\n"
    "s_mov_b64 exec, -1\n"
    "v_mfma_f32_16x16x32_bf16 a[192:195], a[68:71], a[124:127], a[192:195]\n"
    "s_add_u32 s56, s56, 131072\n"
    "v_mfma_f32_16x16x32_bf16 a[196:199], a[68:71], a[128:131], a[196:199]\n"
    "v_mfma_f32_16x16x32_bf16 a[200:203], a[68:71], a[132:135], a[200:203]\n"
    "s_add_u32 m0, s62, 55296\n"
    "v_mfma_f32_16x16x32_bf16 a[204:207], a[68:71], a[136:139], a[204:207]\n"
    "buffer_load_dwordx4 v238, s[44:47], s57 offen lds\n"
    "v_mfma_f32_16x16x32_bf16 a[208:211], a[68:71], a[140:143], a[208:211]\n"
    "s_add_u32 s57, s57, 196608\n"
    "v_mfma_f32_16x16x32_bf16 a[212:215], a[68:71], a[144:147], a[212:215]\n"
    "v_mfma_f32_16x16x32_bf16 v[136:139], a[72:75], a[116:119], v[136:139]\n"
    "s_add_u32 m0, s62, 59392\n"
    "v_mfma_f32_16x16x32_bf16 v[140:143], a[72:75], a[120:123], v[140:143]\n"
    "buffer_load_dwordx4 v238, s[44:47], s58 offen lds\n"
    "v_mfma_f32_16x16x32_bf16 v[144:147], a[72:75], a[124:127], v[144:147]\n"
    "s_add_u32 s58, s58, 196608\n"
    "v_mfma_f32_16x16x32_bf16 v[148:151], a[72:75], a[128:131], v[148:151]\n"
    "v_mfma_f32_16x16x32_bf16 v[152:155], a[72:75], a[132:135], v[152:155]\n"
    "s_add_u32 m0, s62, 63488\n"
    "v_mfma_f32_16x16x32_bf16 v[156:159], a[72:75], a[136:139], v[156:159]\n"
    "buffer_load_dwordx4 v238, s[44:47], s59 offen lds\n"
    "v_mfma_f32_16x16x32_bf16 v[160:163], a[72:75], a[140:143], v[160:163]\n"
    "s_add_u32 s59, s59, 196608\n"
    "v_mfma_f32_16x16x32_bf16 v[164:167], a[72:75], a[144:147], v[164:167]\n"
    "v_mfma_f32_16x16x32_bf16 v[168:171], a[76:79], a[116:119], v[168:171]\n"
    "s_add_u32 m0, s62, 67584\n"
    "v_mfma_f32_16x16x32_bf16 v[172:175], a[76:79], a[120:123], v[172:175]\n"
    "buffer_load_dwordx4 v239, s[48:51], s60 offen lds\n"
    "v_mfma_f32_16x16x32_bf16 v[176:179], a[76:79], a[124:127], v[176:179]\n"
    "v_mfma_f32_16x16x32_bf16 v[180:183], a[76:79], a[128:131], v[180:183]\n"
    "v_mfma_f32_16x16x32_bf16 v[184:187], a[76:79], a[132:135], v[184:187]\n"
    "s_add_u32 m0, s62, 71680\n"
    "v_mfma_f32_16x16x32_bf16 v[188:191], a[76:79], a[136:139], v[188:191]\n"
    "buffer_load_dwordx4 v239, s[52:55], s60 offen lds\n"
    "v_mfma_f32_16x16x32_bf16 v[192:195], a[76:79], a[140:143], v[192:195]\n"
    "s_add_u32 s60, s60, 229376\n"
    "v_mfma_f32_16x16x32_bf16 v[196:199], a[76:79], a[144:147], v[196:199]\n"
    "s_waitcnt vmcnt(22) lgkmcnt(0)\n"
    "s_barrier\n"
    "v_mfma_f32_16x16x32_bf16 v[200:203], a[0:3], a[80:83], v[200:203]\n"
    "ds_read_b128 a[116:119], v225 offset:38912\n"
    "v_mfma_f32_16x16x32_bf16 v[208:211], a[0:3], a[84:87], v[208:211]\n"
    "ds_read_b128 a[120:123], v225 offset:39936\n"
    "v_mfma_f32_16x16x32_bf16 v[216:219], a[0:3], a[88:91], v[216:219]\n"
    "s_add_u32 m0, s63, 76800\n"
    "v_mfma_f32_16x16x32_bf16 a[216:219], a[0:3], a[92:95], a[216:219]\n"
    "buffer_load_dwordx4 v236, s[40:43], s56 offen sc1 lds\n"
    "v_mfma_f32_16x16x32_bf16 a[224:227], a[0:3], a[96:99], a[224:227]\n"
    "ds_read_b128 a[124:127], v225 offset:40960\n"
    "v_mfma_f32_16x16x32_bf16 a[232:235], a[0:3], a[100:103], a[232:235]\n"
    "ds_read_b128 a[128:131], v225 offset:41984\n"
    "v_mfma_f32_16x16x32_bf16 a[240:243], a[0:3], a[104:107], a[240:243]\n"
    "ds_read_b128 a[132:135], v225 offset:43008\n"
    "v_mfma_f32_16x16x32_bf16 a[248:251], a[0:3], a[108:111], a[248:251]\n"
    "ds_read_b128 a[136:139], v225 offset:44032\n"
    "v_mfma_f32_16x16x32_bf16 v[204:207], a[4:7], a[80:83], v[204:207]\n"
    "buffer_load_dwordx4 v236, s[40:43], s56 offen offset:1024 sc1 lds\n"
    "v_mfma_f32_16x16x32_bf16 v[212:215], a[4:7], a[84:87], v[212:215]\n"
    "ds_read_b128 a[140:143], v225 offset:45056\n"
    "v_mfma_f32_16x16x32_bf16 v[220:223], a[4:7], a[88:91], v[220:223]\n"
    "ds_read_b128 a[144:147], v225 offset:46080\n"
    "v_mfma_f32_16x16x32_bf16 a[220:223], a[4:7], a[92:95], a[220:223]\n"
    "ds_read_b128 a[148:151], v227 offset:37888\n"
    "v_mfma_f32_16x16x32_bf16 a[228:231], a[4:7], a[96:99], a[228:231]\n"
    "ds_read_b64_tr_b16 a[40:41], v230 offset:37888\n"
    "v_mfma_f32_16x16x32_bf16 a[236:239], a[4:7], a[100:103], a[236:239]\n"
    "buffer_load_dwordx4 v236, s[40:43], s56 offen offset:2048 sc1 lds\n"
    "v_mfma_f32_16x16x32_bf16 a[244:247], a[4:7], a[104:107], a[244:247]\n"
    "ds_read_b64_tr_b16 a[42:43], v230 offset:38400\n"
    "v_mfma_f32_16x16x32_bf16 a[252:255], a[4:7], a[108:111], a[252:255]\n"
    "ds_read_b64_tr_b16 a[44:45], v231 offset:37888\n"
    "v_mfma_f32_16x16x32_bf16 v[0:3], a[8:11], a[80:83], v[0:3]\n"
    "ds_read_b64_tr_b16 a[46:47], v231 offset:38400\n"
    "v_mfma_f32_16x16x32_bf16 v[4:7], a[8:11], a[84:87], v[4:7]\n"
    "ds_read_b64_tr_b16 a[48:49], v230 offset:41984\n"
    "v_mfma_f32_16x16x32_bf16 v[8:11], a[8:11], a[88:91], v[8:11]\n"
    "buffer_load_dwordx4 v236, s[40:43], s56 offen offset:3072 sc1 lds\n"
    "v_mfma_f32_16x16x32_bf16 v[12:15], a[8:11], a[92:95], v[12:15]\n"
    "ds_read_b64_tr_b16 a[50:51], v230 offset:42496\n"
    "v_mfma_f32_16x16x32_bf16 v[16:19], a[8:11], a[96:99], v[16:19]\n"
    "ds_read_b64_tr_b16 a[52:53], v231 offset:41984\n"
    "v_mfma_f32_16x16x32_bf16 v[20:23], a[8:11], a[100:103], v[20:23]\n"
    "ds_read_b64_tr_b16 a[54:55], v231 offset:42496\n"
    "v_mfma_f32_16x16x32_bf16 v[24:27], a[8:11], a[104:107], v[24:27]\n"
    "s_mov_b32 m0, 75776\n"
    "v_mfma_f32_16x16x32_bf16 v[28:31], a[8:11], a[108:111], v[28:31]\n"
    "s_mov_b64 exec, s[64:65]\n"
    "buffer_load_dwordx4 v237, s[40:43], s56 offen sc1 lds\n"
    "s_mov_b64 exec, -1\n"
    "v_mfma_f32_16x16x32_bf16 v[132:135], a[8:11], a[112:115], v[132:135]\n"
    "s_add_u32 s56, s56, 131072\n"
    "v_mfma_f32_16x16x32_bf16 v[32:35], a[12:15], a[80:83], v[32:35]\n"
    "ds_read_b64_tr_b16 a[56:57], v230 offset:46080\n"
    "v_mfma_f32_16x16x32_bf16 v[36:39], a[12:15], a[84:87], v[36:39]\n"
    "ds_read_b64_tr_b16 a[58:59], v230 offset:46592\n"
    "v_mfma_f32_16x16x32_bf16 v[40:43], a[12:15], a[88:91], v[40:43]\n"
    "s_add_u32 m0, s62, 93184\n"
    "v_mfma_f32_16x16x32_bf16 v[44:47], a[12:15], a[92:95], v[44:47]\n"
    "buffer_load_dwordx4 v238, s[44:47], s57 offen lds\n"
    "v_mfma_f32_16x16x32_bf16 v[48:51], a[12:15], a[96:99], v[48:51]\n"
    "s_add_u32 s57, s57, 196608\n"
    "v_mfma_f32_16x16x32_bf16 v[52:55], a[12:15], a[100:103], v[52:55]\n"
    "ds_read_b64_tr_b16 a[60:61], v231 offset:46080\n"
    "v_mfma_f32_16x16x32_bf16 v[56:59], a[12:15], a[104:107], v[56:59]\n"
    "ds_read_b64_tr_b16 a[62:63], v231 offset:46592\n"
    "v_mfma_f32_16x16x32_bf16 v[60:63], a[12:15], a[108:111], v[60:63]\n"
    "s_add_u32 m0, s62, 97280\n"
    "v_mfma_f32_16x16x32_bf16 v[68:71], a[80:83], a[16:19], v[68:71]\n"
    "buffer_load_dwordx4 v238, s[44:47], s58 offen lds\n"
    "v_mfma_f32_16x16x32_bf16 v[72:75], a[84:87], a[16:19], v[72:75]\n"
    "s_add_u32 s58, s58, 196608\n"
    "v_mfma_f32_16x16x32_bf16 v[76:79], a[88:91], a[16:19], v[76:79]\n"
    "ds_read_b64_tr_b16 a[64:65], v230 offset:50176\n"
    "v_mfma_f32_16x16x32_bf16 v[80:83], a[92:95], a[16:19], v[80:83]\n"
    "ds_read_b64_tr_b16 a[66:67], v230 offset:50688\n"
    "v_mfma_f32_16x16x32_bf16 v[84:87], a[96:99], a[16:19], v[84:87]\n"
    "s_add_u32 m0, s62, 101376\n"
    "v_mfma_f32_16x16x32_bf16 v[88:91], a[100:103], a[16:19], v[88:91]\n"
    "buffer_load_dwordx4 v238, s[44:47], s59 offen lds\n"
    "v_mfma_f32_16x16x32_bf16 v[92:95], a[104:107], a[16:19], v[92:95]\n"
    "s_add_u32 s59, s59, 196608\n"
    "v_mfma_f32_16x16x32_bf16 v[96:99], a[108:111], a[16:19], v[96:99]\n"
    "ds_read_b64_tr_b16 a[68:69], v231 offset:50176\n"
    "v_mfma_f32_16x16x32_bf16 v[64:67], a[112:115], a[16:19], v[64:67]\n"
    "ds_read_b64_tr_b16 a[70:71], v231 offset:50688\n"
    "v_mfma_f32_16x16x32_bf16 v[100:103], a[80:83], a[20:23], v[100:103]\n"
    "s_add_u32 m0, s62, 105472\n"
    "v_mfma_f32_16x16x32_bf16 v[104:107], a[84:87], a[20:23], v[104:107]\n"
    "buffer_load_dwordx4 v239, s[48:51], s60 offen lds\n"
    "v_mfma_f32_16x16x32_bf16 v[108:111], a[88:91], a[20:23], v[108:111]\n"
    "ds_read_b64_tr_b16 a[72:73], v230 offset:54272\n"
    "v_mfma_f32_16x16x32_bf16 v[112:115], a[92:95], a[20:23], v[112:115]\n"
    "ds_read_b64_tr_b16 a[74:75], v230 offset:54784\n"
    "v_mfma_f32_16x16x32_bf16 v[116:119], a[96:99], a[20:23], v[116:119]\n"
    "ds_read_b64_tr_b16 a[76:77], v231 offset:54272\n"
    "v_mfma_f32_16x16x32_bf16 v[120:123], a[100:103], a[20:23], v[120:123]\n"
    "s_add_u32 m0, s62, 109568\n"
    "v_mfma_f32_16x16x32_bf16 v[124:127], a[104:107], a[20:23], v[124:127]\n"
    "buffer_load_dwordx4 v239, s[52:55], s60 offen lds\n"
    "v_mfma_f32_16x16x32_bf16 v[128:131], a[108:111], a[20:23], v[128:131]\n"
    "s_add_u32 s60, s60, 229376\n"
    "v_mfma_f32_16x16x32_bf16 a[152:155], a[24:27], a[80:83], a[152:155]\n"
    "ds_read_b64_tr_b16 a[78:79], v231 offset:54784\n"
    "v_mfma_f32_16x16x32_bf16 a[156:159], a[24:27], a[84:87], a[156:159]\n"
    "v_mfma_f32_16x16x32_bf16 a[160:163], a[24:27], a[88:91], a[160:163]\n"
    "v_mfma_f32_16x16x32_bf16 a[164:167], a[24:27], a[92:95], a[164:167]\n"
    "buffer_store_dwordx4 v[240:243], v244, s[76:79], s75 offen offset:0 sc1\n"
    "v_mfma_f32_16x16x32_bf16 a[168:171], a[24:27], a[96:99], a[168:171]\n"
    "v_mfma_f32_16x16x32_bf16 a[172:175], a[24:27], a[100:103], a[172:175]\n"
    "v_mfma_f32_16x16x32_bf16 a[176:179], a[24:27], a[104:107], a[176:179]\n"
    "v_mfma_f32_16x16x32_bf16 a[180:183], a[24:27], a[108:111], a[180:183]\n"
    "v_mfma_f32_16x16x32_bf16 a[184:187], a[28:31], a[80:83], a[184:187]\n"
    "buffer_store_dwordx4 v[240:243], v244, s[76:79], s75 offen offset:1024 sc1\n"
    "v_mfma_f32_16x16x32_bf16 a[188:191], a[28:31], a[84:87], a[188:191]\n"
    "v_mfma_f32_16x16x32_bf16 a[192:195], a[28:31], a[88:91], a[192:195]\n"
    "v_mfma_f32_16x16x32_bf16 a[196:199], a[28:31], a[92:95], a[196:199]\n"
    "v_mfma_f32_16x16x32_bf16 a[200:203], a[28:31], a[96:99], a[200:203]\n"
    "v_mfma_f32_16x16x32_bf16 a[204:207], a[28:31], a[100:103], a[204:207]\n"
    "buffer_store_dwordx4 v[240:243], v244, s[76:79], s75 offen offset:2048 sc1\n"
    "v_mfma_f32_16x16x32_bf16 a[208:211], a[28:31], a[104:107], a[208:211]\n"
    "v_mfma_f32_16x16x32_bf16 a[212:215], a[28:31], a[108:111], a[212:215]\n"
    "v_mfma_f32_16x16x32_bf16 v[136:139], a[32:35], a[80:83], v[136:139]\n"
    "v_mfma_f32_16x16x32_bf16 v[140:143], a[32:35], a[84:87], v[140:143]\n"
    "v_mfma_f32_16x16x32_bf16 v[144:147], a[32:35], a[88:91], v[144:147]\n"
    "buffer_store_dwordx4 v[240:243], v244, s[76:79], s75 offen offset:3072 sc1\n"
    "v_mfma_f32_16x16x32_bf16 v[148:151], a[32:35], a[92:95], v[148:151]\n"
    "s_add_u32 s75, s75, 4096\n"
    "v_mfma_f32_16x16x32_bf16 v[152:155], a[32:35], a[96:99], v[152:155]\n"
    "v_mfma_f32_16x16x32_bf16 v[156:159], a[32:35], a[100:103], v[156:159]\n"
    "v_mfma_f32_16x16x32_bf16 v[160:163], a[32:35], a[104:107], v[160:163]\n"
    "v_mfma_f32_16x16x32_bf16 v[164:167], a[32:35], a[108:111], v[164:167]\n"
    "v_mfma_f32_16x16x32_bf16 v[168:171], a[36:39], a[80:83], v[168:171]\n"
    "v_mfma_f32_16x16x32_bf16 v[172:175], a[36:39], a[84:87], v[172:175]\n"
    "v_mfma_f32_16x16x32_bf16 v[176:179], a[36:39], a[88:91], v[176:179]\n"
    "v_mfma_f32_16x16x32_bf16 v[180:183], a[36:39], a[92:95], v[180:183]\n"
    "v_mfma_f32_16x16x32_bf16 v[184:187], a[36:39], a[96:99], v[184:187]\n"
    "v_mfma_f32_16x16x32_bf16 v[188:191], a[36:39], a[100:103], v[188:191]\n"
    "v_mfma_f32_16x16x32_bf16 v[192:195], a[36:39], a[104:107], v[192:195]\n"
    "v_mfma_f32_16x16x32_bf16 v[196:199], a[36:39], a[108:111], v[196:199]\n"
    "s_waitcnt vmcnt(24) lgkmcnt(0)\n"
    "s_barrier\n"
    "v_mfma_f32_16x16x32_bf16 v[200:203], a[40:43], a[116:119], v[200:203]\n"
    "ds_read_b128 a[80:83], v224 offset:1024\n"
    "v_mfma_f32_16x16x32_bf16 v[208:211], a[40:43], a[120:123], v[208:211]\n"
    "ds_read_b128 a[84:87], v224 offset:2048\n"
    "v_mfma_f32_16x16x32_bf16 v[216:219], a[40:43], a[124:127], v[216:219]\n"
    "s_add_u32 m0, s63, 114688\n"
    "v_mfma_f32_16x16x32_bf16 a[216:219], a[40:43], a[128:131], a[216:219]\n"
    "buffer_load_dwordx4 v236, s[40:43], s56 offen sc1 lds\n"
    "v_mfma_f32_16x16x32_bf16 a[224:227], a[40:43], a[132:135], a[224:227]\n"
    "ds_read_b128 a[88:91], v224 offset:3072\n"
    "v_mfma_f32_16x16x32_bf16 a[232:235], a[40:43], a[136:139], a[232:235]\n"
    "ds_read_b128 a[92:95], v224 offset:4096\n"
    "v_mfma_f32_16x16x32_bf16 a[240:243], a[40:43], a[140:143], a[240:243]\n"
    "ds_read_b128 a[96:99], v224 offset:5120\n"
    "v_mfma_f32_16x16x32_bf16 a[248:251], a[40:43], a[144:147], a[248:251]\n"
    "ds_read_b128 a[100:103], v224 offset:6144\n"
    "v_mfma_f32_16x16x32_bf16 v[204:207], a[44:47], a[116:119], v[204:207]\n"
    "ds_read_b128 a[104:107], v224 offset:7168\n"
    "v_mfma_f32_16x16x32_bf16 v[212:215], a[44:47], a[120:123], v[212:215]\n"
    "ds_read_b128 a[108:111], v224 offset:8192\n"
    "v_mfma_f32_16x16x32_bf16 v[220:223], a[44:47], a[124:127], v[220:223]\n"
    "buffer_load_dwordx4 v236, s[40:43], s56 offen offset:1024 sc1 lds\n"
    "v_mfma_f32_16x16x32_bf16 a[220:223], a[44:47], a[128:131], a[220:223]\n"
    "ds_read_b128 a[112:115], v226 offset:0\n"
    "v_mfma_f32_16x16x32_bf16 a[228:231], a[44:47], a[132:135], a[228:231]\n"
    "ds_read_b64_tr_b16 a[0:1], v228 offset:0\n"
    "v_mfma_f32_16x16x32_bf16 a[236:239], a[44:47], a[136:139], a[236:239]\n"
    "ds_read_b64_tr_b16 a[2:3], v228 offset:512\n"
    "v_mfma_f32_16x16x32_bf16 a[244:247], a[44:47], a[140:143], a[244:247]\n"
    "ds_read_b64_tr_b16 a[4:5], v229 offset:0\n"
    "v_mfma_f32_16x16x32_bf16 a[252:255], a[44:47], a[144:147], a[252:255]\n"
    "ds_read_b64_tr_b16 a[6:7], v229 offset:512\n"
    "v_mfma_f32_16x16x32_bf16 v[0:3], a[48:51], a[116:119], v[0:3]\n"
    "ds_read_b64_tr_b16 a[8:9], v228 offset:4096\n"
    "v_mfma_f32_16x16x32_bf16 v[4:7], a[48:51], a[120:123], v[4:7]\n"
    "buffer_load_dwordx4 v236, s[40:43], s56 offen offset:2048 sc1 lds\n"
    "v_mfma_f32_16x16x32_bf16 v[8:11], a[48:51], a[124:127], v[8:11]\n"
    "ds_read_b64_tr_b16 a[10:11], v228 offset:4608\n"
    "v_mfma_f32_16x16x32_bf16 v[12:15], a[48:51], a[128:131], v[12:15]\n"
    "ds_read_b64_tr_b16 a[12:13], v229 offset:4096\n"
    "v_mfma_f32_16x16x32_bf16 v[16:19], a[48:51], a[132:135], v[16:19]\n"
    "ds_read_b64_tr_b16 a[14:15], v229 offset:4608\n"
    "v_mfma_f32_16x16x32_bf16 v[20:23], a[48:51], a[136:139], v[20:23]\n"
    "ds_read_b64_tr_b16 a[16:17], v228 offset:8192\n"
    "v_mfma_f32_16x16x32_bf16 v[24:27], a[48:51], a[140:143], v[24:27]\n"
    "ds_read_b64_tr_b16 a[18:19], v228 offset:8704\n"
    "v_mfma_f32_16x16x32_bf16 v[28:31], a[48:51], a[144:147], v[28:31]\n"
    "ds_read_b64_tr_b16 a[20:21], v229 offset:8192\n"
    "v_mfma_f32_16x16x32_bf16 v[132:135], a[48:51], a[148:151], v[132:135]\n"
    "buffer_load_dwordx4 v236, s[40:43], s56 offen offset:3072 sc1 lds\n"
    "v_mfma_f32_16x16x32_bf16 v[32:35], a[52:55], a[116:119], v[32:35]\n"
    "ds_read_b64_tr_b16 a[22:23], v229 offset:8704\n"
    "v_mfma_f32_16x16x32_bf16 v[36:39], a[52:55], a[120:123], v[36:39]\n"
    "ds_read_b64_tr_b16 a[24:25], v228 offset:12288\n"
    "v_mfma_f32_16x16x32_bf16 v[40:43], a[52:55], a[124:127], v[40:43]\n"
    "ds_read_b64_tr_b16 a[26:27], v228 offset:12800\n"
    "v_mfma_f32_16x16x32_bf16 v[44:47], a[52:55], a[128:131], v[44:47]\n"
    "ds_read_b64_tr_b16 a[28:29], v229 offset:12288\n"
    "v_mfma_f32_16x16x32_bf16 v[48:51], a[52:55], a[132:135], v[48:51]\n"
    "ds_read_b64_tr_b16 a[30:31], v229 offset:12800\n"
    "v_mfma_f32_16x16x32_bf16 v[52:55], a[52:55], a[136:139], v[52:55]\n"
    "s_mov_b32 m0, 113664\n"
    "v_mfma_f32_16x16x32_bf16 v[56:59], a[52:55], a[140:143], v[56:59]\n"
    "s_mov_b64 exec, s[64:65]\n"
    "buffer_load_dwordx4 v237, s[40:43], s56 offen sc1 lds\n"
    "s_mov_b64 exec, -1\n"
    "v_mfma_f32_16x16x32_bf16 v[60:63], a[52:55], a[144:147], v[60:63]\n"
    "s_add_u32 s56, s56, 131072\n"
    "v_mfma_f32_16x16x32_bf16 v[68:71], a[116:119], a[56:59], v[68:71]\n"
    "ds_read_b64_tr_b16 a[32:33], v228 offset:16384\n"
    "v_mfma_f32_16x16x32_bf16 v[72:75], a[120:123], a[56:59], v[72:75]\n"
    "ds_read_b64_tr_b16 a[34:35], v228 offset:16896\n"
    "v_mfma_f32_16x16x32_bf16 v[76:79], a[124:127], a[56:59], v[76:79]\n"
    "ds_read_b64_tr_b16 a[36:37], v229 offset:16384\n"
    "v_mfma_f32_16x16x32_bf16 v[80:83], a[128:131], a[56:59], v[80:83]\n"
    "ds_read_b64_tr_b16 a[38:39], v229 offset:16896\n"
    "v_mfma_f32_16x16x32_bf16 v[84:87], a[132:135], a[56:59], v[84:87]\n"
    "s_add_u32 m0, s62, 131072\n"
    "v_mfma_f32_16x16x32_bf16 v[88:91], a[136:139], a[56:59], v[88:91]\n"
    "buffer_load_dwordx4 v238, s[44:47], s57 offen lds\n"
    "v_mfma_f32_16x16x32_bf16 v[92:95], a[140:143], a[56:59], v[92:95]\n"
    "s_add_u32 s57, s57, 196608\n"
    "v_mfma_f32_16x16x32_bf16 v[96:99], a[144:147], a[56:59], v[96:99]\n"
    "v_mfma_f32_16x16x32_bf16 v[64:67], a[148:151], a[56:59], v[64:67]\n"
    "v_mfma_f32_16x16x32_bf16 v[100:103], a[116:119], a[60:63], v[100:103]\n"
    "v_mfma_f32_16x16x32_bf16 v[104:107], a[120:123], a[60:63], v[104:107]\n"
    "v_mfma_f32_16x16x32_bf16 v[108:111], a[124:127], a[60:63], v[108:111]\n"
    "s_add_u32 m0, s62, 135168\n"
    "v_mfma_f32_16x16x32_bf16 v[112:115], a[128:131], a[60:63], v[112:115]\n"
    "buffer_load_dwordx4 v238, s[44:47], s58 offen lds\n"
    "v_mfma_f32_16x16x32_bf16 v[116:119], a[132:135], a[60:63], v[116:119]\n"
    "s_add_u32 s58, s58, 196608\n"
    "v_mfma_f32_16x16x32_bf16 v[120:123], a[136:139], a[60:63], v[120:123]\n"
    "v_mfma_f32_16x16x32_bf16 v[124:127], a[140:143], a[60:63], v[124:127]\n"
    "v_mfma_f32_16x16x32_bf16 v[128:131], a[144:147], a[60:63], v[128:131]\n"
    "v_mfma_f32_16x16x32_bf16 a[152:155], a[64:67], a[116:119], a[152:155]\n"
    "v_mfma_f32_16x16x32_bf16 a[156:159], a[64:67], a[120:123], a[156:159]\n"
    "s_add_u32 m0, s62, 139264\n"
    "v_mfma_f32_16x16x32_bf16 a[160:163], a[64:67], a[124:127], a[160:163]\n"
    "buffer_load_dwordx4 v238, s[44:47], s59 offen lds\n"
    "v_mfma_f32_16x16x32_bf16 a[164:167], a[64:67], a[128:131], a[164:167]\n"
    "s_add_u32 s59, s59, 196608\n"
    "v_mfma_f32_16x16x32_bf16 a[168:171], a[64:67], a[132:135], a[168:171]\n"
    "v_mfma_f32_16x16x32_bf16 a[172:175], a[64:67], a[136:139], a[172:175]\n"
    "v_mfma_f32_16x16x32_bf16 a[176:179], a[64:67], a[140:143], a[176:179]\n"
    "v_mfma_f32_16x16x32_bf16 a[180:183], a[64:67], a[144:147], a[180:183]\n"
    "v_mfma_f32_16x16x32_bf16 a[184:187], a[68:71], a[116:119], a[184:187]\n"
    "s_add_u32 m0, s62, 143360\n"
    "v_mfma_f32_16x16x32_bf16 a[188:191], a[68:71], a[120:123], a[188:191]\n"
    "buffer_load_dwordx4 v239, s[48:51], s60 offen lds\n"
    "v_mfma_f32_16x16x32_bf16 a[192:195], a[68:71], a[124:127], a[192:195]\n"
    "v_mfma_f32_16x16x32_bf16 a[196:199], a[68:71], a[128:131], a[196:199]\n"
    "v_mfma_f32_16x16x32_bf16 a[200:203], a[68:71], a[132:135], a[200:203]\n"
    "v_mfma_f32_16x16x32_bf16 a[204:207], a[68:71], a[136:139], a[204:207]\n"
    "v_mfma_f32_16x16x32_bf16 a[208:211], a[68:71], a[140:143], a[208:211]\n"
    "v_mfma_f32_16x16x32_bf16 a[212:215], a[68:71], a[144:147], a[212:215]\n"
    "s_add_u32 m0, s62, 147456\n"
    "v_mfma_f32_16x16x32_bf16 v[136:139], a[72:75], a[116:119], v[136:139]\n"
    "buffer_load_dwordx4 v239, s[52:55], s60 offen lds\n"
    "v_mfma_f32_16x16x32_bf16 v[140:143], a[72:75], a[120:123], v[140:143]\n"
    "s_add_u32 s60, s60, 229376\n"
    "v_mfma_f32_16x16x32_bf16 v[144:147], a[72:75], a[124:127], v[144:147]\n"
    "v_mfma_f32_16x16x32_bf16 v[148:151], a[72:75], a[128:131], v[148:151]\n"
    "v_mfma_f32_16x16x32_bf16 v[152:155], a[72:75], a[132:135], v[152:155]\n"
    "v_mfma_f32_16x16x32_bf16 v[156:159], a[72:75], a[136:139], v[156:159]\n"
    "v_mfma_f32_16x16x32_bf16 v[160:163], a[72:75], a[140:143], v[160:163]\n"
    "s_mov_b32 m0, 155648\n"
    "v_mfma_f32_16x16x32_bf16 v[164:167], a[72:75], a[144:147], v[164:167]\n"
    "global_load_lds_dwordx4 v244, s[68:69]\n"
    "v_mfma_f32_16x16x32_bf16 v[168:171], a[76:79], a[116:119], v[168:171]\n"
    "v_mfma_f32_16x16x32_bf16 v[172:175], a[76:79], a[120:123], v[172:175]\n"
    "v_mfma_f32_16x16x32_bf16 v[176:179], a[76:79], a[124:127], v[176:179]\n"
    "v_mfma_f32_16x16x32_bf16 v[180:183], a[76:79], a[128:131], v[180:183]\n"
    "v_mfma_f32_16x16x32_bf16 v[184:187], a[76:79], a[132:135], v[184:187]\n"
    "v_mfma_f32_16x16x32_bf16 v[188:191], a[76:79], a[136:139], v[188:191]\n"
    "v_mfma_f32_16x16x32_bf16 v[192:195], a[76:79], a[140:143], v[192:195]\n"
    "global_load_lds_dwordx4 v244, s[68:69] offset:1024\n"
    "v_mfma_f32_16x16x32_bf16 v[196:199], a[76:79], a[144:147], v[196:199]\n"
    "s_mov_b32 s66, 6\n"
    "LOOPm_%=:\n"
    "s_waitcnt vmcnt(21) lgkmcnt(0)\n"
    "s_barrier\n"
    "v_mfma_f32_16x16x32_bf16 v[200:203], a[0:3], a[80:83], v[200:203]\n"
    "ds_read_b128 a[116:119], v224 offset:38912\n"
    "v_mfma_f32_16x16x32_bf16 v[208:211], a[0:3], a[84:87], v[208:211]\n"
    "ds_read_b128 a[120:123], v224 offset:39936\n"
    "v_mfma_f32_16x16x32_bf16 v[216:219], a[0:3], a[88:91], v[216:219]\n"
    "s_add_u32 m0, s63, 1024\n"
    "v_mfma_f32_16x16x32_bf16 a[216:219], a[0:3], a[92:95], a[216:219]\n"
    "buffer_load_dwordx4 v236, s[40:43], s56 offen sc1 lds\n"
    "v_mfma_f32_16x16x32_bf16 a[224:227], a[0:3], a[96:99], a[224:227]\n"
    "ds_read_b128 a[124:127], v224 offset:40960\n"
    "v_mfma_f32_16x16x32_bf16 a[232:235], a[0:3], a[100:103], a[232:235]\n"
    "ds_read_b128 a[128:131], v224 offset:41984\n"
    "v_mfma_f32_16x16x32_bf16 a[240:243], a[0:3], a[104:107], a[240:243]\n"
    "ds_read_b128 a[132:135], v224 offset:43008\n"
    "v_mfma_f32_16x16x32_bf16 a[248:251], a[0:3], a[108:111], a[248:251]\n"
    "ds_read_b128 a[136:139], v224 offset:44032\n"
    "v_mfma_f32_16x16x32_bf16 v[204:207], a[4:7], a[80:83], v[204:207]\n"
    "ds_read_b128 a[140:143], v224 offset:45056\n"
    "v_mfma_f32_16x16x32_bf16 v[212:215], a[4:7], a[84:87], v[212:215]\n"
    "ds_read_b128 a[144:147], v224 offset:46080\n"
    "v_mfma_f32_16x16x32_bf16 v[220:223], a[4:7], a[88:91], v[220:223]\n"
    "buffer_load_dwordx4 v236, s[40:43], s56 offen offset:1024 sc1 lds\n"
    "v_mfma_f32_16x16x32_bf16 a[220:223], a[4:7], a[92:95], a[220:223]\n"
    "ds_read_b128 a[148:151], v226 offset:37888\n"
    "v_mfma_f32_16x16x32_bf16 a[228:231], a[4:7], a[96:99], a[228:231]\n"
    "ds_read_b64_tr_b16 a[40:41], v228 offset:37888\n"
    "v_mfma_f32_16x16x32_bf16 a[236:239], a[4:7], a[100:103], a[236:239]\n"
    "ds_read_b64_tr_b16 a[42:43], v228 offset:38400\n"
    "v_mfma_f32_16x16x32_bf16 a[244:247], a[4:7], a[104:107], a[244:247]\n"
    "ds_read_b64_tr_b16 a[44:45], v229 offset:37888\n"
    "v_mfma_f32_16x16x32_bf16 a[252:255], a[4:7], a[108:111], a[252:255]\n"
    "ds_read_b64_tr_b16 a[46:47], v229 offset:38400\n"
    "v_mfma_f32_16x16x32_bf16 v[0:3], a[8:11], a[80:83], v[0:3]\n"
    "ds_read_b64_tr_b16 a[48:49], v228 offset:41984\n"
    "v_mfma_f32_16x16x32_bf16 v[4:7], a[8:11], a[84:87], v[4:7]\n"
    "buffer_load_dwordx4 v236, s[40:43], s56 offen offset:2048 sc1 lds\n"
    "v_mfma_f32_16x16x32_bf16 v[8:11], a[8:11], a[88:91], v[8:11]\n"
    "ds_read_b64_tr_b16 a[50:51], v228 offset:42496\n"
    "v_mfma_f32_16x16x32_bf16 v[12:15], a[8:11], a[92:95], v[12:15]\n"
    "ds_read_b64_tr_b16 a[52:53], v229 offset:41984\n"
    "v_mfma_f32_16x16x32_bf16 v[16:19], a[8:11], a[96:99], v[16:19]\n"
    "ds_read_b64_tr_b16 a[54:55], v229 offset:42496\n"
    "v_mfma_f32_16x16x32_bf16 v[20:23], a[8:11], a[100:103], v[20:23]\n"
    "ds_read_b64_tr_b16 a[56:57], v228 offset:46080\n"
    "v_mfma_f32_16x16x32_bf16 v[24:27], a[8:11], a[104:107], v[24:27]\n"
    "ds_read_b64_tr_b16 a[58:59], v228 offset:46592\n"
    "v_mfma_f32_16x16x32_bf16 v[28:31], a[8:11], a[108:111], v[28:31]\n"
    "ds_read_b64_tr_b16 a[60:61], v229 offset:46080\n"
    "v_mfma_f32_16x16x32_bf16 v[132:135], a[8:11], a[112:115], v[132:135]\n"
    "buffer_load_dwordx4 v236, s[40:43], s56 offen offset:3072 sc1 lds\n"
    "v_mfma_f32_16x16x32_bf16 v[32:35], a[12:15], a[80:83], v[32:35]\n"
    "ds_read_b64_tr_b16 a[62:63], v229 offset:46592\n"
    "v_mfma_f32_16x16x32_bf16 v[36:39], a[12:15], a[84:87], v[36:39]\n"
    "ds_read_b64_tr_b16 a[64:65], v228 offset:50176\n"
    "v_mfma_f32_16x16x32_bf16 v[40:43], a[12:15], a[88:91], v[40:43]\n"
    "ds_read_b64_tr_b16 a[66:67], v228 offset:50688\n"
    "v_mfma_f32_16x16x32_bf16 v[44:47], a[12:15], a[92:95], v[44:47]\n"
    "ds_read_b64_tr_b16 a[68:69], v229 offset:50176\n"
    "v_mfma_f32_16x16x32_bf16 v[48:51], a[12:15], a[96:99], v[48:51]\n"
    "ds_read_b64_tr_b16 a[70:71], v229 offset:50688\n"
    "v_mfma_f32_16x16x32_bf16 v[52:55], a[12:15], a[100:103], v[52:55]\n"
    "s_mov_b32 m0, 0\n"
    "v_mfma_f32_16x16x32_bf16 v[56:59], a[12:15], a[104:107], v[56:59]\n"
    "s_mov_b64 exec, s[64:65]\n"
    "buffer_load_dwordx4 v237, s[40:43], s56 offen sc1 lds\n"
    "s_mov_b64 exec, -1\n"
    "v_mfma_f32_16x16x32_bf16 v[60:63], a[12:15], a[108:111], v[60:63]\n"
    "s_add_u32 s56, s56, 131072\n"
    "v_mfma_f32_16x16x32_bf16 v[68:71], a[80:83], a[16:19], v[68:71]\n"
    "ds_read_b64_tr_b16 a[72:73], v228 offset:54272\n"
    "v_mfma_f32_16x16x32_bf16 v[72:75], a[84:87], a[16:19], v[72:75]\n"
    "ds_read_b64_tr_b16 a[74:75], v228 offset:54784\n"
    "v_mfma_f32_16x16x32_bf16 v[76:79], a[88:91], a[16:19], v[76:79]\n"
    "ds_read_b64_tr_b16 a[76:77], v229 offset:54272\n"
    "v_mfma_f32_16x16x32_bf16 v[80:83], a[92:95], a[16:19], v[80:83]\n"
    "ds_read_b64_tr_b16 a[78:79], v229 offset:54784\n"
    "v_mfma_f32_16x16x32_bf16 v[84:87], a[96:99], a[16:19], v[84:87]\n"
    "s_add_u32 m0, s62, 17408\n"
    "v_mfma_f32_16x16x32_bf16 v[88:91], a[100:103], a[16:19], v[88:91]\n"
    "buffer_load_dwordx4 v238, s[44:47], s57 offen lds\n"
    "v_mfma_f32_16x16x32_bf16 v[92:95], a[104:107], a[16:19], v[92:95]\n"
    "s_add_u32 s57, s57, 196608\n"
    "v_mfma_f32_16x16x32_bf16 v[96:99], a[108:111], a[16:19], v[96:99]\n"
    "v_mfma_f32_16x16x32_bf16 v[64:67], a[112:115], a[16:19], v[64:67]\n"
    "v_mfma_f32_16x16x32_bf16 v[100:103], a[80:83], a[20:23], v[100:103]\n"
    "v_mfma_f32_16x16x32_bf16 v[104:107], a[84:87], a[20:23], v[104:107]\n"
    "v_mfma_f32_16x16x32_bf16 v[108:111], a[88:91], a[20:23], v[108:111]\n"
    "s_add_u32 m0, s62, 21504\n"
    "v_mfma_f32_16x16x32_bf16 v[112:115], a[92:95], a[20:23], v[112:115]\n"
    "buffer_load_dwordx4 v238, s[44:47], s58 offen lds\n"
    "v_mfma_f32_16x16x32_bf16 v[116:119], a[96:99], a[20:23], v[116:119]\n"
    "s_add_u32 s58, s58, 196608\n"
    "v_mfma_f32_16x16x32_bf16 v[120:123], a[100:103], a[20:23], v[120:123]\n"
    "v_mfma_f32_16x16x32_bf16 v[124:127], a[104:107], a[20:23], v[124:127]\n"
    "v_mfma_f32_16x16x32_bf16 v[128:131], a[108:111], a[20:23], v[128:131]\n"
    "v_mfma_f32_16x16x32_bf16 a[152:155], a[24:27], a[80:83], a[152:155]\n"
    "v_mfma_f32_16x16x32_bf16 a[156:159], a[24:27], a[84:87], a[156:159]\n"
    "s_add_u32 m0, s62, 25600\n"
    "v_mfma_f32_16x16x32_bf16 a[160:163], a[24:27], a[88:91], a[160:163]\n"
    "buffer_load_dwordx4 v238, s[44:47], s59 offen lds\n"
    "v_mfma_f32_16x16x32_bf16 a[164:167], a[24:27], a[92:95], a[164:167]\n"
    "s_add_u32 s59, s59, 196608\n"
    "v_mfma_f32_16x16x32_bf16 a[168:171], a[24:27], a[96:99], a[168:171]\n"
    "v_mfma_f32_16x16x32_bf16 a[172:175], a[24:27], a[100:103], a[172:175]\n"
    "v_mfma_f32_16x16x32_bf16 a[176:179], a[24:27], a[104:107], a[176:179]\n"
    "v_mfma_f32_16x16x32_bf16 a[180:183], a[24:27], a[108:111], a[180:183]\n"
    "v_mfma_f32_16x16x32_bf16 a[184:187], a[28:31], a[80:83], a[184:187]\n"
    "s_add_u32 m0, s62, 29696\n"
    "v_mfma_f32_16x16x32_bf16 a[188:191], a[28:31], a[84:87], a[188:191]\n"
    "buffer_load_dwordx4 v239, s[48:51], s60 offen lds\n"
    "v_mfma_f32_16x16x32_bf16 a[192:195], a[28:31], a[88:91], a[192:195]\n"
    "v_mfma_f32_16x16x32_bf16 a[196:199], a[28:31], a[92:95], a[196:199]\n"
    "v_mfma_f32_16x16x32_bf16 a[200:203], a[28:31], a[96:99], a[200:203]\n"
    "v_mfma_f32_16x16x32_bf16 a[204:207], a[28:31], a[100:103], a[204:207]\n"
    "v_mfma_f32_16x16x32_bf16 a[208:211], a[28:31], a[104:107], a[208:211]\n"
    "v_mfma_f32_16x16x32_bf16 a[212:215], a[28:31], a[108:111], a[212:215]\n"
    "s_add_u32 m0, s62, 33792\n"
    "v_mfma_f32_16x16x32_bf16 v[136:139], a[32:35], a[80:83], v[136:139]\n"
    "buffer_load_dwordx4 v239, s[52:55], s60 offen lds\n"
    "v_mfma_f32_16x16x32_bf16 v[140:143], a[32:35], a[84:87], v[140:143]\n"
    "s_add_u32 s60, s60, 229376\n"
    "v_mfma_f32_16x16x32_bf16 v[144:147], a[32:35], a[88:91], v[144:147]\n"
    "v_mfma_f32_16x16x32_bf16 v[148:151], a[32:35], a[92:95], v[148:151]\n"
    "v_mfma_f32_16x16x32_bf16 v[152:155], a[32:35], a[96:99], v[152:155]\n"
    "v_mfma_f32_16x16x32_bf16 v[156:159], a[32:35], a[100:103], v[156:159]\n"
    "v_mfma_f32_16x16x32_bf16 v[160:163], a[32:35], a[104:107], v[160:163]\n"
    "v_mfma_f32_16x16x32_bf16 v[164:167], a[32:35], a[108:111], v[164:167]\n"
    "buffer_load_dword v232, v245, s[20:23], s74 offen\n"
    "v_mfma_f32_16x16x32_bf16 v[168:171], a[36:39], a[80:83], v[168:171]\n"
    "s_add_u32 s74, s74, s88\n"
    "v_mfma_f32_16x16x32_bf16 v[172:175], a[36:39], a[84:87], v[172:175]\n"
    "v_mfma_f32_16x16x32_bf16 v[176:179], a[36:39], a[88:91], v[176:179]\n"
    "v_mfma_f32_16x16x32_bf16 v[180:183], a[36:39], a[92:95], v[180:183]\n"
    "v_mfma_f32_16x16x32_bf16 v[184:187], a[36:39], a[96:99], v[184:187]\n"
    "v_mfma_f32_16x16x32_bf16 v[188:191], a[36:39], a[100:103], v[188:191]\n"
    "s_mov_b32 m0, 155648\n"
    "v_mfma_f32_16x16x32_bf16 v[192:195], a[36:39], a[104:107], v[192:195]\n"
    "global_load_lds_dwordx4 v244, s[70:71]\n"
    "v_mfma_f32_16x16x32_bf16 v[196:199], a[36:39], a[108:111], v[196:199]\n"
    "s_add_u32 s70, s70, 1024\n"
    "s_addc_u32 s71, s71, 0\n"
    "s_waitcnt vmcnt(23) lgkmcnt(0)\n"
    "s_barrier\n"
    "v_mfma_f32_16x16x32_bf16 v[200:203], a[40:43], a[116:119], v[200:203]\n"
    "ds_read_b128 a[80:83], v225 offset:1024\n"
    "v_mfma_f32_16x16x32_bf16 v[208:211], a[40:43], a[120:123], v[208:211]\n"
    "ds_read_b128 a[84:87], v225 offset:2048\n"
    "v_mfma_f32_16x16x32_bf16 v[216:219], a[40:43], a[124:127], v[216:219]\n"
    "s_add_u32 m0, s63, 38912\n"
    "v_mfma_f32_16x16x32_bf16 a[216:219], a[40:43], a[128:131], a[216:219]\n"
    "buffer_load_dwordx4 v236, s[40:43], s56 offen sc1 lds\n"
    "v_mfma_f32_16x16x32_bf16 a[224:227], a[40:43], a[132:135], a[224:227]\n"
    "ds_read_b128 a[88:91], v225 offset:3072\n"
    "v_mfma_f32_16x16x32_bf16 a[232:235], a[40:43], a[136:139], a[232:235]\n"
    "ds_read_b128 a[92:95], v225 offset:4096\n"
    "v_mfma_f32_16x16x32_bf16 a[240:243], a[40:43], a[140:143], a[240:243]\n"
    "ds_read_b128 a[96:99], v225 offset:5120\n"
    "v_mfma_f32_16x16x32_bf16 a[248:251], a[40:43], a[144:147], a[248:251]\n"
    "ds_read_b128 a[100:103], v225 offset:6144\n"
    "v_mfma_f32_16x16x32_bf16 v[204:207], a[44:47], a[116:119], v[204:207]\n"
    "ds_read_b128 a[104:107], v225 offset:7168\n"
    "v_mfma_f32_16x16x32_bf16 v[212:215], a[44:47], a[120:123], v[212:215]\n"
    "ds_read_b128 a[108:111], v225 offset:8192\n"
    "v_mfma_f32_16x16x32_bf16 v[220:223], a[44:47], a[124:127], v[220:223]\n"
    "ds_read_b128 a[112:115], v227 offset:0\n"
    "v_mfma_f32_16x16x32_bf16 a[220:223], a[44:47], a[128:131], a[220:223]\n"
    "buffer_load_dwordx4 v236, s[40:43], s56 offen offset:1024 sc1 lds\n"
    "v_mfma_f32_16x16x32_bf16 a[228:231], a[44:47], a[132:135], a[228:231]\n"
    "ds_read_b64_tr_b16 a[0:1], v230 offset:0\n"
    "v_mfma_f32_16x16x32_bf16 a[236:239], a[44:47], a[136:139], a[236:239]\n"
    "ds_read_b64_tr_b16 a[2:3], v230 offset:512\n"
    "v_mfma_f32_16x16x32_bf16 a[244:247], a[44:47], a[140:143], a[244:247]\n"
    "ds_read_b64_tr_b16 a[4:5], v231 offset:0\n"
    "v_mfma_f32_16x16x32_bf16 a[252:255], a[44:47], a[144:147], a[252:255]\n"
    "ds_read_b64_tr_b16 a[6:7], v231 offset:512\n"
    "v_mfma_f32_16x16x32_bf16 v[0:3], a[48:51], a[116:119], v[0:3]\n"
    "ds_read_b64_tr_b16 a[8:9], v230 offset:4096\n"
    "v_mfma_f32_16x16x32_bf16 v[4:7], a[48:51], a[120:123], v[4:7]\n"
    "ds_read_b64_tr_b16 a[10:11], v230 offset:4608\n"
    "v_mfma_f32_16x16x32_bf16 v[8:11], a[48:51], a[124:127], v[8:11]\n"
    "ds_read_b64_tr_b16 a[12:13], v231 offset:4096\n"
    "v_mfma_f32_16x16x32_bf16 v[12:15], a[48:51], a[128:131], v[12:15]\n"
    "buffer_load_dwordx4 v236, s[40:43], s56 offen offset:2048 sc1 lds\n"
    "v_mfma_f32_16x16x32_bf16 v[16:19], a[48:51], a[132:135], v[16:19]\n"
    "ds_read_b64_tr_b16 a[14:15], v231 offset:4608\n"
    "v_mfma_f32_16x16x32_bf16 v[20:23], a[48:51], a[136:139], v[20:23]\n"
    "ds_read_b64_tr_b16 a[16:17], v230 offset:8192\n"
    "v_mfma_f32_16x16x32_bf16 v[24:27], a[48:51], a[140:143], v[24:27]\n"
    "ds_read_b64_tr_b16 a[18:19], v230 offset:8704\n"
    "v_mfma_f32_16x16x32_bf16 v[28:31], a[48:51], a[144:147], v[28:31]\n"
    "ds_read_b64_tr_b16 a[20:21], v231 offset:8192\n"
    "v_mfma_f32_16x16x32_bf16 v[132:135], a[48:51], a[148:151], v[132:135]\n"
    "ds_read_b64_tr_b16 a[22:23], v231 offset:8704\n"
    "v_mfma_f32_16x16x32_bf16 v[32:35], a[52:55], a[116:119], v[32:35]\n"
    "ds_read_b64_tr_b16 a[24:25], v230 offset:12288\n"
    "v_mfma_f32_16x16x32_bf16 v[36:39], a[52:55], a[120:123], v[36:39]\n"
    "ds_read_b64_tr_b16 a[26:27], v230 offset:12800\n"
    "v_mfma_f32_16x16x32_bf16 v[40:43], a[52:55], a[124:127], v[40:43]\n"
    "buffer_load_dwordx4 v236, s[40:43], s56 offen offset:3072 sc1 lds\n"
    "v_mfma_f32_16x16x32_bf16 v[44:47], a[52:55], a[128:131], v[44:47]\n"
    "ds_read_b64_tr_b16 a[28:29], v231 offset:12288\n"
    "v_mfma_f32_16x16x32_bf16 v[48:51], a[52:55], a[132:135], v[48:51]\n"
    "ds_read_b64_tr_b16 a[30:31], v231 offset:12800\n"
    "v_mfma_f32_16x16x32_bf16 v[52:55], a[52:55], a[136:139], v[52:55]\n"
    "ds_read_b64_tr_b16 a[32:33], v230 offset:16384\n"
    "v_mfma_f32_16x16x32_bf16 v[56:59], a[52:55], a[140:143], v[56:59]\n"
    "ds_read_b64_tr_b16 a[34:35], v230 offset:16896\n"
    "v_mfma_f32_16x16x32_bf16 v[60:63], a[52:55], a[144:147], v[60:63]\n"
    "ds_read_b64_tr_b16 a[36:37], v231 offset:16384\n"
    "v_mfma_f32_16x16x32_bf16 v[68:71], a[116:119], a[56:59], v[68:71]\n"
    "ds_read_b64_tr_b16 a[38:39], v231 offset:16896\n"
    "v_mfma_f32_16x16x32_bf16 v[72:75], a[120:123], a[56:59], v[72:75]\n"
    "s_mov_b32 m0, 37888\n"
    "v_mfma_f32_16x16x32_bf16 v[76:79], a[124:127], a[56:59], v[76:79]\n"
    "s_mov_b64 exec, s[64:65]\n"
    "buffer_load_dwordx4 v237, s[40:43], s56 offen sc1 lds\n"
    "s_mov_b64 exec, -1\n"
    "v_mfma_f32_16x16x32_bf16 v[80:83], a[128:131], a[56:59], v[80:83]\n"
    "s_add_u32 s56, s56, 131072\n"
    "v_mfma_f32_16x16x32_bf16 v[84:87], a[132:135], a[56:59], v[84:87]\n"
    "v_mfma_f32_16x16x32_bf16 v[88:91], a[136:139], a[56:59], v[88:91]\n"
    "v_mfma_f32_16x16x32_bf16 v[92:95], a[140:143], a[56:59], v[92:95]\n"
    "v_mfma_f32_16x16x32_bf16 v[96:99], a[144:147], a[56:59], v[96:99]\n"
    "v_mfma_f32_16x16x32_bf16 v[64:67], a[148:151], a[56:59], v[64:67]\n"
    "v_mfma_f32_16x16x32_bf16 v[100:103], a[116:119], a[60:63], v[100:103]\n"
    "s_add_u32 m0, s62, 55296\n"
    "v_mfma_f32_16x16x32_bf16 v[104:107], a[120:123], a[60:63], v[104:107]\n"
    "buffer_load_dwordx4 v238, s[44:47], s57 offen lds\n"
    "v_mfma_f32_16x16x32_bf16 v[108:111], a[124:127], a[60:63], v[108:111]\n"
    "s_add_u32 s57, s57, 196608\n"
    "v_mfma_f32_16x16x32_bf16 v[112:115], a[128:131], a[60:63], v[112:115]\n"
    "v_mfma_f32_16x16x32_bf16 v[116:119], a[132:135], a[60:63], v[116:119]\n"
    "v_mfma_f32_16x16x32_bf16 v[120:123], a[136:139], a[60:63], v[120:123]\n"
    "v_mfma_f32_16x16x32_bf16 v[124:127], a[140:143], a[60:63], v[124:127]\n"
    "v_mfma_f32_16x16x32_bf16 v[128:131], a[144:147], a[60:63], v[128:131]\n"
    "v_mfma_f32_16x16x32_bf16 a[152:155], a[64:67], a[116:119], a[152:155]\n"
    "s_add_u32 m0, s62, 59392\n"
    "v_mfma_f32_16x16x32_bf16 a[156:159], a[64:67], a[120:123], a[156:159]\n"
    "buffer_load_dwordx4 v238, s[44:47], s58 offen lds\n"
    "v_mfma_f32_16x16x32_bf16 a[160:163], a[64:67], a[124:127], a[160:163]\n"
    "s_add_u32 s58, s58, 196608\n"
    "v_mfma_f32_16x16x32_bf16 a[164:167], a[64:67], a[128:131], a[164:167]\n"
    "v_mfma_f32_16x16x32_bf16 a[168:171], a[64:67], a[132:135], a[168:171]\n"
    "v_mfma_f32_16x16x32_bf16 a[172:175], a[64:67], a[136:139], a[172:175]\n"
    "v_mfma_f32_16x16x32_bf16 a[176:179], a[64:67], a[140:143], a[176:179]\n"
    "v_mfma_f32_16x16x32_bf16 a[180:183], a[64:67], a[144:147], a[180:183]\n"
    "v_mfma_f32_16x16x32_bf16 a[184:187], a[68:71], a[116:119], a[184:187]\n"
    "s_add_u32 m0, s62, 63488\n"
    "v_mfma_f32_16x16x32_bf16 a[188:191], a[68:71], a[120:123], a[188:191]\n"
    "buffer_load_dwordx4 v238, s[44:47], s59 offen lds\n"
    "v_mfma_f32_16x16x32_bf16 a[192:195], a[68:71], a[124:127], a[192:195]\n"
    "s_add_u32 s59, s59, 196608\n"
    "v_mfma_f32_16x16x32_bf16 a[196:199], a[68:71], a[128:131], a[196:199]\n"
    "v_mfma_f32_16x16x32_bf16 a[200:203], a[68:71], a[132:135], a[200:203]\n"
    "v_mfma_f32_16x16x32_bf16 a[204:207], a[68:71], a[136:139], a[204:207]\n"
    "v_mfma_f32_16x16x32_bf16 a[208:211], a[68:71], a[140:143], a[208:211]\n"
    "v_mfma_f32_16x16x32_bf16 a[212:215], a[68:71], a[144:147], a[212:215]\n"
    "v_mfma_f32_16x16x32_bf16 v[136:139], a[72:75], a[116:119], v[136:139]\n"
    "s_add_u32 m0, s62, 67584\n"
    "v_mfma_f32_16x16x32_bf16 v[140:143], a[72:75], a[120:123], v[140:143]\n"
    "buffer_load_dwordx4 v239, s[48:51], s60 offen lds\n"
    "v_mfma_f32_16x16x32_bf16 v[144:147], a[72:75], a[124:127], v[144:147]\n"
    "v_mfma_f32_16x16x32_bf16 v[148:151], a[72:75], a[128:131], v[148:151]\n"
    "v_mfma_f32_16x16x32_bf16 v[152:155], a[72:75], a[132:135], v[152:155]\n"
    "v_mfma_f32_16x16x32_bf16 v[156:159], a[72:75], a[136:139], v[156:159]\n"
    "v_mfma_f32_16x16x32_bf16 v[160:163], a[72:75], a[140:143], v[160:163]\n"
    "v_mfma_f32_16x16x32_bf16 v[164:167], a[72:75], a[144:147], v[164:167]\n"
    "v_mfma_f32_16x16x32_bf16 v[168:171], a[76:79], a[116:119], v[168:171]\n"
    "s_add_u32 m0, s62, 71680\n"
    "v_mfma_f32_16x16x32_bf16 v[172:175], a[76:79], a[120:123], v[172:175]\n"
    "buffer_load_dwordx4 v239, s[52:55], s60 offen lds\n"
    "v_mfma_f32_16x16x32_bf16 v[176:179], a[76:79], a[124:127], v[176:179]\n"
    "s_add_u32 s60, s60, 229376\n"
    "v_mfma_f32_16x16x32_bf16 v[180:183], a[76:79], a[128:131], v[180:183]\n"
    "v_mfma_f32_16x16x32_bf16 v[184:187], a[76:79], a[132:135], v[184:187]\n"
    "v_mfma_f32_16x16x32_bf16 v[188:191], a[76:79], a[136:139], v[188:191]\n"
    "v_mfma_f32_16x16x32_bf16 v[192:195], a[76:79], a[140:143], v[192:195]\n"
    "v_mfma_f32_16x16x32_bf16 v[196:199], a[76:79], a[144:147], v[196:199]\n"
    "s_waitcnt vmcnt(22) lgkmcnt(0)\n"
    "s_barrier\n"
    "v_mfma_f32_16x16x32_bf16 v[200:203], a[0:3], a[80:83], v[200:203]\n"
    "ds_read_b128 a[116:119], v225 offset:38912\n"
    "v_mfma_f32_16x16x32_bf16 v[208:211], a[0:3], a[84:87], v[208:211]\n"
    "ds_read_b128 a[120:123], v225 offset:39936\n"
    "v_mfma_f32_16x16x32_bf16 v[216:219], a[0:3], a[88:91], v[216:219]\n"
    "s_add_u32 m0, s63, 76800\n"
    "v_mfma_f32_16x16x32_bf16 a[216:219], a[0:3], a[92:95], a[216:219]\n"
    "buffer_load_dwordx4 v236, s[40:43], s56 offen sc1 lds\n"
    "v_mfma_f32_16x16x32_bf16 a[224:227], a[0:3], a[96:99], a[224:227]\n"
    "ds_read_b128 a[124:127], v225 offset:40960\n"
    "v_mfma_f32_16x16x32_bf16 a[232:235], a[0:3], a[100:103], a[232:235]\n"
    "ds_read_b128 a[128:131], v225 offset:41984\n"
    "v_mfma_f32_16x16x32_bf16 a[240:243], a[0:3], a[104:107], a[240:243]\n"
    "ds_read_b128 a[132:135], v225 offset:43008\n"
    "v_mfma_f32_16x16x32_bf16 a[248:251], a[0:3], a[108:111], a[248:251]\n"
    "ds_read_b128 a[136:139], v225 offset:44032\n"
    "v_mfma_f32_16x16x32_bf16 v[204:207], a[4:7], a[80:83], v[204:207]\n"
    "ds_read_b128 a[140:143], v225 offset:45056\n"
    "v_mfma_f32_16x16x32_bf16 v[212:215], a[4:7], a[84:87], v[212:215]\n"
    "ds_read_b128 a[144:147], v225 offset:46080\n"
    "v_mfma_f32_16x16x32_bf16 v[220:223], a[4:7], a[88:91], v[220:223]\n"
    "buffer_load_dwordx4 v236, s[40:43], s56 offen offset:1024 sc1 lds\n"
    "v_mfma_f32_16x16x32_bf16 a[220:223], a[4:7], a[92:95], a[220:223]\n"
    "ds_read_b128 a[148:151], v227 offset:37888\n"
    "v_mfma_f32_16x16x32_bf16 a[228:231], a[4:7], a[96:99], a[228:231]\n"
    "ds_read_b64_tr_b16 a[40:41], v230 offset:37888\n"
    "v_mfma_f32_16x16x32_bf16 a[236:239], a[4:7], a[100:103], a[236:239]\n"
    "ds_read_b64_tr_b16 a[42:43], v230 offset:38400\n"
    "v_mfma_f32_16x16x32_bf16 a[244:247], a[4:7], a[104:107], a[244:247]\n"
    "ds_read_b64_tr_b16 a[44:45], v231 offset:37888\n"
    "v_mfma_f32_16x16x32_bf16 a[252:255], a[4:7], a[108:111], a[252:255]\n"
    "ds_read_b64_tr_b16 a[46:47], v231 offset:38400\n"
    "v_mfma_f32_16x16x32_bf16 v[0:3], a[8:11], a[80:83], v[0:3]\n"
    "ds_read_b64_tr_b16 a[48:49], v230 offset:41984\n"
    "v_mfma_f32_16x16x32_bf16 v[4:7], a[8:11], a[84:87], v[4:7]\n"
    "buffer_load_dwordx4 v236, s[40:43], s56 offen offset:2048 sc1 lds\n"
    "v_mfma_f32_16x16x32_bf16 v[8:11], a[8:11], a[88:91], v[8:11]\n"
    "ds_read_b64_tr_b16 a[50:51], v230 offset:42496\n"
    "v_mfma_f32_16x16x32_bf16 v[12:15], a[8:11], a[92:95], v[12:15]\n"
    "ds_read_b64_tr_b16 a[52:53], v231 offset:41984\n"
    "v_mfma_f32_16x16x32_bf16 v[16:19], a[8:11], a[96:99], v[16:19]\n"
    "ds_read_b64_tr_b16 a[54:55], v231 offset:42496\n"
    "v_mfma_f32_16x16x32_bf16 v[20:23], a[8:11], a[100:103], v[20:23]\n"
    "ds_read_b64_tr_b16 a[56:57], v230 offset:46080\n"
    "v_mfma_f32_16x16x32_bf16 v[24:27], a[8:11], a[104:107], v[24:27]\n"
    "ds_read_b64_tr_b16 a[58:59], v230 offset:46592\n"
    "v_mfma_f32_16x16x32_bf16 v[28:31], a[8:11], a[108:111], v[28:31]\n"
    "ds_read_b64_tr_b16 a[60:61], v231 offset:46080\n"
    "v_mfma_f32_16x16x32_bf16 v[132:135], a[8:11], a[112:115], v[132:135]\n"
    "buffer_load_dwordx4 v236, s[40:43], s56 offen offset:3072 sc1 lds\n"
    "v_mfma_f32_16x16x32_bf16 v[32:35], a[12:15], a[80:83], v[32:35]\n"
    "ds_read_b64_tr_b16 a[62:63], v231 offset:46592\n"
    "v_mfma_f32_16x16x32_bf16 v[36:39], a[12:15], a[84:87], v[36:39]\n"
    "ds_read_b64_tr_b16 a[64:65], v230 offset:50176\n"
    "v_mfma_f32_16x16x32_bf16 v[40:43], a[12:15], a[88:91], v[40:43]\n"
    "ds_read_b64_tr_b16 a[66:67], v230 offset:50688\n"
    "v_mfma_f32_16x16x32_bf16 v[44:47], a[12:15], a[92:95], v[44:47]\n"
    "ds_read_b64_tr_b16 a[68:69], v231 offset:50176\n"
    "v_mfma_f32_16x16x32_bf16 v[48:51], a[12:15], a[96:99], v[48:51]\n"
    "ds_read_b64_tr_b16 a[70:71], v231 offset:50688\n"
    "v_mfma_f32_16x16x32_bf16 v[52:55], a[12:15], a[100:103], v[52:55]\n"
    "s_mov_b32 m0, 75776\n"
    "v_mfma_f32_16x16x32_bf16 v[56:59], a[12:15], a[104:107], v[56:59]\n"
    "s_mov_b64 exec, s[64:65]\n"
    "buffer_load_dwordx4 v237, s[40:43], s56 offen sc1 lds\n"
    "s_mov_b64 exec, -1\n"
    "v_mfma_f32_16x16x32_bf16 v[60:63], a[12:15], a[108:111], v[60:63]\n"
    "s_add_u32 s56, s56, 131072\n"
    "v_mfma_f32_16x16x32_bf16 v[68:71], a[80:83], a[16:19], v[68:71]\n"
    "ds_read_b64_tr_b16 a[72:73], v230 offset:54272\n"
    "v_mfma_f32_16x16x32_bf16 v[72:75], a[84:87], a[16:19], v[72:75]\n"
    "ds_read_b64_tr_b16 a[74:75], v230 offset:54784\n"
    "v_mfma_f32_16x16x32_bf16 v[76:79], a[88:91], a[16:19], v[76:79]\n"
    "ds_read_b64_tr_b16 a[76:77], v231 offset:54272\n"
    "v_mfma_f32_16x16x32_bf16 v[80:83], a[92:95], a[16:19], v[80:83]\n"
    "ds_read_b64_tr_b16 a[78:79], v231 offset:54784\n"
    "v_mfma_f32_16x16x32_bf16 v[84:87], a[96:99], a[16:19], v[84:87]\n"
    "s_add_u32 m0, s62, 93184\n"
    "v_mfma_f32_16x16x32_bf16 v[88:91], a[100:103], a[16:19], v[88:91]\n"
    "buffer_load_dwordx4 v238, s[44:47], s57 offen lds\n"
    "v_mfma_f32_16x16x32_bf16 v[92:95], a[104:107], a[16:19], v[92:95]\n"
    "s_add_u32 s57, s57, 196608\n"
    "v_mfma_f32_16x16x32_bf16 v[96:99], a[108:111], a[16:19], v[96:99]\n"
    "v_mfma_f32_16x16x32_bf16 v[64:67], a[112:115], a[16:19], v[64:67]\n"
    "v_mfma_f32_16x16x32_bf16 v[100:103], a[80:83], a[20:23], v[100:103]\n"
    "v_mfma_f32_16x16x32_bf16 v[104:107], a[84:87], a[20:23], v[104:107]\n"
    "v_mfma_f32_16x16x32_bf16 v[108:111], a[88:91], a[20:23], v[108:111]\n"
    "s_add_u32 m0, s62, 97280\n"
    "v_mfma_f32_16x16x32_bf16 v[112:115], a[92:95], a[20:23], v[112:115]\n"
    "buffer_load_dwordx4 v238, s[44:47], s58 offen lds\n"
    "v_mfma_f32_16x16x32_bf16 v[116:119], a[96:99], a[20:23], v[116:119]\n"
    "s_add_u32 s58, s58, 196608\n"
    "v_mfma_f32_16x16x32_bf16 v[120:123], a[100:103], a[20:23], v[120:123]\n"
    "v_mfma_f32_16x16x32_bf16 v[124:127], a[104:107], a[20:23], v[124:127]\n"
    "v_mfma_f32_16x16x32_bf16 v[128:131], a[108:111], a[20:23], v[128:131]\n"
    "v_mfma_f32_16x16x32_bf16 a[152:155], a[24:27], a[80:83], a[152:155]\n"
    "v_mfma_f32_16x16x32_bf16 a[156:159], a[24:27], a[84:87], a[156:159]\n"
    "s_add_u32 m0, s62, 101376\n"
    "v_mfma_f32_16x16x32_bf16 a[160:163], a[24:27], a[88:91], a[160:163]\n"
    "buffer_load_dwordx4 v238, s[44:47], s59 offen lds\n"
    "v_mfma_f32_16x16x32_bf16 a[164:167], a[24:27], a[92:95], a[164:167]\n"
    "s_add_u32 s59, s59, 196608\n"
    "v_mfma_f32_16x16x32_bf16 a[168:171], a[24:27], a[96:99], a[168:171]\n"
    "v_mfma_f32_16x16x32_bf16 a[172:175], a[24:27], a[100:103], a[172:175]\n"
    "v_mfma_f32_16x16x32_bf16 a[176:179], a[24:27], a[104:107], a[176:179]\n"
    "v_mfma_f32_16x16x32_bf16 a[180:183], a[24:27], a[108:111], a[180:183]\n"
    "v_mfma_f32_16x16x32_bf16 a[184:187], a[28:31], a[80:83], a[184:187]\n"
    "s_add_u32 m0, s62, 105472\n"
    "v_mfma_f32_16x16x32_bf16 a[188:191], a[28:31], a[84:87], a[188:191]\n"
    "buffer_load_dwordx4 v239, s[48:51], s60 offen lds\n"
    "v_mfma_f32_16x16x32_bf16 a[192:195], a[28:31], a[88:91], a[192:195]\n"
    "v_mfma_f32_16x16x32_bf16 a[196:199], a[28:31], a[92:95], a[196:199]\n"
    "v_mfma_f32_16x16x32_bf16 a[200:203], a[28:31], a[96:99], a[200:203]\n"
    "v_mfma_f32_16x16x32_bf16 a[204:207], a[28:31], a[100:103], a[204:207]\n"
    "v_mfma_f32_16x16x32_bf16 a[208:211], a[28:31], a[104:107], a[208:211]\n"
    "v_mfma_f32_16x16x32_bf16 a[212:215], a[28:31], a[108:111], a[212:215]\n"
    "s_add_u32 m0, s62, 109568\n"
    "v_mfma_f32_16x16x32_bf16 v[136:139], a[32:35], a[80:83], v[136:139]\n"
    "buffer_load_dwordx4 v239, s[52:55], s60 offen lds\n"
    "v_mfma_f32_16x16x32_bf16 v[140:143], a[32:35], a[84:87], v[140:143]\n"
    "s_add_u32 s60, s60, 229376\n"
    "v_mfma_f32_16x16x32_bf16 v[144:147], a[32:35], a[88:91], v[144:147]\n"
    "v_mfma_f32_16x16x32_bf16 v[148:151], a[32:35], a[92:95], v[148:151]\n"
    "v_mfma_f32_16x16x32_bf16 v[152:155], a[32:35], a[96:99], v[152:155]\n"
    "v_mfma_f32_16x16x32_bf16 v[156:159], a[32:35], a[100:103], v[156:159]\n"
    "v_mfma_f32_16x16x32_bf16 v[160:163], a[32:35], a[104:107], v[160:163]\n"
    "s_sub_u32 s92, 6, s66\n"
    "s_min_u32 m0, s92, 3\n"
    "s_nop 0\n"
    "s_movrels_b32 s92, s84\n"
    "v_mfma_f32_16x16x32_bf16 v[164:167], a[32:35], a[108:111], v[164:167]\n"
    "buffer_load_dword v232, v245, s[80:83], s92 offen\n"
    "v_mfma_f32_16x16x32_bf16 v[168:171], a[36:39], a[80:83], v[168:171]\n"
    "v_mfma_f32_16x16x32_bf16 v[172:175], a[36:39], a[84:87], v[172:175]\n"
    "v_mfma_f32_16x16x32_bf16 v[176:179], a[36:39], a[88:91], v[176:179]\n"
    "v_mfma_f32_16x16x32_bf16 v[180:183], a[36:39], a[92:95], v[180:183]\n"
    "v_mfma_f32_16x16x32_bf16 v[184:187], a[36:39], a[96:99], v[184:187]\n"
    "v_mfma_f32_16x16x32_bf16 v[188:191], a[36:39], a[100:103], v[188:191]\n"
    "v_mfma_f32_16x16x32_bf16 v[192:195], a[36:39], a[104:107], v[192:195]\n"
    "v_mfma_f32_16x16x32_bf16 v[196:199], a[36:39], a[108:111], v[196:199]\n"
    "s_waitcnt vmcnt(23) lgkmcnt(0)\n"
    "s_barrier\n"
    "v_mfma_f32_16x16x32_bf16 v[200:203], a[40:43], a[116:119], v[200:203]\n"
    "ds_read_b128 a[80:83], v224 offset:1024\n"
    "v_mfma_f32_16x16x32_bf16 v[208:211], a[40:43], a[120:123], v[208:211]\n"
    "ds_read_b128 a[84:87], v224 offset:2048\n"
    "v_mfma_f32_16x16x32_bf16 v[216:219], a[40:43], a[124:127], v[216:219]\n"
    "s_add_u32 m0, s63, 114688\n"
    "v_mfma_f32_16x16x32_bf16 a[216:219], a[40:43], a[128:131], a[216:219]\n"
    "buffer_load_dwordx4 v236, s[40:43], s56 offen sc1 lds\n"
    "v_mfma_f32_16x16x32_bf16 a[224:227], a[40:43], a[132:135], a[224:227]\n"
    "ds_read_b128 a[88:91], v224 offset:3072\n"
    "v_mfma_f32_16x16x32_bf16 a[232:235], a[40:43], a[136:139], a[232:235]\n"
    "ds_read_b128 a[92:95], v224 offset:4096\n"
    "v_mfma_f32_16x16x32_bf16 a[240:243], a[40:43], a[140:143], a[240:243]\n"
    "ds_read_b128 a[96:99], v224 offset:5120\n"
    "v_mfma_f32_16x16x32_bf16 a[248:251], a[40:43], a[144:147], a[248:251]\n"
    "ds_read_b128 a[100:103], v224 offset:6144\n"
    "v_mfma_f32_16x16x32_bf16 v[204:207], a[44:47], a[116:119], v[204:207]\n"
    "ds_read_b128 a[104:107], v224 offset:7168\n"
    "v_mfma_f32_16x16x32_bf16 v[212:215], a[44:47], a[120:123], v[212:215]\n"
    "ds_read_b128 a[108:111], v224 offset:8192\n"
    "v_mfma_f32_16x16x32_bf16 v[220:223], a[44:47], a[124:127], v[220:223]\n"
    "ds_read_b128 a[112:115], v226 offset:0\n"
    "v_mfma_f32_16x16x32_bf16 a[220:223], a[44:47], a[128:131], a[220:223]\n"
    "buffer_load_dwordx4 v236, s[40:43], s56 offen offset:1024 sc1 lds\n"
    "v_mfma_f32_16x16x32_bf16 a[228:231], a[44:47], a[132:135], a[228:231]\n"
    "ds_read_b64_tr_b16 a[0:1], v228 offset:0\n"
    "v_mfma_f32_16x16x32_bf16 a[236:239], a[44:47], a[136:139], a[236:239]\n"
    "ds_read_b64_tr_b16 a[2:3], v228 offset:512\n"
    "v_mfma_f32_16x16x32_bf16 a[244:247], a[44:47], a[140:143], a[244:247]\n"
    "ds_read_b64_tr_b16 a[4:5], v229 offset:0\n"
    "v_mfma_f32_16x16x32_bf16 a[252:255], a[44:47], a[144:147], a[252:255]\n"
    "ds_read_b64_tr_b16 a[6:7], v229 offset:512\n"
    "v_mfma_f32_16x16x32_bf16 v[0:3], a[48:51], a[116:119], v[0:3]\n"
    "ds_read_b64_tr_b16 a[8:9], v228 offset:4096\n"
    "v_mfma_f32_16x16x32_bf16 v[4:7], a[48:51], a[120:123], v[4:7]\n"
    "ds_read_b64_tr_b16 a[10:11], v228 offset:4608\n"
    "v_mfma_f32_16x16x32_bf16 v[8:11], a[48:51], a[124:127], v[8:11]\n"
    "ds_read_b64_tr_b16 a[12:13], v229 offset:4096\n"
    "v_mfma_f32_16x16x32_bf16 v[12:15], a[48:51], a[128:131], v[12:15]\n"
    "buffer_load_dwordx4 v236, s[40:43], s56 offen offset:2048 sc1 lds\n"
    "v_mfma_f32_16x16x32_bf16 v[16:19], a[48:51], a[132:135], v[16:19]\n"
    "ds_read_b64_tr_b16 a[14:15], v229 offset:4608\n"
    "v_mfma_f32_16x16x32_bf16 v[20:23], a[48:51], a[136:139], v[20:23]\n"
    "ds_read_b64_tr_b16 a[16:17], v228 offset:8192\n"
    "v_mfma_f32_16x16x32_bf16 v[24:27], a[48:51], a[140:143], v[24:27]\n"
    "ds_read_b64_tr_b16 a[18:19], v228 offset:8704\n"
    "v_mfma_f32_16x16x32_bf16 v[28:31], a[48:51], a[144:147], v[28:31]\n"
    "ds_read_b64_tr_b16 a[20:21], v229 offset:8192\n"
    "v_mfma_f32_16x16x32_bf16 v[132:135], a[48:51], a[148:151], v[132:135]\n"
    "ds_read_b64_tr_b16 a[22:23], v229 offset:8704\n"
    "v_mfma_f32_16x16x32_bf16 v[32:35], a[52:55], a[116:119], v[32:35]\n"
    "ds_read_b64_tr_b16 a[24:25], v228 offset:12288\n"
    "v_mfma_f32_16x16x32_bf16 v[36:39], a[52:55], a[120:123], v[36:39]\n"
    "ds_read_b64_tr_b16 a[26:27], v228 offset:12800\n"
    "v_mfma_f32_16x16x32_bf16 v[40:43], a[52:55], a[124:127], v[40:43]\n"
    "buffer_load_dwordx4 v236, s[40:43], s56 offen offset:3072 sc1 lds\n"
    "v_mfma_f32_16x16x32_bf16 v[44:47], a[52:55], a[128:131], v[44:47]\n"
    "ds_read_b64_tr_b16 a[28:29], v229 offset:12288\n"
    "v_mfma_f32_16x16x32_bf16 v[48:51], a[52:55], a[132:135], v[48:51]\n"
    "ds_read_b64_tr_b16 a[30:31], v229 offset:12800\n"
    "v_mfma_f32_16x16x32_bf16 v[52:55], a[52:55], a[136:139], v[52:55]\n"
    "ds_read_b64_tr_b16 a[32:33], v228 offset:16384\n"
    "v_mfma_f32_16x16x32_bf16 v[56:59], a[52:55], a[140:143], v[56:59]\n"
    "ds_read_b64_tr_b16 a[34:35], v228 offset:16896\n"
    "v_mfma_f32_16x16x32_bf16 v[60:63], a[52:55], a[144:147], v[60:63]\n"
    "ds_read_b64_tr_b16 a[36:37], v229 offset:16384\n"
    "v_mfma_f32_16x16x32_bf16 v[68:71], a[116:119], a[56:59], v[68:71]\n"
    "ds_read_b64_tr_b16 a[38:39], v229 offset:16896\n"
    "v_mfma_f32_16x16x32_bf16 v[72:75], a[120:123], a[56:59], v[72:75]\n"
    "s_mov_b32 m0, 113664\n"
    "v_mfma_f32_16x16x32_bf16 v[76:79], a[124:127], a[56:59], v[76:79]\n"
    "s_mov_b64 exec, s[64:65]\n"
    "buffer_load_dwordx4 v237, s[40:43], s56 offen sc1 lds\n"
    "s_mov_b64 exec, -1\n"
    "v_mfma_f32_16x16x32_bf16 v[80:83], a[128:131], a[56:59], v[80:83]\n"
    "s_add_u32 s56, s56, 131072\n"
    "v_mfma_f32_16x16x32_bf16 v[84:87], a[132:135], a[56:59], v[84:87]\n"
    "v_mfma_f32_16x16x32_bf16 v[88:91], a[136:139], a[56:59], v[88:91]\n"
    "v_mfma_f32_16x16x32_bf16 v[92:95], a[140:143], a[56:59], v[92:95]\n"
    "v_mfma_f32_16x16x32_bf16 v[96:99], a[144:147], a[56:59], v[96:99]\n"
    "v_mfma_f32_16x16x32_bf16 v[64:67], a[148:151], a[56:59], v[64:67]\n"
    "v_mfma_f32_16x16x32_bf16 v[100:103], a[116:119], a[60:63], v[100:103]\n"
    "s_add_u32 m0, s62, 131072\n"
    "v_mfma_f32_16x16x32_bf16 v[104:107], a[120:123], a[60:63], v[104:107]\n"
    "buffer_load_dwordx4 v238, s[44:47], s57 offen lds\n"
    "v_mfma_f32_16x16x32_bf16 v[108:111], a[124:127], a[60:63], v[108:111]\n"
    "s_add_u32 s57, s57, 196608\n"
    "v_mfma_f32_16x16x32_bf16 v[112:115], a[128:131], a[60:63], v[112:115]\n"
    "v_mfma_f32_16x16x32_bf16 v[116:119], a[132:135], a[60:63], v[116:119]\n"
    "v_mfma_f32_16x16x32_bf16 v[120:123], a[136:139], a[60:63], v[120:123]\n"
    "v_mfma_f32_16x16x32_bf16 v[124:127], a[140:143], a[60:63], v[124:127]\n"
    "v_mfma_f32_16x16x32_bf16 v[128:131], a[144:147], a[60:63], v[128:131]\n"
    "v_mfma_f32_16x16x32_bf16 a[152:155], a[64:67], a[116:119], a[152:155]\n"
    "s_add_u32 m0, s62, 135168\n"
    "v_mfma_f32_16x16x32_bf16 a[156:159], a[64:67], a[120:123], a[156:159]\n"
    "buffer_load_dwordx4 v238, s[44:47], s58 offen lds\n"
    "v_mfma_f32_16x16x32_bf16 a[160:163], a[64:67], a[124:127], a[160:163]\n"
    "s_add_u32 s58, s58, 196608\n"
    "v_mfma_f32_16x16x32_bf16 a[164:167], a[64:67], a[128:131], a[164:167]\n"
    "v_mfma_f32_16x16x32_bf16 a[168:171], a[64:67], a[132:135], a[168:171]\n"
    "v_mfma_f32_16x16x32_bf16 a[172:175], a[64:67], a[136:139], a[172:175]\n"
    "v_mfma_f32_16x16x32_bf16 a[176:179], a[64:67], a[140:143], a[176:179]\n"
    "v_mfma_f32_16x16x32_bf16 a[180:183], a[64:67], a[144:147], a[180:183]\n"
    "v_mfma_f32_16x16x32_bf16 a[184:187], a[68:71], a[116:119], a[184:187]\n"
    "s_add_u32 m0, s62, 139264\n"
    "v_mfma_f32_16x16x32_bf16 a[188:191], a[68:71], a[120:123], a[188:191]\n"
    "buffer_load_dwordx4 v238, s[44:47], s59 offen lds\n"
    "v_mfma_f32_16x16x32_bf16 a[192:195], a[68:71], a[124:127], a[192:195]\n"
    "s_add_u32 s59, s59, 196608\n"
    "v_mfma_f32_16x16x32_bf16 a[196:199], a[68:71], a[128:131], a[196:199]\n"
    "v_mfma_f32_16x16x32_bf16 a[200:203], a[68:71], a[132:135], a[200:203]\n"
    "v_mfma_f32_16x16x32_bf16 a[204:207], a[68:71], a[136:139], a[204:207]\n"
    "v_mfma_f32_16x16x32_bf16 a[208:211], a[68:71], a[140:143], a[208:211]\n"
    "v_mfma_f32_16x16x32_bf16 a[212:215], a[68:71], a[144:147], a[212:215]\n"
    "v_mfma_f32_16x16x32_bf16 v[136:139], a[72:75], a[116:119], v[136:139]\n"
    "s_add_u32 m0, s62, 143360\n"
    "v_mfma_f32_16x16x32_bf16 v[140:143], a[72:75], a[120:123], v[140:143]\n"
    "buffer_load_dwordx4 v239, s[48:51], s60 offen lds\n"
    "v_mfma_f32_16x16x32_bf16 v[144:147], a[72:75], a[124:127], v[144:147]\n"
    "v_mfma_f32_16x16x32_bf16 v[148:151], a[72:75], a[128:131], v[148:151]\n"
    "v_mfma_f32_16x16x32_bf16 v[152:155], a[72:75], a[132:135], v[152:155]\n"
    "v_mfma_f32_16x16x32_bf16 v[156:159], a[72:75], a[136:139], v[156:159]\n"
    "v_mfma_f32_16x16x32_bf16 v[160:163], a[72:75], a[140:143], v[160:163]\n"
    "v_mfma_f32_16x16x32_bf16 v[164:167], a[72:75], a[144:147], v[164:167]\n"
    "v_mfma_f32_16x16x32_bf16 v[168:171], a[76:79], a[116:119], v[168:171]\n"
    "s_add_u32 m0, s62, 147456\n"
    "v_mfma_f32_16x16x32_bf16 v[172:175], a[76:79], a[120:123], v[172:175]\n"
    "buffer_load_dwordx4 v239, s[52:55], s60 offen lds\n"
    "v_mfma_f32_16x16x32_bf16 v[176:179], a[76:79], a[124:127], v[176:179]\n"
    "s_add_u32 s60, s60, 229376\n"
    "v_mfma_f32_16x16x32_bf16 v[180:183], a[76:79], a[128:131], v[180:183]\n"
    "v_mfma_f32_16x16x32_bf16 v[184:187], a[76:79], a[132:135], v[184:187]\n"
    "v_mfma_f32_16x16x32_bf16 v[188:191], a[76:79], a[136:139], v[188:191]\n"
    "v_mfma_f32_16x16x32_bf16 v[192:195], a[76:79], a[140:143], v[192:195]\n"
    "v_mfma_f32_16x16x32_bf16 v[196:199], a[76:79], a[144:147], v[196:199]\n"
    "s_sub_u32 s66, s66, 1\n"
    "s_cmp_lg_u32 s66, 0\n"
    "s_cbranch_scc1 LOOPm_%=\n"
    "s_waitcnt vmcnt(21) lgkmcnt(0)\n"
    "s_barrier\n"
    "s_lshl_b32 s56, s67, 3\n"
    "v_lshrrev_b32 v236, 7, v244\n"
    "v_add_u32 v236, s56, v236\n"
    "v_bfe_u32 v237, v236, 1, 2\n"
    "v_lshlrev_b32 v237, 1, v237\n"
    "v_bfe_u32 v238, v244, 4, 3\n"
    "v_xor_b32 v237, v237, v238\n"
    "v_lshlrev_b32 v237, 4, v237\n"
    "v_lshl_add_u32 v236, v236, 11, v237\n"
    "v_mfma_f32_16x16x32_bf16 v[200:203], a[0:3], a[80:83], v[200:203]\n"
    "ds_read_b128 a[116:119], v224 offset:38912\n"
    "v_mfma_f32_16x16x32_bf16 v[208:211], a[0:3], a[84:87], v[208:211]\n"
    "ds_read_b128 a[120:123], v224 offset:39936\n"
    "v_mfma_f32_16x16x32_bf16 v[216:219], a[0:3], a[88:91], v[216:219]\n"
    "s_add_u32 m0, s62, 0\n"
    "v_mfma_f32_16x16x32_bf16 a[216:219], a[0:3], a[92:95], a[216:219]\n"
    "buffer_load_dwordx4 v236, s[24:27], s28 offen lds\n"
    "v_mfma_f32_16x16x32_bf16 a[224:227], a[0:3], a[96:99], a[224:227]\n"
    "ds_read_b128 a[124:127], v224 offset:40960\n"
    "v_mfma_f32_16x16x32_bf16 a[232:235], a[0:3], a[100:103], a[232:235]\n"
    "ds_read_b128 a[128:131], v224 offset:41984\n"
    "v_mfma_f32_16x16x32_bf16 a[240:243], a[0:3], a[104:107], a[240:243]\n"
    "ds_read_b128 a[132:135], v224 offset:43008\n"
    "v_mfma_f32_16x16x32_bf16 a[248:251], a[0:3], a[108:111], a[248:251]\n"
    "ds_read_b128 a[136:139], v224 offset:44032\n"
    "v_mfma_f32_16x16x32_bf16 v[204:207], a[4:7], a[80:83], v[204:207]\n"
    "ds_read_b128 a[140:143], v224 offset:45056\n"
    "v_mfma_f32_16x16x32_bf16 v[212:215], a[4:7], a[84:87], v[212:215]\n"
    "ds_read_b128 a[144:147], v224 offset:46080\n"
    "v_mfma_f32_16x16x32_bf16 v[220:223], a[4:7], a[88:91], v[220:223]\n"
    "s_add_u32 m0, s62, 3968\n"
    "v_mfma_f32_16x16x32_bf16 a[220:223], a[4:7], a[92:95], a[220:223]\n"
    "buffer_load_dwordx4 v236, s[24:27], s28 offen offset:128 lds\n"
    "v_mfma_f32_16x16x32_bf16 a[228:231], a[4:7], a[96:99], a[228:231]\n"
    "s_add_u32 s28, s28, 131072\n"
    "v_mfma_f32_16x16x32_bf16 a[236:239], a[4:7], a[100:103], a[236:239]\n"
    "ds_read_b128 a[148:151], v226 offset:37888\n"
    "v_mfma_f32_16x16x32_bf16 a[244:247], a[4:7], a[104:107], a[244:247]\n"
    "ds_read_b64_tr_b16 a[40:41], v228 offset:37888\n"
    "v_mfma_f32_16x16x32_bf16 a[252:255], a[4:7], a[108:111], a[252:255]\n"
    "ds_read_b64_tr_b16 a[42:43], v228 offset:38400\n"
    "v_mfma_f32_16x16x32_bf16 v[0:3], a[8:11], a[80:83], v[0:3]\n"
    "ds_read_b64_tr_b16 a[44:45], v229 offset:37888\n"
    "v_mfma_f32_16x16x32_bf16 v[4:7], a[8:11], a[84:87], v[4:7]\n"
    "ds_read_b64_tr_b16 a[46:47], v229 offset:38400\n"
    "v_mfma_f32_16x16x32_bf16 v[8:11], a[8:11], a[88:91], v[8:11]\n"
    "s_add_u32 m0, s62, 8192\n"
    "v_mfma_f32_16x16x32_bf16 v[12:15], a[8:11], a[92:95], v[12:15]\n"
    "buffer_load_dwordx4 v236, s[24:27], s29 offen lds\n"
    "v_mfma_f32_16x16x32_bf16 v[16:19], a[8:11], a[96:99], v[16:19]\n"
    "ds_read_b64_tr_b16 a[48:49], v228 offset:41984\n"
    "v_mfma_f32_16x16x32_bf16 v[20:23], a[8:11], a[100:103], v[20:23]\n"
    "ds_read_b64_tr_b16 a[50:51], v228 offset:42496\n"
    "v_mfma_f32_16x16x32_bf16 v[24:27], a[8:11], a[104:107], v[24:27]\n"
    "ds_read_b64_tr_b16 a[52:53], v229 offset:41984\n"
    "v_mfma_f32_16x16x32_bf16 v[28:31], a[8:11], a[108:111], v[28:31]\n"
    "ds_read_b64_tr_b16 a[54:55], v229 offset:42496\n"
    "v_mfma_f32_16x16x32_bf16 v[132:135], a[8:11], a[112:115], v[132:135]\n"
    "ds_read_b64_tr_b16 a[56:57], v228 offset:46080\n"
    "v_mfma_f32_16x16x32_bf16 v[32:35], a[12:15], a[80:83], v[32:35]\n"
    "ds_read_b64_tr_b16 a[58:59], v228 offset:46592\n"
    "v_mfma_f32_16x16x32_bf16 v[36:39], a[12:15], a[84:87], v[36:39]\n"
    "s_add_u32 m0, s62, 12160\n"
    "v_mfma_f32_16x16x32_bf16 v[40:43], a[12:15], a[88:91], v[40:43]\n"
    "buffer_load_dwordx4 v236, s[24:27], s29 offen offset:128 lds\n"
    "v_mfma_f32_16x16x32_bf16 v[44:47], a[12:15], a[92:95], v[44:47]\n"
    "s_add_u32 s29, s29, 131072\n"
    "v_mfma_f32_16x16x32_bf16 v[48:51], a[12:15], a[96:99], v[48:51]\n"
    "ds_read_b64_tr_b16 a[60:61], v229 offset:46080\n"
    "v_mfma_f32_16x16x32_bf16 v[52:55], a[12:15], a[100:103], v[52:55]\n"
    "ds_read_b64_tr_b16 a[62:63], v229 offset:46592\n"
    "v_mfma_f32_16x16x32_bf16 v[56:59], a[12:15], a[104:107], v[56:59]\n"
    "ds_read_b64_tr_b16 a[64:65], v228 offset:50176\n"
    "v_mfma_f32_16x16x32_bf16 v[60:63], a[12:15], a[108:111], v[60:63]\n"
    "ds_read_b64_tr_b16 a[66:67], v228 offset:50688\n"
    "v_mfma_f32_16x16x32_bf16 v[68:71], a[80:83], a[16:19], v[68:71]\n"
    "ds_read_b64_tr_b16 a[68:69], v229 offset:50176\n"
    "v_mfma_f32_16x16x32_bf16 v[72:75], a[84:87], a[16:19], v[72:75]\n"
    "s_add_u32 m0, s62, 16384\n"
    "v_mfma_f32_16x16x32_bf16 v[76:79], a[88:91], a[16:19], v[76:79]\n"
    "buffer_load_dwordx4 v236, s[24:27], s28 offen lds\n"
    "v_mfma_f32_16x16x32_bf16 v[80:83], a[92:95], a[16:19], v[80:83]\n"
    "ds_read_b64_tr_b16 a[70:71], v229 offset:50688\n"
    "v_mfma_f32_16x16x32_bf16 v[84:87], a[96:99], a[16:19], v[84:87]\n"
    "ds_read_b64_tr_b16 a[72:73], v228 offset:54272\n"
    "v_mfma_f32_16x16x32_bf16 v[88:91], a[100:103], a[16:19], v[88:91]\n"
    "ds_read_b64_tr_b16 a[74:75], v228 offset:54784\n"
    "v_mfma_f32_16x16x32_bf16 v[92:95], a[104:107], a[16:19], v[92:95]\n"
    "ds_read_b64_tr_b16 a[76:77], v229 offset:54272\n"
    "v_mfma_f32_16x16x32_bf16 v[96:99], a[108:111], a[16:19], v[96:99]\n"
    "ds_read_b64_tr_b16 a[78:79], v229 offset:54784\n"
    "v_mfma_f32_16x16x32_bf16 v[64:67], a[112:115], a[16:19], v[64:67]\n"
    "v_mfma_f32_16x16x32_bf16 v[100:103], a[80:83], a[20:23], v[100:103]\n"
    "s_add_u32 m0, s62, 20352\n"
    "v_mfma_f32_16x16x32_bf16 v[104:107], a[84:87], a[20:23], v[104:107]\n"
    "buffer_load_dwordx4 v236, s[24:27], s28 offen offset:128 lds\n"
    "v_mfma_f32_16x16x32_bf16 v[108:111], a[88:91], a[20:23], v[108:111]\n"
    "s_add_u32 s28, s28, 131072\n"
    "v_mfma_f32_16x16x32_bf16 v[112:115], a[92:95], a[20:23], v[112:115]\n"
    "v_mfma_f32_16x16x32_bf16 v[116:119], a[96:99], a[20:23], v[116:119]\n"
    "v_mfma_f32_16x16x32_bf16 v[120:123], a[100:103], a[20:23], v[120:123]\n"
    "v_mfma_f32_16x16x32_bf16 v[124:127], a[104:107], a[20:23], v[124:127]\n"
    "v_mfma_f32_16x16x32_bf16 v[128:131], a[108:111], a[20:23], v[128:131]\n"
    "v_mfma_f32_16x16x32_bf16 a[152:155], a[24:27], a[80:83], a[152:155]\n"
    "s_add_u32 m0, s62, 24576\n"
    "v_mfma_f32_16x16x32_bf16 a[156:159], a[24:27], a[84:87], a[156:159]\n"
    "buffer_load_dwordx4 v236, s[24:27], s29 offen lds\n"
    "v_mfma_f32_16x16x32_bf16 a[160:163], a[24:27], a[88:91], a[160:163]\n"
    "v_mfma_f32_16x16x32_bf16 a[164:167], a[24:27], a[92:95], a[164:167]\n"
    "v_mfma_f32_16x16x32_bf16 a[168:171], a[24:27], a[96:99], a[168:171]\n"
    "v_mfma_f32_16x16x32_bf16 a[172:175], a[24:27], a[100:103], a[172:175]\n"
    "v_mfma_f32_16x16x32_bf16 a[176:179], a[24:27], a[104:107], a[176:179]\n"
    "v_mfma_f32_16x16x32_bf16 a[180:183], a[24:27], a[108:111], a[180:183]\n"
    "v_mfma_f32_16x16x32_bf16 a[184:187], a[28:31], a[80:83], a[184:187]\n"
    "s_add_u32 m0, s62, 28544\n"
    "v_mfma_f32_16x16x32_bf16 a[188:191], a[28:31], a[84:87], a[188:191]\n"
    "buffer_load_dwordx4 v236, s[24:27], s29 offen offset:128 lds\n"
    "v_mfma_f32_16x16x32_bf16 a[192:195], a[28:31], a[88:91], a[192:195]\n"
    "s_add_u32 s29, s29, 131072\n"
    "v_mfma_f32_16x16x32_bf16 a[196:199], a[28:31], a[92:95], a[196:199]\n"
    "v_mfma_f32_16x16x32_bf16 a[200:203], a[28:31], a[96:99], a[200:203]\n"
    "v_mfma_f32_16x16x32_bf16 a[204:207], a[28:31], a[100:103], a[204:207]\n"
    "v_mfma_f32_16x16x32_bf16 a[208:211], a[28:31], a[104:107], a[208:211]\n"
    "v_mfma_f32_16x16x32_bf16 a[212:215], a[28:31], a[108:111], a[212:215]\n"
    "v_mfma_f32_16x16x32_bf16 v[136:139], a[32:35], a[80:83], v[136:139]\n"
    "v_mfma_f32_16x16x32_bf16 v[140:143], a[32:35], a[84:87], v[140:143]\n"
    "v_mfma_f32_16x16x32_bf16 v[144:147], a[32:35], a[88:91], v[144:147]\n"
    "v_mfma_f32_16x16x32_bf16 v[148:151], a[32:35], a[92:95], v[148:151]\n"
    "v_mfma_f32_16x16x32_bf16 v[152:155], a[32:35], a[96:99], v[152:155]\n"
    "v_mfma_f32_16x16x32_bf16 v[156:159], a[32:35], a[100:103], v[156:159]\n"
    "v_mfma_f32_16x16x32_bf16 v[160:163], a[32:35], a[104:107], v[160:163]\n"
    "v_mfma_f32_16x16x32_bf16 v[164:167], a[32:35], a[108:111], v[164:167]\n"
    "v_mfma_f32_16x16x32_bf16 v[168:171], a[36:39], a[80:83], v[168:171]\n"
    "v_mfma_f32_16x16x32_bf16 v[172:175], a[36:39], a[84:87], v[172:175]\n"
    "v_mfma_f32_16x16x32_bf16 v[176:179], a[36:39], a[88:91], v[176:179]\n"
    "v_mfma_f32_16x16x32_bf16 v[180:183], a[36:39], a[92:95], v[180:183]\n"
    "v_mfma_f32_16x16x32_bf16 v[184:187], a[36:39], a[96:99], v[184:187]\n"
    "v_mfma_f32_16x16x32_bf16 v[188:191], a[36:39], a[100:103], v[188:191]\n"
    "v_mfma_f32_16x16x32_bf16 v[192:195], a[36:39], a[104:107], v[192:195]\n"
    "v_mfma_f32_16x16x32_bf16 v[196:199], a[36:39], a[108:111], v[196:199]\n"
    "s_waitcnt vmcnt(19) lgkmcnt(0)\n"
    "s_barrier\n"
    "v_mfma_f32_16x16x32_bf16 v[200:203], a[40:43], a[116:119], v[200:203]\n"
    "ds_read_b128 a[80:83], v225 offset:1024\n"
    "v_mfma_f32_16x16x32_bf16 v[208:211], a[40:43], a[120:123], v[208:211]\n"
    "ds_read_b128 a[84:87], v225 offset:2048\n"
    "v_mfma_f32_16x16x32_bf16 v[216:219], a[40:43], a[124:127], v[216:219]\n"
    "s_add_u32 m0, s62, 32768\n"
    "v_mfma_f32_16x16x32_bf16 a[216:219], a[40:43], a[128:131], a[216:219]\n"
    "buffer_load_dwordx4 v236, s[24:27], s28 offen lds\n"
    "v_mfma_f32_16x16x32_bf16 a[224:227], a[40:43], a[132:135], a[224:227]\n"
    "ds_read_b128 a[88:91], v225 offset:3072\n"
    "v_mfma_f32_16x16x32_bf16 a[232:235], a[40:43], a[136:139], a[232:235]\n"
    "ds_read_b128 a[92:95], v225 offset:4096\n"
    "v_mfma_f32_16x16x32_bf16 a[240:243], a[40:43], a[140:143], a[240:243]\n"
    "ds_read_b128 a[96:99], v225 offset:5120\n"
    "v_mfma_f32_16x16x32_bf16 a[248:251], a[40:43], a[144:147], a[248:251]\n"
    "ds_read_b128 a[100:103], v225 offset:6144\n"
    "v_mfma_f32_16x16x32_bf16 v[204:207], a[44:47], a[116:119], v[204:207]\n"
    "ds_read_b128 a[104:107], v225 offset:7168\n"
    "v_mfma_f32_16x16x32_bf16 v[212:215], a[44:47], a[120:123], v[212:215]\n"
    "ds_read_b128 a[108:111], v225 offset:8192\n"
    "v_mfma_f32_16x16x32_bf16 v[220:223], a[44:47], a[124:127], v[220:223]\n"
    "s_add_u32 m0, s62, 36736\n"
    "v_mfma_f32_16x16x32_bf16 a[220:223], a[44:47], a[128:131], a[220:223]\n"
    "buffer_load_dwordx4 v236, s[24:27], s28 offen offset:128 lds\n"
    "v_mfma_f32_16x16x32_bf16 a[228:231], a[44:47], a[132:135], a[228:231]\n"
    "s_add_u32 s28, s28, 131072\n"
    "v_mfma_f32_16x16x32_bf16 a[236:239], a[44:47], a[136:139], a[236:239]\n"
    "ds_read_b128 a[112:115], v227 offset:0\n"
    "v_mfma_f32_16x16x32_bf16 a[244:247], a[44:47], a[140:143], a[244:247]\n"
    "ds_read_b64_tr_b16 a[0:1], v230 offset:0\n"
    "v_mfma_f32_16x16x32_bf16 a[252:255], a[44:47], a[144:147], a[252:255]\n"
    "ds_read_b64_tr_b16 a[2:3], v230 offset:512\n"
    "v_mfma_f32_16x16x32_bf16 v[0:3], a[48:51], a[116:119], v[0:3]\n"
    "ds_read_b64_tr_b16 a[4:5], v231 offset:0\n"
    "v_mfma_f32_16x16x32_bf16 v[4:7], a[48:51], a[120:123], v[4:7]\n"
    "ds_read_b64_tr_b16 a[6:7], v231 offset:512\n"
    "v_mfma_f32_16x16x32_bf16 v[8:11], a[48:51], a[124:127], v[8:11]\n"
    "s_add_u32 m0, s62, 40960\n"
    "v_mfma_f32_16x16x32_bf16 v[12:15], a[48:51], a[128:131], v[12:15]\n"
    "buffer_load_dwordx4 v236, s[24:27], s29 offen lds\n"
    "v_mfma_f32_16x16x32_bf16 v[16:19], a[48:51], a[132:135], v[16:19]\n"
    "ds_read_b64_tr_b16 a[8:9], v230 offset:4096\n"
    "v_mfma_f32_16x16x32_bf16 v[20:23], a[48:51], a[136:139], v[20:23]\n"
    "ds_read_b64_tr_b16 a[10:11], v230 offset:4608\n"
    "v_mfma_f32_16x16x32_bf16 v[24:27], a[48:51], a[140:143], v[24:27]\n"
    "ds_read_b64_tr_b16 a[12:13], v231 offset:4096\n"
    "v_mfma_f32_16x16x32_bf16 v[28:31], a[48:51], a[144:147], v[28:31]\n"
    "ds_read_b64_tr_b16 a[14:15], v231 offset:4608\n"
    "v_mfma_f32_16x16x32_bf16 v[132:135], a[48:51], a[148:151], v[132:135]\n"
    "ds_read_b64_tr_b16 a[16:17], v230 offset:8192\n"
    "v_mfma_f32_16x16x32_bf16 v[32:35], a[52:55], a[116:119], v[32:35]\n"
    "ds_read_b64_tr_b16 a[18:19], v230 offset:8704\n"
    "v_mfma_f32_16x16x32_bf16 v[36:39], a[52:55], a[120:123], v[36:39]\n"
    "s_add_u32 m0, s62, 44928\n"
    "v_mfma_f32_16x16x32_bf16 v[40:43], a[52:55], a[124:127], v[40:43]\n"
    "buffer_load_dwordx4 v236, s[24:27], s29 offen offset:128 lds\n"
    "v_mfma_f32_16x16x32_bf16 v[44:47], a[52:55], a[128:131], v[44:47]\n"
    "s_add_u32 s29, s29, 131072\n"
    "v_mfma_f32_16x16x32_bf16 v[48:51], a[52:55], a[132:135], v[48:51]\n"
    "ds_read_b64_tr_b16 a[20:21], v231 offset:8192\n"
    "v_mfma_f32_16x16x32_bf16 v[52:55], a[52:55], a[136:139], v[52:55]\n"
    "ds_read_b64_tr_b16 a[22:23], v231 offset:8704\n"
    "v_mfma_f32_16x16x32_bf16 v[56:59], a[52:55], a[140:143], v[56:59]\n"
    "ds_read_b64_tr_b16 a[24:25], v230 offset:12288\n"
    "v_mfma_f32_16x16x32_bf16 v[60:63], a[52:55], a[144:147], v[60:63]\n"
    "ds_read_b64_tr_b16 a[26:27], v230 offset:12800\n"
    "v_mfma_f32_16x16x32_bf16 v[68:71], a[116:119], a[56:59], v[68:71]\n"
    "ds_read_b64_tr_b16 a[28:29], v231 offset:12288\n"
    "v_mfma_f32_16x16x32_bf16 v[72:75], a[120:123], a[56:59], v[72:75]\n"
    "s_add_u32 m0, s62, 49152\n"
    "v_mfma_f32_16x16x32_bf16 v[76:79], a[124:127], a[56:59], v[76:79]\n"
    "buffer_load_dwordx4 v236, s[24:27], s28 offen lds\n"
    "v_mfma_f32_16x16x32_bf16 v[80:83], a[128:131], a[56:59], v[80:83]\n"
    "ds_read_b64_tr_b16 a[30:31], v231 offset:12800\n"
    "v_mfma_f32_16x16x32_bf16 v[84:87], a[132:135], a[56:59], v[84:87]\n"
    "ds_read_b64_tr_b16 a[32:33], v230 offset:16384\n"
    "v_mfma_f32_16x16x32_bf16 v[88:91], a[136:139], a[56:59], v[88:91]\n"
    "ds_read_b64_tr_b16 a[34:35], v230 offset:16896\n"
    "v_mfma_f32_16x16x32_bf16 v[92:95], a[140:143], a[56:59], v[92:95]\n"
    "ds_read_b64_tr_b16 a[36:37], v231 offset:16384\n"
    "v_mfma_f32_16x16x32_bf16 v[96:99], a[144:147], a[56:59], v[96:99]\n"
    "ds_read_b64_tr_b16 a[38:39], v231 offset:16896\n"
    "v_mfma_f32_16x16x32_bf16 v[64:67], a[148:151], a[56:59], v[64:67]\n"
    "v_mfma_f32_16x16x32_bf16 v[100:103], a[116:119], a[60:63], v[100:103]\n"
    "s_add_u32 m0, s62, 53120\n"
    "v_mfma_f32_16x16x32_bf16 v[104:107], a[120:123], a[60:63], v[104:107]\n"
    "buffer_load_dwordx4 v236, s[24:27], s28 offen offset:128 lds\n"
    "v_mfma_f32_16x16x32_bf16 v[108:111], a[124:127], a[60:63], v[108:111]\n"
    "s_add_u32 s28, s28, 131072\n"
    "v_mfma_f32_16x16x32_bf16 v[112:115], a[128:131], a[60:63], v[112:115]\n"
    "v_mfma_f32_16x16x32_bf16 v[116:119], a[132:135], a[60:63], v[116:119]\n"
    "v_mfma_f32_16x16x32_bf16 v[120:123], a[136:139], a[60:63], v[120:123]\n"
    "v_mfma_f32_16x16x32_bf16 v[124:127], a[140:143], a[60:63], v[124:127]\n"
    "v_mfma_f32_16x16x32_bf16 v[128:131], a[144:147], a[60:63], v[128:131]\n"
    "v_mfma_f32_16x16x32_bf16 a[152:155], a[64:67], a[116:119], a[152:155]\n"
    "s_add_u32 m0, s62, 57344\n"
    "v_mfma_f32_16x16x32_bf16 a[156:159], a[64:67], a[120:123], a[156:159]\n"
    "buffer_load_dwordx4 v236, s[24:27], s29 offen lds\n"
    "v_mfma_f32_16x16x32_bf16 a[160:163], a[64:67], a[124:127], a[160:163]\n"
    "v_mfma_f32_16x16x32_bf16 a[164:167], a[64:67], a[128:131], a[164:167]\n"
    "v_mfma_f32_16x16x32_bf16 a[168:171], a[64:67], a[132:135], a[168:171]\n"
    "v_mfma_f32_16x16x32_bf16 a[172:175], a[64:67], a[136:139], a[172:175]\n"
    "v_mfma_f32_16x16x32_bf16 a[176:179], a[64:67], a[140:143], a[176:179]\n"
    "v_mfma_f32_16x16x32_bf16 a[180:183], a[64:67], a[144:147], a[180:183]\n"
    "v_mfma_f32_16x16x32_bf16 a[184:187], a[68:71], a[116:119], a[184:187]\n"
    "s_add_u32 m0, s62, 61312\n"
    "v_mfma_f32_16x16x32_bf16 a[188:191], a[68:71], a[120:123], a[188:191]\n"
    "buffer_load_dwordx4 v236, s[24:27], s29 offen offset:128 lds\n"
    "v_mfma_f32_16x16x32_bf16 a[192:195], a[68:71], a[124:127], a[192:195]\n"
    "s_add_u32 s29, s29, 131072\n"
    "v_mfma_f32_16x16x32_bf16 a[196:199], a[68:71], a[128:131], a[196:199]\n"
    "v_mfma_f32_16x16x32_bf16 a[200:203], a[68:71], a[132:135], a[200:203]\n"
    "v_mfma_f32_16x16x32_bf16 a[204:207], a[68:71], a[136:139], a[204:207]\n"
    "v_mfma_f32_16x16x32_bf16 a[208:211], a[68:71], a[140:143], a[208:211]\n"
    "v_mfma_f32_16x16x32_bf16 a[212:215], a[68:71], a[144:147], a[212:215]\n"
    "v_mfma_f32_16x16x32_bf16 v[136:139], a[72:75], a[116:119], v[136:139]\n"
    "v_mfma_f32_16x16x32_bf16 v[140:143], a[72:75], a[120:123], v[140:143]\n"
    "v_mfma_f32_16x16x32_bf16 v[144:147], a[72:75], a[124:127], v[144:147]\n"
    "v_mfma_f32_16x16x32_bf16 v[148:151], a[72:75], a[128:131], v[148:151]\n"
    "v_mfma_f32_16x16x32_bf16 v[152:155], a[72:75], a[132:135], v[152:155]\n"
    "v_mfma_f32_16x16x32_bf16 v[156:159], a[72:75], a[136:139], v[156:159]\n"
    "v_mfma_f32_16x16x32_bf16 v[160:163], a[72:75], a[140:143], v[160:163]\n"
    "v_mfma_f32_16x16x32_bf16 v[164:167], a[72:75], a[144:147], v[164:167]\n"
    "v_mfma_f32_16x16x32_bf16 v[168:171], a[76:79], a[116:119], v[168:171]\n"
    "v_mfma_f32_16x16x32_bf16 v[172:175], a[76:79], a[120:123], v[172:175]\n"
    "v_mfma_f32_16x16x32_bf16 v[176:179], a[76:79], a[124:127], v[176:179]\n"
    "v_mfma_f32_16x16x32_bf16 v[180:183], a[76:79], a[128:131], v[180:183]\n"
    "v_mfma_f32_16x16x32_bf16 v[184:187], a[76:79], a[132:135], v[184:187]\n"
    "v_mfma_f32_16x16x32_bf16 v[188:191], a[76:79], a[136:139], v[188:191]\n"
    "v_mfma_f32_16x16x32_bf16 v[192:195], a[76:79], a[140:143], v[192:195]\n"
    "v_mfma_f32_16x16x32_bf16 v[196:199], a[76:79], a[144:147], v[196:199]\n"
    "s_cmp_ge_u32 s67, 2\n"
    "s_cbranch_scc1 EPI1_%=\n"
    "s_waitcnt vmcnt(16) lgkmcnt(0)\n"
    "s_barrier\n"
    "v_mfma_f32_16x16x32_bf16 a[152:155], a[24:27], a[80:83], a[152:155]\n"
    "ds_read_b128 a[116:119], v225 offset:38912\n"
    "v_mfma_f32_16x16x32_bf16 a[156:159], a[24:27], a[84:87], a[156:159]\n"
    "ds_read_b128 a[120:123], v225 offset:39936\n"
    "v_mfma_f32_16x16x32_bf16 a[160:163], a[24:27], a[88:91], a[160:163]\n"
    "ds_read_b128 a[124:127], v225 offset:40960\n"
    "v_mfma_f32_16x16x32_bf16 a[164:167], a[24:27], a[92:95], a[164:167]\n"
    "ds_read_b128 a[128:131], v225 offset:41984\n"
    "v_mfma_f32_16x16x32_bf16 a[184:187], a[28:31], a[80:83], a[184:187]\n"
    "ds_read_b64_tr_b16 a[64:65], v230 offset:50176\n"
    "v_mfma_f32_16x16x32_bf16 a[188:191], a[28:31], a[84:87], a[188:191]\n"
    "ds_read_b64_tr_b16 a[66:67], v230 offset:50688\n"
    "v_mfma_f32_16x16x32_bf16 a[192:195], a[28:31], a[88:91], a[192:195]\n"
    "ds_read_b64_tr_b16 a[68:69], v231 offset:50176\n"
    "v_mfma_f32_16x16x32_bf16 a[196:199], a[28:31], a[92:95], a[196:199]\n"
    "ds_read_b64_tr_b16 a[70:71], v231 offset:50688\n"
    "v_mfma_f32_16x16x32_bf16 v[136:139], a[32:35], a[80:83], v[136:139]\n"
    "ds_read_b64_tr_b16 a[72:73], v230 offset:54272\n"
    "v_mfma_f32_16x16x32_bf16 v[140:143], a[32:35], a[84:87], v[140:143]\n"
    "ds_read_b64_tr_b16 a[74:75], v230 offset:54784\n"
    "v_mfma_f32_16x16x32_bf16 v[144:147], a[32:35], a[88:91], v[144:147]\n"
    "ds_read_b64_tr_b16 a[76:77], v231 offset:54272\n"
    "v_mfma_f32_16x16x32_bf16 v[148:151], a[32:35], a[92:95], v[148:151]\n"
    "ds_read_b64_tr_b16 a[78:79], v231 offset:54784\n"
    "v_mfma_f32_16x16x32_bf16 v[168:171], a[36:39], a[80:83], v[168:171]\n"
    "ds_read_b128 a[132:135], v225 offset:43008\n"
    "v_mfma_f32_16x16x32_bf16 v[172:175], a[36:39], a[84:87], v[172:175]\n"
    "s_add_u32 m0, s62, 65536\n"
    "ds_read_b128 a[136:139], v225 offset:44032\n"
    "v_mfma_f32_16x16x32_bf16 v[176:179], a[36:39], a[88:91], v[176:179]\n"
    "buffer_load_dwordx4 v236, s[24:27], s28 offen lds\n"
    "v_mfma_f32_16x16x32_bf16 v[180:183], a[36:39], a[92:95], v[180:183]\n"
    "ds_read_b128 a[140:143], v225 offset:45056\n"
    "s_waitcnt lgkmcnt(14)\n"
    "s_waitcnt lgkmcnt(9)\n"
    "v_mfma_f32_16x16x32_bf16 a[152:155], a[64:67], a[116:119], a[152:155]\n"
    "ds_read_b128 a[144:147], v225 offset:46080\n"
    "v_mfma_f32_16x16x32_bf16 a[156:159], a[64:67], a[120:123], a[156:159]\n"
    "s_add_u32 m0, s62, 69504\n"
    "ds_read_b128 a[148:151], v227 offset:37888\n"
    "v_mfma_f32_16x16x32_bf16 a[160:163], a[64:67], a[124:127], a[160:163]\n"
    "buffer_load_dwordx4 v236, s[24:27], s28 offen offset:128 lds\n"
    "v_mfma_f32_16x16x32_bf16 a[164:167], a[64:67], a[128:131], a[164:167]\n"
    "s_add_u32 s28, s28, 131072\n"
    "ds_read_b64_tr_b16 a[48:49], v230 offset:41984\n"
    "s_waitcnt lgkmcnt(10)\n"
    "v_mfma_f32_16x16x32_bf16 a[184:187], a[68:71], a[116:119], a[184:187]\n"
    "ds_read_b64_tr_b16 a[50:51], v230 offset:42496\n"
    "v_mfma_f32_16x16x32_bf16 a[188:191], a[68:71], a[120:123], a[188:191]\n"
    "s_add_u32 m0, s62, 73728\n"
    "ds_read_b64_tr_b16 a[52:53], v231 offset:41984\n"
    "v_mfma_f32_16x16x32_bf16 a[192:195], a[68:71], a[124:127], a[192:195]\n"
    "buffer_load_dwordx4 v236, s[24:27], s29 offen lds\n"
    "v_mfma_f32_16x16x32_bf16 a[196:199], a[68:71], a[128:131], a[196:199]\n"
    "ds_read_b64_tr_b16 a[54:55], v231 offset:42496\n"
    "s_waitcnt lgkmcnt(11)\n"
    "v_mfma_f32_16x16x32_bf16 v[136:139], a[72:75], a[116:119], v[136:139]\n"
    "ds_read_b64_tr_b16 a[56:57], v230 offset:46080\n"
    "v_mfma_f32_16x16x32_bf16 v[140:143], a[72:75], a[120:123], v[140:143]\n"
    "s_add_u32 m0, s62, 77696\n"
    "ds_read_b64_tr_b16 a[58:59], v230 offset:46592\n"
    "v_mfma_f32_16x16x32_bf16 v[144:147], a[72:75], a[124:127], v[144:147]\n"
    "buffer_load_dwordx4 v236, s[24:27], s29 offen offset:128 lds\n"
    "v_mfma_f32_16x16x32_bf16 v[148:151], a[72:75], a[128:131], v[148:151]\n"
    "s_add_u32 s29, s29, 131072\n"
    "ds_read_b64_tr_b16 a[60:61], v231 offset:46080\n"
    "s_waitcnt lgkmcnt(12)\n"
    "v_mfma_f32_16x16x32_bf16 v[168:171], a[76:79], a[116:119], v[168:171]\n"
    "ds_read_b64_tr_b16 a[62:63], v231 offset:46592\n"
    "v_mfma_f32_16x16x32_bf16 v[172:175], a[76:79], a[120:123], v[172:175]\n"
    "s_add_u32 m0, s62, 81920\n"
    "ds_read_b64_tr_b16 a[40:41], v230 offset:37888\n"
    "v_mfma_f32_16x16x32_bf16 v[176:179], a[76:79], a[124:127], v[176:179]\n"
    "buffer_load_dwordx4 v236, s[24:27], s28 offen lds\n"
    "v_mfma_f32_16x16x32_bf16 v[180:183], a[76:79], a[128:131], v[180:183]\n"
    "ds_read_b64_tr_b16 a[42:43], v230 offset:38400\n"
    "v_mfma_f32_16x16x32_bf16 a[168:171], a[24:27], a[96:99], a[168:171]\n"
    "ds_read_b64_tr_b16 a[44:45], v231 offset:37888\n"
    "v_mfma_f32_16x16x32_bf16 a[172:175], a[24:27], a[100:103], a[172:175]\n"
    "s_add_u32 m0, s62, 85888\n"
    "ds_read_b64_tr_b16 a[46:47], v231 offset:38400\n"
    "v_mfma_f32_16x16x32_bf16 a[176:179], a[24:27], a[104:107], a[176:179]\n"
    "buffer_load_dwordx4 v236, s[24:27], s28 offen offset:128 lds\n"
    "v_accvgpr_read_b32 v232, a152\n"
    "v_accvgpr_read_b32 v233, a153\n"
    "v_mfma_f32_16x16x32_bf16 a[180:183], a[24:27], a[108:111], a[180:183]\n"
    "s_add_u32 s28, s28, 131072\n"
    "v_accvgpr_read_b32 v234, a154\n"
    "v_accvgpr_read_b32 v235, a155\n"
    "v_mfma_f32_16x16x32_bf16 a[200:203], a[28:31], a[96:99], a[200:203]\n"
    "v_accvgpr_read_b32 v240, a184\n"
    "v_accvgpr_read_b32 v241, a185\n"
    "v_mfma_f32_16x16x32_bf16 a[204:207], a[28:31], a[100:103], a[204:207]\n"
    "s_add_u32 m0, s62, 90112\n"
    "v_accvgpr_read_b32 v242, a186\n"
    "v_accvgpr_read_b32 v243, a187\n"
    "v_mfma_f32_16x16x32_bf16 a[208:211], a[28:31], a[104:107], a[208:211]\n"
    "buffer_load_dwordx4 v236, s[24:27], s29 offen lds\n"
    "v_pk_mul_f32 v[136:137], v[136:137], v[232:233]\n"
    "v_pk_mul_f32 v[138:139], v[138:139], v[234:235]\n"
    "v_mfma_f32_16x16x32_bf16 a[212:215], a[28:31], a[108:111], a[212:215]\n"
    "v_pk_mul_f32 v[168:169], v[168:169], v[240:241]\n"
    "v_pk_mul_f32 v[170:171], v[170:171], v[242:243]\n"
    "v_mfma_f32_16x16x32_bf16 v[152:155], a[32:35], a[96:99], v[152:155]\n"
    "v_cvt_pk_bf16_f32 v136, v136, v137\n"
    "v_cvt_pk_bf16_f32 v137, v138, v139\n"
    "v_mfma_f32_16x16x32_bf16 v[156:159], a[32:35], a[100:103], v[156:159]\n"
    "s_add_u32 m0, s62, 94080\n"
    "v_cvt_pk_bf16_f32 v138, v168, v169\n"
    "v_cvt_pk_bf16_f32 v139, v170, v171\n"
    "v_mfma_f32_16x16x32_bf16 v[160:163], a[32:35], a[104:107], v[160:163]\n"
    "buffer_load_dwordx4 v236, s[24:27], s29 offen offset:128 lds\n"
    "buffer_store_dwordx4 v[136:139], v244, s[80:83], s86 offen sc1\n"
    "v_mfma_f32_16x16x32_bf16 v[164:167], a[32:35], a[108:111], v[164:167]\n"
    "s_add_u32 s29, s29, 131072\n"
    "v_accvgpr_read_b32 v232, a156\n"
    "v_accvgpr_read_b32 v233, a157\n"
    "v_mfma_f32_16x16x32_bf16 v[184:187], a[36:39], a[96:99], v[184:187]\n"
    "v_accvgpr_read_b32 v234, a158\n"
    "v_accvgpr_read_b32 v235, a159\n"
    "v_mfma_f32_16x16x32_bf16 v[188:191], a[36:39], a[100:103], v[188:191]\n"
    "v_accvgpr_read_b32 v240, a188\n"
    "v_accvgpr_read_b32 v241, a189\n"
    "v_mfma_f32_16x16x32_bf16 v[192:195], a[36:39], a[104:107], v[192:195]\n"
    "v_accvgpr_read_b32 v242, a190\n"
    "v_accvgpr_read_b32 v243, a191\n"
    "v_mfma_f32_16x16x32_bf16 v[196:199], a[36:39], a[108:111], v[196:199]\n"
    "v_pk_mul_f32 v[140:141], v[140:141], v[232:233]\n"
    "v_pk_mul_f32 v[142:143], v[142:143], v[234:235]\n"
    "s_waitcnt lgkmcnt(0)\n"
    "s_barrier\n"
    "v_mfma_f32_16x16x32_bf16 a[168:171], a[64:67], a[132:135], a[168:171]\n"
    "v_pk_mul_f32 v[172:173], v[172:173], v[240:241]\n"
    "v_pk_mul_f32 v[174:175], v[174:175], v[242:243]\n"
    "v_mfma_f32_16x16x32_bf16 a[172:175], a[64:67], a[136:139], a[172:175]\n"
    "s_add_u32 m0, s62, 98304\n"
    "v_cvt_pk_bf16_f32 v140, v140, v141\n"
    "v_cvt_pk_bf16_f32 v141, v142, v143\n"
    "v_mfma_f32_16x16x32_bf16 a[176:179], a[64:67], a[140:143], a[176:179]\n"
    "buffer_load_dwordx4 v236, s[24:27], s28 offen lds\n"
    "v_cvt_pk_bf16_f32 v142, v172, v173\n"
    "v_cvt_pk_bf16_f32 v143, v174, v175\n"
    "v_mfma_f32_16x16x32_bf16 a[180:183], a[64:67], a[144:147], a[180:183]\n"
    "s_add_u32 m0, s62, 102272\n"
    "buffer_store_dwordx4 v[140:143], v244, s[80:83], s86 offen offset:1024 sc1\n"
    "v_mfma_f32_16x16x32_bf16 a[200:203], a[68:71], a[132:135], a[200:203]\n"
    "buffer_load_dwordx4 v236, s[24:27], s28 offen offset:128 lds\n"
    "v_accvgpr_read_b32 v232, a160\n"
    "v_accvgpr_read_b32 v233, a161\n"
    "v_mfma_f32_16x16x32_bf16 a[204:207], a[68:71], a[136:139], a[204:207]\n"
    "s_add_u32 s28, s28, 131072\n"
    "s_add_u32 m0, s62, 106496\n"
    "v_accvgpr_read_b32 v234, a162\n"
    "v_accvgpr_read_b32 v235, a163\n"
    "v_mfma_f32_16x16x32_bf16 a[208:211], a[68:71], a[140:143], a[208:211]\n"
    "buffer_load_dwordx4 v236, s[24:27], s29 offen lds\n"
    "v_accvgpr_read_b32 v240, a192\n"
    "v_accvgpr_read_b32 v241, a193\n"
    "v_mfma_f32_16x16x32_bf16 a[212:215], a[68:71], a[144:147], a[212:215]\n"
    "s_add_u32 m0, s62, 110464\n"
    "v_accvgpr_read_b32 v242, a194\n"
    "v_accvgpr_read_b32 v243, a195\n"
    "v_mfma_f32_16x16x32_bf16 v[152:155], a[72:75], a[132:135], v[152:155]\n"
    "buffer_load_dwordx4 v236, s[24:27], s29 offen offset:128 lds\n"
    "v_pk_mul_f32 v[144:145], v[144:145], v[232:233]\n"
    "v_pk_mul_f32 v[146:147], v[146:147], v[234:235]\n"
    "v_mfma_f32_16x16x32_bf16 v[156:159], a[72:75], a[136:139], v[156:159]\n"
    "s_add_u32 s29, s29, 131072\n"
    "s_add_u32 m0, s62, 114688\n"
    "v_pk_mul_f32 v[176:177], v[176:177], v[240:241]\n"
    "v_pk_mul_f32 v[178:179], v[178:179], v[242:243]\n"
    "v_mfma_f32_16x16x32_bf16 v[160:163], a[72:75], a[140:143], v[160:163]\n"
    "buffer_load_dwordx4 v236, s[24:27], s28 offen lds\n"
    "v_cvt_pk_bf16_f32 v144, v144, v145\n"
    "v_cvt_pk_bf16_f32 v145, v146, v147\n"
    "v_mfma_f32_16x16x32_bf16 v[164:167], a[72:75], a[144:147], v[164:167]\n"
    "s_add_u32 m0, s62, 118656\n"
    "v_cvt_pk_bf16_f32 v146, v176, v177\n"
    "v_cvt_pk_bf16_f32 v147, v178, v179\n"
    "v_mfma_f32_16x16x32_bf16 v[184:187], a[76:79], a[132:135], v[184:187]\n"
    "buffer_load_dwordx4 v236, s[24:27], s28 offen offset:128 lds\n"
    "buffer_store_dwordx4 v[144:147], v244, s[80:83], s86 offen offset:2048 sc1\n"
    "v_mfma_f32_16x16x32_bf16 v[188:191], a[76:79], a[136:139], v[188:191]\n"
    "s_add_u32 s28, s28, 131072\n"
    "s_add_u32 m0, s62, 122880\n"
    "v_accvgpr_read_b32 v232, a164\n"
    "v_accvgpr_read_b32 v233, a165\n"
    "v_mfma_f32_16x16x32_bf16 v[192:195], a[76:79], a[140:143], v[192:195]\n"
    "buffer_load_dwordx4 v236, s[24:27], s29 offen lds\n"
    "v_accvgpr_read_b32 v234, a166\n"
    "v_accvgpr_read_b32 v235, a167\n"
    "v_mfma_f32_16x16x32_bf16 v[196:199], a[76:79], a[144:147], v[196:199]\n"
    "s_add_u32 m0, s62, 126848\n"
    "v_accvgpr_read_b32 v240, a196\n"
    "v_accvgpr_read_b32 v241, a197\n"
    "v_mfma_f32_16x16x32_bf16 v[0:3], a[8:11], a[80:83], v[0:3]\n"
    "buffer_load_dwordx4 v236, s[24:27], s29 offen offset:128 lds\n"
    "v_accvgpr_read_b32 v242, a198\n"
    "v_accvgpr_read_b32 v243, a199\n"
    "v_mfma_f32_16x16x32_bf16 v[4:7], a[8:11], a[84:87], v[4:7]\n"
    "s_add_u32 s29, s29, 131072\n"
    "s_add_u32 m0, s62, 131072\n"
    "v_pk_mul_f32 v[148:149], v[148:149], v[232:233]\n"
    "v_pk_mul_f32 v[150:151], v[150:151], v[234:235]\n"
    "v_mfma_f32_16x16x32_bf16 v[8:11], a[8:11], a[88:91], v[8:11]\n"
    "buffer_load_dwordx4 v236, s[24:27], s28 offen lds\n"
    "v_pk_mul_f32 v[180:181], v[180:181], v[240:241]\n"
    "v_pk_mul_f32 v[182:183], v[182:183], v[242:243]\n"
    "v_mfma_f32_16x16x32_bf16 v[12:15], a[8:11], a[92:95], v[12:15]\n"
    "s_add_u32 m0, s62, 135040\n"
    "v_cvt_pk_bf16_f32 v148, v148, v149\n"
    "v_cvt_pk_bf16_f32 v149, v150, v151\n"
    "v_mfma_f32_16x16x32_bf16 v[16:19], a[8:11], a[96:99], v[16:19]\n"
    "buffer_load_dwordx4 v236, s[24:27], s28 offen offset:128 lds\n"
    "v_cvt_pk_bf16_f32 v150, v180, v181\n"
    "v_cvt_pk_bf16_f32 v151, v182, v183\n"
    "v_mfma_f32_16x16x32_bf16 v[20:23], a[8:11], a[100:103], v[20:23]\n"
    "s_add_u32 s28, s28, 131072\n"
    "s_add_u32 m0, s62, 139264\n"
    "buffer_store_dwordx4 v[148:151], v244, s[80:83], s86 offen offset:3072 sc1\n"
    "v_mfma_f32_16x16x32_bf16 v[24:27], a[8:11], a[104:107], v[24:27]\n"
    "buffer_load_dwordx4 v236, s[24:27], s29 offen lds\n"
    "v_accvgpr_read_b32 v232, a168\n"
    "v_accvgpr_read_b32 v233, a169\n"
    "v_mfma_f32_16x16x32_bf16 v[28:31], a[8:11], a[108:111], v[28:31]\n"
    "s_add_u32 m0, s62, 143232\n"
    "v_accvgpr_read_b32 v234, a170\n"
    "v_accvgpr_read_b32 v235, a171\n"
    "v_mfma_f32_16x16x32_bf16 v[132:135], a[8:11], a[112:115], v[132:135]\n"
    "buffer_load_dwordx4 v236, s[24:27], s29 offen offset:128 lds\n"
    "v_accvgpr_read_b32 v240, a200\n"
    "v_accvgpr_read_b32 v241, a201\n"
    "v_mfma_f32_16x16x32_bf16 v[32:35], a[12:15], a[80:83], v[32:35]\n"
    "s_add_u32 s29, s29, 131072\n"
    "v_accvgpr_read_b32 v242, a202\n"
    "v_accvgpr_read_b32 v243, a203\n"
    "v_mfma_f32_16x16x32_bf16 v[36:39], a[12:15], a[84:87], v[36:39]\n"
    "v_pk_mul_f32 v[152:153], v[152:153], v[232:233]\n"
    "v_pk_mul_f32 v[154:155], v[154:155], v[234:235]\n"
    "v_mfma_f32_16x16x32_bf16 v[40:43], a[12:15], a[88:91], v[40:43]\n"
    "v_pk_mul_f32 v[184:185], v[184:185], v[240:241]\n"
    "v_pk_mul_f32 v[186:187], v[186:187], v[242:243]\n"
    "v_mfma_f32_16x16x32_bf16 v[44:47], a[12:15], a[92:95], v[44:47]\n"
    "v_cvt_pk_bf16_f32 v152, v152, v153\n"
    "v_cvt_pk_bf16_f32 v153, v154, v155\n"
    "v_mfma_f32_16x16x32_bf16 v[48:51], a[12:15], a[96:99], v[48:51]\n"
    "v_cvt_pk_bf16_f32 v154, v184, v185\n"
    "v_cvt_pk_bf16_f32 v155, v186, v187\n"
    "v_mfma_f32_16x16x32_bf16 v[52:55], a[12:15], a[100:103], v[52:55]\n"
    "buffer_store_dwordx4 v[152:155], v244, s[80:83], s87 offen sc1\n"
    "v_mfma_f32_16x16x32_bf16 v[56:59], a[12:15], a[104:107], v[56:59]\n"
    "v_accvgpr_read_b32 v232, a172\n"
    "v_accvgpr_read_b32 v233, a173\n"
    "v_mfma_f32_16x16x32_bf16 v[60:63], a[12:15], a[108:111], v[60:63]\n"
    "v_accvgpr_read_b32 v234, a174\n"
    "v_accvgpr_read_b32 v235, a175\n"
    "v_mfma_f32_16x16x32_bf16 v[68:71], a[80:83], a[16:19], v[68:71]\n"
    "v_accvgpr_read_b32 v240, a204\n"
    "v_accvgpr_read_b32 v241, a205\n"
    "v_mfma_f32_16x16x32_bf16 v[72:75], a[84:87], a[16:19], v[72:75]\n"
    "v_accvgpr_read_b32 v242, a206\n"
    "v_accvgpr_read_b32 v243, a207\n"
    "v_mfma_f32_16x16x32_bf16 v[76:79], a[88:91], a[16:19], v[76:79]\n"
    "v_pk_mul_f32 v[156:157], v[156:157], v[232:233]\n"
    "v_pk_mul_f32 v[158:159], v[158:159], v[234:235]\n"
    "v_mfma_f32_16x16x32_bf16 v[80:83], a[92:95], a[16:19], v[80:83]\n"
    "v_pk_mul_f32 v[188:189], v[188:189], v[240:241]\n"
    "v_pk_mul_f32 v[190:191], v[190:191], v[242:243]\n"
    "v_mfma_f32_16x16x32_bf16 v[84:87], a[96:99], a[16:19], v[84:87]\n"
    "v_cvt_pk_bf16_f32 v156, v156, v157\n"
    "v_cvt_pk_bf16_f32 v157, v158, v159\n"
    "v_mfma_f32_16x16x32_bf16 v[88:91], a[100:103], a[16:19], v[88:91]\n"
    "v_cvt_pk_bf16_f32 v158, v188, v189\n"
    "v_cvt_pk_bf16_f32 v159, v190, v191\n"
    "v_mfma_f32_16x16x32_bf16 v[92:95], a[104:107], a[16:19], v[92:95]\n"
    "buffer_store_dwordx4 v[156:159], v244, s[80:83], s87 offen offset:1024 sc1\n"
    "v_mfma_f32_16x16x32_bf16 v[96:99], a[108:111], a[16:19], v[96:99]\n"
    "v_accvgpr_read_b32 v232, a176\n"
    "v_accvgpr_read_b32 v233, a177\n"
    "v_mfma_f32_16x16x32_bf16 v[64:67], a[112:115], a[16:19], v[64:67]\n"
    "v_accvgpr_read_b32 v234, a178\n"
    "v_accvgpr_read_b32 v235, a179\n"
    "v_mfma_f32_16x16x32_bf16 v[100:103], a[80:83], a[20:23], v[100:103]\n"
    "v_accvgpr_read_b32 v240, a208\n"
    "v_accvgpr_read_b32 v241, a209\n"
    "v_mfma_f32_16x16x32_bf16 v[104:107], a[84:87], a[20:23], v[104:107]\n"
    "v_accvgpr_read_b32 v242, a210\n"
    "v_accvgpr_read_b32 v243, a211\n"
    "v_mfma_f32_16x16x32_bf16 v[108:111], a[88:91], a[20:23], v[108:111]\n"
    "v_pk_mul_f32 v[160:161], v[160:161], v[232:233]\n"
    "v_pk_mul_f32 v[162:163], v[162:163], v[234:235]\n"
    "v_mfma_f32_16x16x32_bf16 v[112:115], a[92:95], a[20:23], v[112:115]\n"
    "v_pk_mul_f32 v[192:193], v[192:193], v[240:241]\n"
    "v_pk_mul_f32 v[194:195], v[194:195], v[242:243]\n"
    "v_mfma_f32_16x16x32_bf16 v[116:119], a[96:99], a[20:23], v[116:119]\n"
    "v_cvt_pk_bf16_f32 v160, v160, v161\n"
    "v_cvt_pk_bf16_f32 v161, v162, v163\n"
    "v_mfma_f32_16x16x32_bf16 v[120:123], a[100:103], a[20:23], v[120:123]\n"
    "v_cvt_pk_bf16_f32 v162, v192, v193\n"
    "v_cvt_pk_bf16_f32 v163, v194, v195\n"
    "v_mfma_f32_16x16x32_bf16 v[124:127], a[104:107], a[20:23], v[124:127]\n"
    "buffer_store_dwordx4 v[160:163], v244, s[80:83], s87 offen offset:2048 sc1\n"
    "v_mfma_f32_16x16x32_bf16 v[128:131], a[108:111], a[20:23], v[128:131]\n"
    "v_accvgpr_read_b32 v232, a180\n"
    "v_accvgpr_read_b32 v233, a181\n"
    "v_mfma_f32_16x16x32_bf16 v[0:3], a[48:51], a[116:119], v[0:3]\n"
    "v_accvgpr_read_b32 v234, a182\n"
    "v_accvgpr_read_b32 v235, a183\n"
    "v_mfma_f32_16x16x32_bf16 v[4:7], a[48:51], a[120:123], v[4:7]\n"
    "v_accvgpr_read_b32 v240, a212\n"
    "v_accvgpr_read_b32 v241, a213\n"
    "v_mfma_f32_16x16x32_bf16 v[8:11], a[48:51], a[124:127], v[8:11]\n"
    "v_accvgpr_read_b32 v242, a214\n"
    "v_accvgpr_read_b32 v243, a215\n"
    "v_mfma_f32_16x16x32_bf16 v[12:15], a[48:51], a[128:131], v[12:15]\n"
    "v_pk_mul_f32 v[164:165], v[164:165], v[232:233]\n"
    "v_pk_mul_f32 v[166:167], v[166:167], v[234:235]\n"
    "v_mfma_f32_16x16x32_bf16 v[16:19], a[48:51], a[132:135], v[16:19]\n"
    "v_pk_mul_f32 v[196:197], v[196:197], v[240:241]\n"
    "v_pk_mul_f32 v[198:199], v[198:199], v[242:243]\n"
    "v_mfma_f32_16x16x32_bf16 v[20:23], a[48:51], a[136:139], v[20:23]\n"
    "v_cvt_pk_bf16_f32 v164, v164, v165\n"
    "v_cvt_pk_bf16_f32 v165, v166, v167\n"
    "v_mfma_f32_16x16x32_bf16 v[24:27], a[48:51], a[140:143], v[24:27]\n"
    "v_cvt_pk_bf16_f32 v166, v196, v197\n"
    "v_cvt_pk_bf16_f32 v167, v198, v199\n"
    "v_mfma_f32_16x16x32_bf16 v[28:31], a[48:51], a[144:147], v[28:31]\n"
    "buffer_store_dwordx4 v[164:167], v244, s[80:83], s87 offen offset:3072 sc1\n"
    "v_mfma_f32_16x16x32_bf16 v[132:135], a[48:51], a[148:151], v[132:135]\n"
    "v_mfma_f32_16x16x32_bf16 v[32:35], a[52:55], a[116:119], v[32:35]\n"
    "v_mfma_f32_16x16x32_bf16 v[36:39], a[52:55], a[120:123], v[36:39]\n"
    "v_mfma_f32_16x16x32_bf16 v[40:43], a[52:55], a[124:127], v[40:43]\n"
    "v_mfma_f32_16x16x32_bf16 v[44:47], a[52:55], a[128:131], v[44:47]\n"
    "v_mfma_f32_16x16x32_bf16 v[48:51], a[52:55], a[132:135], v[48:51]\n"
    "v_mfma_f32_16x16x32_bf16 v[52:55], a[52:55], a[136:139], v[52:55]\n"
    "v_mfma_f32_16x16x32_bf16 v[56:59], a[52:55], a[140:143], v[56:59]\n"
    "v_mfma_f32_16x16x32_bf16 v[60:63], a[52:55], a[144:147], v[60:63]\n"
    "v_mfma_f32_16x16x32_bf16 v[68:71], a[116:119], a[56:59], v[68:71]\n"
    "v_mfma_f32_16x16x32_bf16 v[72:75], a[120:123], a[56:59], v[72:75]\n"
    "v_mfma_f32_16x16x32_bf16 v[76:79], a[124:127], a[56:59], v[76:79]\n"
    "v_mfma_f32_16x16x32_bf16 v[80:83], a[128:131], a[56:59], v[80:83]\n"
    "v_mfma_f32_16x16x32_bf16 v[84:87], a[132:135], a[56:59], v[84:87]\n"
    "v_mfma_f32_16x16x32_bf16 v[88:91], a[136:139], a[56:59], v[88:91]\n"
    "v_mfma_f32_16x16x32_bf16 v[92:95], a[140:143], a[56:59], v[92:95]\n"
    "v_mfma_f32_16x16x32_bf16 v[96:99], a[144:147], a[56:59], v[96:99]\n"
    "v_mfma_f32_16x16x32_bf16 v[64:67], a[148:151], a[56:59], v[64:67]\n"
    "v_mfma_f32_16x16x32_bf16 v[100:103], a[116:119], a[60:63], v[100:103]\n"
    "v_mfma_f32_16x16x32_bf16 v[104:107], a[120:123], a[60:63], v[104:107]\n"
    "v_mfma_f32_16x16x32_bf16 v[108:111], a[124:127], a[60:63], v[108:111]\n"
    "v_mfma_f32_16x16x32_bf16 v[112:115], a[128:131], a[60:63], v[112:115]\n"
    "v_mfma_f32_16x16x32_bf16 v[116:119], a[132:135], a[60:63], v[116:119]\n"
    "v_mfma_f32_16x16x32_bf16 v[120:123], a[136:139], a[60:63], v[120:123]\n"
    "v_mfma_f32_16x16x32_bf16 v[124:127], a[140:143], a[60:63], v[124:127]\n"
    "v_mfma_f32_16x16x32_bf16 v[128:131], a[144:147], a[60:63], v[128:131]\n"
    "v_mfma_f32_16x16x32_bf16 v[200:203], a[0:3], a[80:83], v[200:203]\n"
    "v_mfma_f32_16x16x32_bf16 v[208:211], a[0:3], a[84:87], v[208:211]\n"
    "v_mfma_f32_16x16x32_bf16 v[216:219], a[0:3], a[88:91], v[216:219]\n"
    "v_mfma_f32_16x16x32_bf16 a[216:219], a[0:3], a[92:95], a[216:219]\n"
    "v_mfma_f32_16x16x32_bf16 a[224:227], a[0:3], a[96:99], a[224:227]\n"
    "v_mfma_f32_16x16x32_bf16 a[232:235], a[0:3], a[100:103], a[232:235]\n"
    "v_mfma_f32_16x16x32_bf16 a[240:243], a[0:3], a[104:107], a[240:243]\n"
    "v_mfma_f32_16x16x32_bf16 a[248:251], a[0:3], a[108:111], a[248:251]\n"
    "v_mfma_f32_16x16x32_bf16 v[204:207], a[4:7], a[80:83], v[204:207]\n"
    "v_mfma_f32_16x16x32_bf16 v[212:215], a[4:7], a[84:87], v[212:215]\n"
    "v_mfma_f32_16x16x32_bf16 v[220:223], a[4:7], a[88:91], v[220:223]\n"
    "v_mfma_f32_16x16x32_bf16 a[220:223], a[4:7], a[92:95], a[220:223]\n"
    "v_mfma_f32_16x16x32_bf16 a[228:231], a[4:7], a[96:99], a[228:231]\n"
    "v_mfma_f32_16x16x32_bf16 a[236:239], a[4:7], a[100:103], a[236:239]\n"
    "v_mfma_f32_16x16x32_bf16 a[244:247], a[4:7], a[104:107], a[244:247]\n"
    "v_mfma_f32_16x16x32_bf16 a[252:255], a[4:7], a[108:111], a[252:255]\n"
    "v_mfma_f32_16x16x32_bf16 v[200:203], a[40:43], a[116:119], v[200:203]\n"
    "v_mfma_f32_16x16x32_bf16 v[208:211], a[40:43], a[120:123], v[208:211]\n"
    "v_mfma_f32_16x16x32_bf16 v[216:219], a[40:43], a[124:127], v[216:219]\n"
    "v_mfma_f32_16x16x32_bf16 a[216:219], a[40:43], a[128:131], a[216:219]\n"
    "v_mfma_f32_16x16x32_bf16 a[224:227], a[40:43], a[132:135], a[224:227]\n"
    "v_mfma_f32_16x16x32_bf16 a[232:235], a[40:43], a[136:139], a[232:235]\n"
    "v_mfma_f32_16x16x32_bf16 a[240:243], a[40:43], a[140:143], a[240:243]\n"
    "v_mfma_f32_16x16x32_bf16 a[248:251], a[40:43], a[144:147], a[248:251]\n"
    "v_mfma_f32_16x16x32_bf16 v[204:207], a[44:47], a[116:119], v[204:207]\n"
    "v_mfma_f32_16x16x32_bf16 v[212:215], a[44:47], a[120:123], v[212:215]\n"
    "v_mfma_f32_16x16x32_bf16 v[220:223], a[44:47], a[124:127], v[220:223]\n"
    "v_mfma_f32_16x16x32_bf16 a[220:223], a[44:47], a[128:131], a[220:223]\n"
    "v_mfma_f32_16x16x32_bf16 a[228:231], a[44:47], a[132:135], a[228:231]\n"
    "v_mfma_f32_16x16x32_bf16 a[236:239], a[44:47], a[136:139], a[236:239]\n"
    "v_mfma_f32_16x16x32_bf16 a[244:247], a[44:47], a[140:143], a[244:247]\n"
    "v_mfma_f32_16x16x32_bf16 a[252:255], a[44:47], a[144:147], a[252:255]\n"
    "s_nop 7\n"
    "v_lshrrev_b32 v245, 1, v244\n"
    "v_add_u32 v245, s90, v245\n"
    "s_mov_b32 s92, 0x3e8293ee\n"
    "s_mov_b32 s93, 0xbe8293ee\n"
    "v_cvt_pk_bf16_f32 v28, v28, v29\n"
    "v_cvt_pk_bf16_f32 v29, v30, v31\n"
    "ds_write_b64 v245, v[28:29] offset:0\n"
    "v_cvt_pk_bf16_f32 v30, v60, v61\n"
    "v_cvt_pk_bf16_f32 v31, v62, v63\n"
    "v_cvt_pk_bf16_f32 v99, v98, v99\n"
    "ds_write_b64 v245, v[30:31] offset:512\n"
    "v_cvt_pk_bf16_f32 v98, v96, v97\n"
    "v_cvt_pk_bf16_f32 v131, v130, v131\n"
    "ds_write_b64 v245, v[98:99] offset:1024\n"
    "v_cvt_pk_bf16_f32 v130, v128, v129\n"
    "ds_write_b64 v245, v[130:131] offset:1536\n"
    "v_cvt_pk_bf16_f32 v96, v92, v93\n"
    "v_cvt_pk_bf16_f32 v4, v4, v5\n"
    "v_cvt_pk_bf16_f32 v0, v0, v1\n"
    "v_cvt_pk_bf16_f32 v208, v208, v209\n"
    "v_cvt_pk_bf16_f32 v93, v86, v87\n"
    "v_cvt_pk_bf16_f32 v5, v6, v7\n"
    "v_cvt_pk_bf16_f32 v1, v2, v3\n"
    "v_cvt_pk_bf16_f32 v209, v210, v211\n"
    "v_cvt_pk_bf16_f32 v97, v94, v95\n"
    "v_cvt_pk_bf16_f32 v12, v12, v13\n"
    "v_cvt_pk_bf16_f32 v8, v8, v9\n"
    "v_cvt_pk_bf16_f32 v6, v36, v37\n"
    "v_cvt_pk_bf16_f32 v7, v38, v39\n"
    "v_cvt_pk_bf16_f32 v86, v72, v73\n"
    "v_cvt_pk_bf16_f32 v2, v32, v33\n"
    "v_cvt_pk_bf16_f32 v3, v34, v35\n"
    "v_cvt_pk_bf16_f32 v210, v212, v213\n"
    "v_cvt_pk_bf16_f32 v211, v214, v215\n"
    "v_cvt_pk_bf16_f32 v216, v216, v217\n"
    "v_cvt_pk_bf16_f32 v94, v88, v89\n"
    "v_cvt_pk_bf16_f32 v13, v14, v15\n"
    "v_cvt_pk_bf16_f32 v9, v10, v11\n"
    "v_cvt_pk_bf16_f32 v87, v74, v75\n"
    "v_cvt_pk_bf16_f32 v217, v218, v219\n"
    "v_accvgpr_read_b32 v72, a216\n"
    "v_accvgpr_read_b32 v73, a217\n"
    "v_cvt_pk_bf16_f32 v95, v90, v91\n"
    "v_cvt_pk_bf16_f32 v14, v44, v45\n"
    "v_cvt_pk_bf16_f32 v15, v46, v47\n"
    "v_cvt_pk_bf16_f32 v10, v40, v41\n"
    "v_cvt_pk_bf16_f32 v11, v42, v43\n"
    "v_cvt_pk_bf16_f32 v89, v78, v79\n"
    "v_cvt_pk_bf16_f32 v88, v76, v77\n"
    "v_cvt_pk_bf16_f32 v218, v220, v221\n"
    "v_cvt_pk_bf16_f32 v219, v222, v223\n"
    "v_accvgpr_read_b32 v74, a218\n"
    "v_accvgpr_read_b32 v75, a219\n"
    "v_cvt_pk_bf16_f32 v72, v72, v73\n"
    "v_cvt_pk_bf16_f32 v91, v82, v83\n"
    "v_mfma_f32_16x16x32_bf16 v[40:43], v[0:3], v[208:211], v[246:249]\n"
    "v_accvgpr_read_b32 v76, a220\n"
    "v_accvgpr_read_b32 v77, a221\n"
    "v_mfma_f32_16x16x32_bf16 v[44:47], v[4:7], v[208:211], v[250:253]\n"
    "v_accvgpr_read_b32 v78, a222\n"
    "v_accvgpr_read_b32 v79, a223\n"
    "v_cvt_pk_bf16_f32 v73, v74, v75\n"
    "v_cvt_pk_bf16_f32 v128, v124, v125\n"
    "v_cvt_pk_bf16_f32 v83, v66, v67\n"
    "v_cvt_pk_bf16_f32 v82, v64, v65\n"
    "v_cvt_pk_bf16_f32 v74, v76, v77\n"
    "v_cvt_pk_bf16_f32 v75, v78, v79\n"
    "v_cvt_pk_bf16_f32 v125, v118, v119\n"
    "v_cvt_pk_bf16_f32 v124, v116, v117\n"
    "v_mfma_f32_16x16x32_bf16 v[60:63], v[4:7], v[216:219], v[246:249]\n"
    "v_cvt_pk_bf16_f32 v119, v106, v107\n"
    "v_cvt_pk_bf16_f32 v118, v104, v105\n"
    "v_mfma_f32_16x16x32_bf16 v[64:67], v[8:11], v[216:219], v[250:253]\n"
    "v_cvt_pk_bf16_f32 v117, v102, v103\n"
    "v_cvt_pk_bf16_f32 v116, v100, v101\n"
    "v_mfma_f32_16x16x32_bf16 v[100:103], v[8:11], v[72:75], v[246:249]\n"
    "v_max_f32 v40, v40, v41\n"
    "v_max_f32 v42, v42, v43\n"
    "v_mfma_f32_16x16x32_bf16 v[104:107], v[12:15], v[72:75], v[250:253]\n"
    "v_max_f32 v44, v44, v45\n"
    "v_max_f32 v46, v46, v47\n"
    "v_max3_f32 v41, v40, v42, v44\n"
    "v_max_f32 v41, v41, v46\n"
    "v_max_f32 v60, v60, v61\n"
    "v_max_f32 v62, v62, v63\n"
    "v_max_f32 v64, v64, v65\n"
    "v_mov_b32 v43, v41\n"
    "v_max_f32 v66, v66, v67\n"
    "v_max3_f32 v61, v60, v62, v64\n"
    "v_cvt_pk_bf16_f32 v132, v132, v133\n"
    "v_max_f32 v61, v61, v66\n"
    "v_max_f32 v100, v100, v101\n"
    "v_max_f32 v102, v102, v103\n"
    "v_max_f32 v104, v104, v105\n"
    "v_cvt_pk_bf16_f32 v133, v134, v135\n"
    "v_mov_b32 v63, v61\n"
    "v_max_f32 v106, v106, v107\n"
    "v_max3_f32 v101, v100, v102, v104\n"
    "v_permlane16_swap_b32 v41, v43\n"
    "v_max_f32 v101, v101, v106\n"
    "v_max_f32 v41, v41, v43\n"
    "v_mov_b32 v103, v101\n"
    "v_mov_b32 v43, v41\n"
    "v_permlane16_swap_b32 v61, v63\n"
    "v_max_f32 v61, v61, v63\n"
    "v_mov_b32 v63, v61\n"
    "v_permlane16_swap_b32 v101, v103\n"
    "v_permlane32_swap_b32 v41, v43\n"
    "v_max_f32 v101, v101, v103\n"
    "v_max_f32 v41, v41, v43\n"
    "v_mov_b32 v103, v101\n"
    "v_mul_f32 v45, s93, v41\n"
    "v_permlane32_swap_b32 v61, v63\n"
    "v_fma_f32 v40, v40, s92, v45\n"
    "v_fma_f32 v42, v42, s92, v45\n"
    "v_fma_f32 v44, v44, s92, v45\n"
    "v_fma_f32 v46, v46, s92, v45\n"
    "v_max_f32 v61, v61, v63\n"
    "v_exp_f32 v40, v40\n"
    "v_exp_f32 v42, v42\n"
    "v_exp_f32 v44, v44\n"
    "v_exp_f32 v46, v46\n"
    "v_mul_f32 v65, s93, v61\n"
    "v_permlane32_swap_b32 v101, v103\n"
    "v_fma_f32 v60, v60, s92, v65\n"
    "v_fma_f32 v62, v62, s92, v65\n"
    "v_fma_f32 v64, v64, s92, v65\n"
    "v_fma_f32 v66, v66, s92, v65\n"
    "v_max_f32 v101, v101, v103\n"
    "v_exp_f32 v60, v60\n"
    "v_exp_f32 v62, v62\n"
    "v_exp_f32 v64, v64\n"
    "v_exp_f32 v66, v66\n"
    "v_mul_f32 v105, s93, v101\n"
    "v_cvt_pk_bf16_f32 v41, v40, v42\n"
    "v_cvt_pk_bf16_f32 v43, v44, v46\n"
    "s_waitcnt lgkmcnt(0)\n"
    "s_barrier\n"
    "ds_read_b64 v[134:135], v245 offset:2048\n"
    "v_fma_f32 v100, v100, s92, v105\n"
    "v_fma_f32 v102, v102, s92, v105\n"
    "v_fma_f32 v104, v104, s92, v105\n"
    "v_fma_f32 v106, v106, s92, v105\n"
    "v_cvt_pk_bf16_f32 v16, v16, v17\n"
    "v_mov_b32 v240, 0x3f803f80\n"
    "v_mov_b32 v241, 0x3f803f80\n"
    "v_mov_b32 v242, 0x3f803f80\n"
    "v_mov_b32 v243, 0x3f803f80\n"
    "v_perm_b32 v208, v41, v41, v254\n"
    "v_perm_b32 v209, v41, v41, v255\n"
    "v_perm_b32 v210, v43, v43, v254\n"
    "v_perm_b32 v211, v43, v43, v255\n"
    "v_exp_f32 v100, v100\n"
    "v_exp_f32 v102, v102\n"
    "v_exp_f32 v104, v104\n"
    "v_exp_f32 v106, v106\n"
    "v_cvt_pk_bf16_f32 v17, v18, v19\n"
    "v_cvt_pk_bf16_f32 v61, v60, v62\n"
    "v_cvt_pk_bf16_f32 v63, v64, v66\n"
    "v_cvt_pk_bf16_f32 v18, v48, v49\n"
    "v_cvt_pk_bf16_f32 v19, v50, v51\n"
    "v_cvt_pk_bf16_f32 v92, v84, v85\n"
    "v_perm_b32 v216, v61, v61, v254\n"
    "v_perm_b32 v217, v61, v61, v255\n"
    "v_perm_b32 v218, v63, v63, v254\n"
    "v_perm_b32 v219, v63, v63, v255\n"
    "v_cvt_pk_bf16_f32 v85, v70, v71\n"
    "v_cvt_pk_bf16_f32 v84, v68, v69\n"
    "v_mfma_f32_16x16x32_bf16 v[48:51], v[240:243], v[208:211], 0\n"
    "v_cvt_pk_bf16_f32 v101, v100, v102\n"
    "v_cvt_pk_bf16_f32 v103, v104, v106\n"
    "v_cvt_pk_bf16_f32 v129, v126, v127\n"
    "v_perm_b32 v72, v101, v101, v254\n"
    "v_perm_b32 v73, v101, v101, v255\n"
    "v_perm_b32 v74, v103, v103, v254\n"
    "v_perm_b32 v75, v103, v103, v255\n"
    "v_cvt_pk_bf16_f32 v126, v120, v121\n"
    "v_mfma_f32_16x16x32_bf16 v[68:71], v[240:243], v[216:219], 0\n"
    "v_cvt_pk_bf16_f32 v121, v110, v111\n"
    "v_cvt_pk_bf16_f32 v120, v108, v109\n"
    "v_mfma_f32_16x16x32_bf16 v[40:43], v[84:87], v[208:211], 0\n"
    "v_cvt_pk_bf16_f32 v90, v80, v81\n"
    "v_cvt_pk_bf16_f32 v127, v122, v123\n"
    "v_mfma_f32_16x16x32_bf16 v[108:111], v[240:243], v[72:75], 0\n"
    "v_cvt_pk_bf16_f32 v123, v114, v115\n"
    "v_cvt_pk_bf16_f32 v122, v112, v113\n"
    "v_mfma_f32_16x16x32_bf16 v[44:47], v[116:119], v[208:211], 0\n"
    "v_rcp_f32 v48, v48\n"
    "v_mfma_f32_16x16x32_bf16 v[60:63], v[86:89], v[216:219], 0\n"
    "v_accvgpr_read_b32 v136, a224\n"
    "v_accvgpr_read_b32 v137, a225\n"
    "v_mfma_f32_16x16x32_bf16 v[64:67], v[118:121], v[216:219], 0\n"
    "v_rcp_f32 v68, v68\n"
    "v_mfma_f32_16x16x32_bf16 v[100:103], v[88:91], v[72:75], 0\n"
    "v_accvgpr_read_b32 v138, a226\n"
    "v_accvgpr_read_b32 v139, a227\n"
    "v_mfma_f32_16x16x32_bf16 v[104:107], v[120:123], v[72:75], 0\n"
    "v_cvt_pk_bf16_f32 v136, v136, v137\n"
    "v_pk_mul_f32 v[40:41], v[40:41], v[48:49] op_sel_hi:[1,0]\n"
    "v_accvgpr_read_b32 v140, a228\n"
    "v_accvgpr_read_b32 v141, a229\n"
    "v_accvgpr_read_b32 v142, a230\n"
    "v_accvgpr_read_b32 v143, a231\n"
    "v_cvt_pk_bf16_f32 v137, v138, v139\n"
    "v_accvgpr_read_b32 v156, a232\n"
    "v_accvgpr_read_b32 v157, a233\n"
    "v_cvt_pk_bf16_f32 v20, v20, v21\n"
    "v_pk_mul_f32 v[42:43], v[42:43], v[48:49] op_sel_hi:[1,0]\n"
    "v_cvt_pk_bf16_f32 v40, v40, v41\n"
    "v_rcp_f32 v108, v108\n"
    "v_cvt_pk_bf16_f32 v138, v140, v141\n"
    "v_cvt_pk_bf16_f32 v139, v142, v143\n"
    "v_accvgpr_read_b32 v158, a234\n"
    "v_accvgpr_read_b32 v159, a235\n"
    "v_cvt_pk_bf16_f32 v156, v156, v157\n"
    "v_cvt_pk_bf16_f32 v21, v22, v23\n"
    "v_pk_mul_f32 v[44:45], v[44:45], v[48:49] op_sel_hi:[1,0]\n"
    "v_pk_mul_f32 v[46:47], v[46:47], v[48:49] op_sel_hi:[1,0]\n"
    "v_cvt_pk_bf16_f32 v41, v42, v43\n"
    "v_pk_mul_f32 v[60:61], v[60:61], v[68:69] op_sel_hi:[1,0]\n"
    "v_accvgpr_read_b32 v160, a236\n"
    "v_accvgpr_read_b32 v161, a237\n"
    "v_accvgpr_read_b32 v162, a238\n"
    "v_accvgpr_read_b32 v163, a239\n"
    "v_cvt_pk_bf16_f32 v157, v158, v159\n"
    "v_accvgpr_read_b32 v32, a240\n"
    "v_accvgpr_read_b32 v33, a241\n"
    "v_cvt_pk_bf16_f32 v24, v24, v25\n"
    "v_cvt_pk_bf16_f32 v22, v52, v53\n"
    "v_cvt_pk_bf16_f32 v23, v54, v55\n"
    "v_cvt_pk_bf16_f32 v42, v44, v45\n"
    "v_cvt_pk_bf16_f32 v43, v46, v47\n"
    "v_pk_mul_f32 v[62:63], v[62:63], v[68:69] op_sel_hi:[1,0]\n"
    "v_cvt_pk_bf16_f32 v60, v60, v61\n"
    "v_cvt_pk_bf16_f32 v158, v160, v161\n"
    "v_cvt_pk_bf16_f32 v159, v162, v163\n"
    "v_accvgpr_read_b32 v34, a242\n"
    "v_accvgpr_read_b32 v35, a243\n"
    "v_cvt_pk_bf16_f32 v32, v32, v33\n"
    "v_cvt_pk_bf16_f32 v25, v26, v27\n"
    "buffer_store_dwordx4 v[40:43], v244, s[80:83], s84 offen offset:1024 sc1\n"
    "v_pk_mul_f32 v[64:65], v[64:65], v[68:69] op_sel_hi:[1,0]\n"
    "v_pk_mul_f32 v[66:67], v[66:67], v[68:69] op_sel_hi:[1,0]\n"
    "v_cvt_pk_bf16_f32 v61, v62, v63\n"
    "v_pk_mul_f32 v[100:101], v[100:101], v[108:109] op_sel_hi:[1,0]\n"
    "v_mfma_f32_16x16x32_bf16 v[144:147], v[12:15], v[136:139], v[246:249]\n"
    "v_accvgpr_read_b32 v36, a244\n"
    "v_accvgpr_read_b32 v37, a245\n"
    "v_mfma_f32_16x16x32_bf16 v[148:151], v[16:19], v[136:139], v[250:253]\n"
    "v_accvgpr_read_b32 v38, a246\n"
    "v_accvgpr_read_b32 v39, a247\n"
    "v_cvt_pk_bf16_f32 v33, v34, v35\n"
    "v_accvgpr_read_b32 v52, a248\n"
    "v_accvgpr_read_b32 v53, a249\n"
    "v_cvt_pk_bf16_f32 v26, v56, v57\n"
    "v_cvt_pk_bf16_f32 v27, v58, v59\n"
    "v_cvt_pk_bf16_f32 v62, v64, v65\n"
    "v_cvt_pk_bf16_f32 v63, v66, v67\n"
    "v_pk_mul_f32 v[102:103], v[102:103], v[108:109] op_sel_hi:[1,0]\n"
    "v_cvt_pk_bf16_f32 v100, v100, v101\n"
    "v_cvt_pk_bf16_f32 v34, v36, v37\n"
    "v_cvt_pk_bf16_f32 v35, v38, v39\n"
    "v_accvgpr_read_b32 v54, a250\n"
    "v_accvgpr_read_b32 v55, a251\n"
    "v_cvt_pk_bf16_f32 v52, v52, v53\n"
    "buffer_store_dwordx4 v[60:63], v244, s[80:83], s84 offen offset:2048 sc1\n"
    "v_pk_mul_f32 v[104:105], v[104:105], v[108:109] op_sel_hi:[1,0]\n"
    "v_pk_mul_f32 v[106:107], v[106:107], v[108:109] op_sel_hi:[1,0]\n"
    "v_cvt_pk_bf16_f32 v101, v102, v103\n"
    "v_mfma_f32_16x16x32_bf16 v[164:167], v[16:19], v[156:159], v[246:249]\n"
    "v_accvgpr_read_b32 v56, a252\n"
    "v_accvgpr_read_b32 v57, a253\n"
    "v_mfma_f32_16x16x32_bf16 v[168:171], v[20:23], v[156:159], v[250:253]\n"
    "v_accvgpr_read_b32 v58, a254\n"
    "v_accvgpr_read_b32 v59, a255\n"
    "v_cvt_pk_bf16_f32 v53, v54, v55\n"
    "v_cvt_pk_bf16_f32 v102, v104, v105\n"
    "v_cvt_pk_bf16_f32 v103, v106, v107\n"
    "v_cvt_pk_bf16_f32 v54, v56, v57\n"
    "v_cvt_pk_bf16_f32 v55, v58, v59\n"
    "v_cvt_pk_bf16_f32 v200, v200, v201\n"
    "buffer_store_dwordx4 v[100:103], v244, s[80:83], s84 offen offset:3072 sc1\n"
    "v_mfma_f32_16x16x32_bf16 v[40:43], v[20:23], v[32:35], v[246:249]\n"
    "ds_read_b64 v[114:115], v245 offset:2560\n"
    "v_cvt_pk_bf16_f32 v201, v202, v203\n"
    "v_mfma_f32_16x16x32_bf16 v[44:47], v[24:27], v[32:35], v[250:253]\n"
    "v_min_f32 v176, s91, v246\n"
    "v_min_f32 v177, s91, v247\n"
    "v_min_f32 v178, s91, v248\n"
    "v_min_f32 v179, s91, v249\n"
    "v_cvt_pk_bf16_f32 v202, v204, v205\n"
    "v_cvt_pk_bf16_f32 v203, v206, v207\n"
    "v_mfma_f32_16x16x32_bf16 v[60:63], v[24:27], v[52:55], v[246:249]\n"
    "v_max_f32 v144, v144, v145\n"
    "v_max_f32 v146, v146, v147\n"
    "v_mfma_f32_16x16x32_bf16 v[64:67], v[28:31], v[52:55], v[250:253]\n"
    "v_max_f32 v148, v148, v149\n"
    "v_max_f32 v150, v150, v151\n"
    "s_waitcnt lgkmcnt(1)\n"
    "v_mfma_f32_16x16x32_bf16 v[100:103], v[132:135], v[200:203], v[176:179]\n"
    "v_max3_f32 v145, v144, v146, v148\n"
    "v_max_f32 v145, v145, v150\n"
    "v_mfma_f32_16x16x32_bf16 v[104:107], v[0:3], v[200:203], v[250:253]\n"
    "v_max_f32 v164, v164, v165\n"
    "v_max_f32 v166, v166, v167\n"
    "v_max_f32 v168, v168, v169\n"
    "v_mov_b32 v147, v145\n"
    "v_max_f32 v170, v170, v171\n"
    "v_max3_f32 v165, v164, v166, v168\n"
    "v_max_f32 v165, v165, v170\n"
    "v_max_f32 v40, v40, v41\n"
    "v_max_f32 v42, v42, v43\n"
    "v_max_f32 v44, v44, v45\n"
    "v_mov_b32 v167, v165\n"
    "v_max_f32 v46, v46, v47\n"
    "v_max3_f32 v41, v40, v42, v44\n"
    "v_permlane16_swap_b32 v145, v147\n"
    "v_max_f32 v41, v41, v46\n"
    "v_max_f32 v60, v60, v61\n"
    "v_max_f32 v62, v62, v63\n"
    "v_max_f32 v64, v64, v65\n"
    "v_max_f32 v145, v145, v147\n"
    "v_mov_b32 v43, v41\n"
    "v_max_f32 v66, v66, v67\n"
    "v_max3_f32 v61, v60, v62, v64\n"
    "v_mov_b32 v147, v145\n"
    "v_permlane16_swap_b32 v165, v167\n"
    "v_max_f32 v61, v61, v66\n"
    "v_max_f32 v100, v100, v101\n"
    "v_max_f32 v102, v102, v103\n"
    "v_max_f32 v104, v104, v105\n"
    "v_max_f32 v165, v165, v167\n"
    "v_mov_b32 v63, v61\n"
    "v_max_f32 v106, v106, v107\n"
    "v_max3_f32 v101, v100, v102, v104\n"
    "v_mov_b32 v167, v165\n"
    "v_permlane16_swap_b32 v41, v43\n"
    "v_max_f32 v101, v101, v106\n"
    "v_permlane32_swap_b32 v145, v147\n"
    "v_max_f32 v41, v41, v43\n"
    "v_mov_b32 v103, v101\n"
    "v_max_f32 v145, v145, v147\n"
    "v_mov_b32 v43, v41\n"
    "v_permlane16_swap_b32 v61, v63\n"
    "v_mul_f32 v149, s93, v145\n"
    "v_permlane32_swap_b32 v165, v167\n"
    "v_max_f32 v61, v61, v63\n"
    "v_fma_f32 v144, v144, s92, v149\n"
    "v_fma_f32 v146, v146, s92, v149\n"
    "v_fma_f32 v148, v148, s92, v149\n"
    "v_fma_f32 v150, v150, s92, v149\n"
    "v_max_f32 v165, v165, v167\n"
    "v_mov_b32 v63, v61\n"
    "v_permlane16_swap_b32 v101, v103\n"
    "v_exp_f32 v144, v144\n"
    "v_exp_f32 v146, v146\n"
    "v_exp_f32 v148, v148\n"
    "v_exp_f32 v150, v150\n"
    "v_mul_f32 v169, s93, v165\n"
    "v_permlane32_swap_b32 v41, v43\n"
    "v_max_f32 v101, v101, v103\n"
    "v_fma_f32 v164, v164, s92, v169\n"
    "v_fma_f32 v166, v166, s92, v169\n"
    "v_fma_f32 v168, v168, s92, v169\n"
    "v_fma_f32 v170, v170, s92, v169\n"
    "v_max_f32 v41, v41, v43\n"
    "v_mov_b32 v103, v101\n"
    "v_exp_f32 v164, v164\n"
    "v_exp_f32 v166, v166\n"
    "v_exp_f32 v168, v168\n"
    "v_exp_f32 v170, v170\n"
    "v_mul_f32 v45, s93, v41\n"
    "v_permlane32_swap_b32 v61, v63\n"
    "v_cvt_pk_bf16_f32 v145, v144, v146\n"
    "v_cvt_pk_bf16_f32 v147, v148, v150\n"
    "v_fma_f32 v40, v40, s92, v45\n"
    "v_fma_f32 v42, v42, s92, v45\n"
    "v_fma_f32 v44, v44, s92, v45\n"
    "v_fma_f32 v46, v46, s92, v45\n"
    "v_max_f32 v61, v61, v63\n"
    "v_perm_b32 v136, v145, v145, v254\n"
    "v_perm_b32 v137, v145, v145, v255\n"
    "v_perm_b32 v138, v147, v147, v254\n"
    "v_perm_b32 v139, v147, v147, v255\n"
    "v_exp_f32 v40, v40\n"
    "v_exp_f32 v42, v42\n"
    "v_exp_f32 v44, v44\n"
    "v_exp_f32 v46, v46\n"
    "v_mul_f32 v65, s93, v61\n"
    "v_permlane32_swap_b32 v101, v103\n"
    "v_cvt_pk_bf16_f32 v165, v164, v166\n"
    "v_cvt_pk_bf16_f32 v167, v168, v170\n"
    "v_fma_f32 v60, v60, s92, v65\n"
    "v_fma_f32 v62, v62, s92, v65\n"
    "v_fma_f32 v64, v64, s92, v65\n"
    "v_fma_f32 v66, v66, s92, v65\n"
    "v_max_f32 v101, v101, v103\n"
    "v_perm_b32 v156, v165, v165, v254\n"
    "v_perm_b32 v157, v165, v165, v255\n"
    "v_perm_b32 v158, v167, v167, v254\n"
    "v_perm_b32 v159, v167, v167, v255\n"
    "v_exp_f32 v60, v60\n"
    "v_exp_f32 v62, v62\n"
    "v_exp_f32 v64, v64\n"
    "v_exp_f32 v66, v66\n"
    "v_mul_f32 v105, s93, v101\n"
    "v_mfma_f32_16x16x32_bf16 v[152:155], v[240:243], v[136:139], 0\n"
    "v_cvt_pk_bf16_f32 v41, v40, v42\n"
    "v_cvt_pk_bf16_f32 v43, v44, v46\n"
    "v_fma_f32 v100, v100, s92, v105\n"
    "v_fma_f32 v102, v102, s92, v105\n"
    "v_fma_f32 v104, v104, s92, v105\n"
    "v_fma_f32 v106, v106, s92, v105\n"
    "v_perm_b32 v32, v41, v41, v254\n"
    "v_perm_b32 v33, v41, v41, v255\n"
    "v_perm_b32 v34, v43, v43, v254\n"
    "v_perm_b32 v35, v43, v43, v255\n"
    "v_exp_f32 v100, v100\n"
    "v_exp_f32 v102, v102\n"
    "v_exp_f32 v104, v104\n"
    "v_exp_f32 v106, v106\n"
    "v_mfma_f32_16x16x32_bf16 v[172:175], v[240:243], v[156:159], 0\n"
    "v_cvt_pk_bf16_f32 v61, v60, v62\n"
    "v_cvt_pk_bf16_f32 v63, v64, v66\n"
    "v_mfma_f32_16x16x32_bf16 v[144:147], v[90:93], v[136:139], 0\n"
    "v_perm_b32 v52, v61, v61, v254\n"
    "v_perm_b32 v53, v61, v61, v255\n"
    "v_perm_b32 v54, v63, v63, v254\n"
    "v_perm_b32 v55, v63, v63, v255\n"
    "v_mfma_f32_16x16x32_bf16 v[48:51], v[240:243], v[32:35], 0\n"
    "v_cvt_pk_bf16_f32 v101, v100, v102\n"
    "v_cvt_pk_bf16_f32 v103, v104, v106\n"
    "v_mfma_f32_16x16x32_bf16 v[148:151], v[122:125], v[136:139], 0\n"
    "v_perm_b32 v200, v101, v101, v254\n"
    "v_perm_b32 v201, v101, v101, v255\n"
    "v_mfma_f32_16x16x32_bf16 v[164:167], v[92:95], v[156:159], 0\n"
    "v_perm_b32 v202, v103, v103, v254\n"
    "v_perm_b32 v203, v103, v103, v255\n"
    "v_mfma_f32_16x16x32_bf16 v[68:71], v[240:243], v[52:55], 0\n"
    "v_rcp_f32 v152, v152\n"
    "v_mfma_f32_16x16x32_bf16 v[168:171], v[124:127], v[156:159], 0\n"
    "v_rcp_f32 v172, v172\n"
    "v_mfma_f32_16x16x32_bf16 v[40:43], v[94:97], v[32:35], 0\n"
    "v_pk_mul_f32 v[144:145], v[144:145], v[152:153] op_sel_hi:[1,0]\n"
    "v_pk_mul_f32 v[146:147], v[146:147], v[152:153] op_sel_hi:[1,0]\n"
    "v_mfma_f32_16x16x32_bf16 v[108:111], v[240:243], v[200:203], 0\n"
    "v_cvt_pk_bf16_f32 v144, v144, v145\n"
    "v_rcp_f32 v48, v48\n"
    "v_mfma_f32_16x16x32_bf16 v[44:47], v[126:129], v[32:35], 0\n"
    "v_pk_mul_f32 v[148:149], v[148:149], v[152:153] op_sel_hi:[1,0]\n"
    "v_pk_mul_f32 v[150:151], v[150:151], v[152:153] op_sel_hi:[1,0]\n"
    "v_mfma_f32_16x16x32_bf16 v[60:63], v[96:99], v[52:55], 0\n"
    "v_cvt_pk_bf16_f32 v145, v146, v147\n"
    "v_pk_mul_f32 v[164:165], v[164:165], v[172:173] op_sel_hi:[1,0]\n"
    "v_mfma_f32_16x16x32_bf16 v[64:67], v[128:131], v[52:55], 0\n"
    "v_cvt_pk_bf16_f32 v146, v148, v149\n"
    "v_cvt_pk_bf16_f32 v147, v150, v151\n"
    "v_mfma_f32_16x16x32_bf16 v[100:103], v[82:85], v[200:203], 0\n"
    "v_pk_mul_f32 v[166:167], v[166:167], v[172:173] op_sel_hi:[1,0]\n"
    "v_cvt_pk_bf16_f32 v164, v164, v165\n"
    "s_waitcnt lgkmcnt(0)\n"
    "v_mfma_f32_16x16x32_bf16 v[104:107], v[114:117], v[200:203], 0\n"
    "v_rcp_f32 v68, v68\n"
    "buffer_store_dwordx4 v[144:147], v244, s[80:83], s85 offen sc1\n"
    "v_pk_mul_f32 v[168:169], v[168:169], v[172:173] op_sel_hi:[1,0]\n"
    "v_pk_mul_f32 v[170:171], v[170:171], v[172:173] op_sel_hi:[1,0]\n"
    "v_cvt_pk_bf16_f32 v165, v166, v167\n"
    "v_pk_mul_f32 v[40:41], v[40:41], v[48:49] op_sel_hi:[1,0]\n"
    "v_cvt_pk_bf16_f32 v166, v168, v169\n"
    "v_cvt_pk_bf16_f32 v167, v170, v171\n"
    "v_pk_mul_f32 v[42:43], v[42:43], v[48:49] op_sel_hi:[1,0]\n"
    "v_cvt_pk_bf16_f32 v40, v40, v41\n"
    "v_rcp_f32 v108, v108\n"
    "buffer_store_dwordx4 v[164:167], v244, s[80:83], s85 offen offset:1024 sc1\n"
    "v_pk_mul_f32 v[44:45], v[44:45], v[48:49] op_sel_hi:[1,0]\n"
    "v_pk_mul_f32 v[46:47], v[46:47], v[48:49] op_sel_hi:[1,0]\n"
    "v_cvt_pk_bf16_f32 v41, v42, v43\n"
    "v_pk_mul_f32 v[60:61], v[60:61], v[68:69] op_sel_hi:[1,0]\n"
    "v_cvt_pk_bf16_f32 v42, v44, v45\n"
    "v_cvt_pk_bf16_f32 v43, v46, v47\n"
    "v_pk_mul_f32 v[62:63], v[62:63], v[68:69] op_sel_hi:[1,0]\n"
    "v_cvt_pk_bf16_f32 v60, v60, v61\n"
    "buffer_store_dwordx4 v[40:43], v244, s[80:83], s85 offen offset:2048 sc1\n"
    "v_pk_mul_f32 v[64:65], v[64:65], v[68:69] op_sel_hi:[1,0]\n"
    "v_pk_mul_f32 v[66:67], v[66:67], v[68:69] op_sel_hi:[1,0]\n"
    "v_cvt_pk_bf16_f32 v61, v62, v63\n"
    "v_pk_mul_f32 v[100:101], v[100:101], v[108:109] op_sel_hi:[1,0]\n"
    "v_cvt_pk_bf16_f32 v62, v64, v65\n"
    "v_cvt_pk_bf16_f32 v63, v66, v67\n"
    "v_pk_mul_f32 v[102:103], v[102:103], v[108:109] op_sel_hi:[1,0]\n"
    "v_cvt_pk_bf16_f32 v100, v100, v101\n"
    "buffer_store_dwordx4 v[60:63], v244, s[80:83], s85 offen offset:3072 sc1\n"
    "v_pk_mul_f32 v[104:105], v[104:105], v[108:109] op_sel_hi:[1,0]\n"
    "v_pk_mul_f32 v[106:107], v[106:107], v[108:109] op_sel_hi:[1,0]\n"
    "v_cvt_pk_bf16_f32 v101, v102, v103\n"
    "v_cvt_pk_bf16_f32 v102, v104, v105\n"
    "v_cvt_pk_bf16_f32 v103, v106, v107\n"
    "buffer_store_dwordx4 v[100:103], v244, s[80:83], s84 offen sc1\n"
    "s_branch EPIEND_%=\n"
    "EPI1_%=:\n"
    "s_waitcnt vmcnt(16) lgkmcnt(0)\n"
    "s_barrier\n"
    "v_mfma_f32_16x16x32_bf16 a[152:155], a[24:27], a[80:83], a[152:155]\n"
    "ds_read_b128 a[116:119], v225 offset:38912\n"
    "v_mfma_f32_16x16x32_bf16 a[156:159], a[24:27], a[84:87], a[156:159]\n"
    "ds_read_b128 a[120:123], v225 offset:39936\n"
    "v_mfma_f32_16x16x32_bf16 a[160:163], a[24:27], a[88:91], a[160:163]\n"
    "ds_read_b128 a[124:127], v225 offset:40960\n"
    "v_mfma_f32_16x16x32_bf16 a[164:167], a[24:27], a[92:95], a[164:167]\n"
    "ds_read_b128 a[128:131], v225 offset:41984\n"
    "v_mfma_f32_16x16x32_bf16 a[184:187], a[28:31], a[80:83], a[184:187]\n"
    "ds_read_b64_tr_b16 a[64:65], v230 offset:50176\n"
    "v_mfma_f32_16x16x32_bf16 a[188:191], a[28:31], a[84:87], a[188:191]\n"
    "ds_read_b64_tr_b16 a[66:67], v230 offset:50688\n"
    "v_mfma_f32_16x16x32_bf16 a[192:195], a[28:31], a[88:91], a[192:195]\n"
    "ds_read_b64_tr_b16 a[68:69], v231 offset:50176\n"
    "v_mfma_f32_16x16x32_bf16 a[196:199], a[28:31], a[92:95], a[196:199]\n"
    "ds_read_b64_tr_b16 a[70:71], v231 offset:50688\n"
    "v_mfma_f32_16x16x32_bf16 v[136:139], a[32:35], a[80:83], v[136:139]\n"
    "ds_read_b64_tr_b16 a[72:73], v230 offset:54272\n"
    "v_mfma_f32_16x16x32_bf16 v[140:143], a[32:35], a[84:87], v[140:143]\n"
    "ds_read_b64_tr_b16 a[74:75], v230 offset:54784\n"
    "v_mfma_f32_16x16x32_bf16 v[144:147], a[32:35], a[88:91], v[144:147]\n"
    "ds_read_b64_tr_b16 a[76:77], v231 offset:54272\n"
    "v_mfma_f32_16x16x32_bf16 v[148:151], a[32:35], a[92:95], v[148:151]\n"
    "ds_read_b64_tr_b16 a[78:79], v231 offset:54784\n"
    "v_mfma_f32_16x16x32_bf16 v[168:171], a[36:39], a[80:83], v[168:171]\n"
    "ds_read_b128 a[132:135], v225 offset:43008\n"
    "v_mfma_f32_16x16x32_bf16 v[172:175], a[36:39], a[84:87], v[172:175]\n"
    "s_add_u32 m0, s62, 65536\n"
    "ds_read_b128 a[136:139], v225 offset:44032\n"
    "v_mfma_f32_16x16x32_bf16 v[176:179], a[36:39], a[88:91], v[176:179]\n"
    "buffer_load_dwordx4 v236, s[24:27], s28 offen lds\n"
    "v_mfma_f32_16x16x32_bf16 v[180:183], a[36:39], a[92:95], v[180:183]\n"
    "ds_read_b128 a[140:143], v225 offset:45056\n"
    "s_waitcnt lgkmcnt(14)\n"
    "s_waitcnt lgkmcnt(9)\n"
    "v_mfma_f32_16x16x32_bf16 a[152:155], a[64:67], a[116:119], a[152:155]\n"
    "ds_read_b128 a[144:147], v225 offset:46080\n"
    "v_mfma_f32_16x16x32_bf16 a[156:159], a[64:67], a[120:123], a[156:159]\n"
    "s_add_u32 m0, s62, 69504\n"
    "ds_read_b128 a[148:151], v227 offset:37888\n"
    "v_mfma_f32_16x16x32_bf16 a[160:163], a[64:67], a[124:127], a[160:163]\n"
    "buffer_load_dwordx4 v236, s[24:27], s28 offen offset:128 lds\n"
    "v_mfma_f32_16x16x32_bf16 a[164:167], a[64:67], a[128:131], a[164:167]\n"
    "s_add_u32 s28, s28, 131072\n"
    "ds_read_b64_tr_b16 a[48:49], v230 offset:41984\n"
    "s_waitcnt lgkmcnt(10)\n"
    "v_mfma_f32_16x16x32_bf16 a[184:187], a[68:71], a[116:119], a[184:187]\n"
    "ds_read_b64_tr_b16 a[50:51], v230 offset:42496\n"
    "v_mfma_f32_16x16x32_bf16 a[188:191], a[68:71], a[120:123], a[188:191]\n"
    "s_add_u32 m0, s62, 73728\n"
    "ds_read_b64_tr_b16 a[52:53], v231 offset:41984\n"
    "v_mfma_f32_16x16x32_bf16 a[192:195], a[68:71], a[124:127], a[192:195]\n"
    "buffer_load_dwordx4 v236, s[24:27], s29 offen lds\n"
    "v_mfma_f32_16x16x32_bf16 a[196:199], a[68:71], a[128:131], a[196:199]\n"
    "ds_read_b64_tr_b16 a[54:55], v231 offset:42496\n"
    "s_waitcnt lgkmcnt(11)\n"
    "v_mfma_f32_16x16x32_bf16 v[136:139], a[72:75], a[116:119], v[136:139]\n"
    "ds_read_b64_tr_b16 a[56:57], v230 offset:46080\n"
    "v_mfma_f32_16x16x32_bf16 v[140:143], a[72:75], a[120:123], v[140:143]\n"
    "s_add_u32 m0, s62, 77696\n"
    "ds_read_b64_tr_b16 a[58:59], v230 offset:46592\n"
    "v_mfma_f32_16x16x32_bf16 v[144:147], a[72:75], a[124:127], v[144:147]\n"
    "buffer_load_dwordx4 v236, s[24:27], s29 offen offset:128 lds\n"
    "v_mfma_f32_16x16x32_bf16 v[148:151], a[72:75], a[128:131], v[148:151]\n"
    "s_add_u32 s29, s29, 131072\n"
    "ds_read_b64_tr_b16 a[60:61], v231 offset:46080\n"
    "s_waitcnt lgkmcnt(12)\n"
    "v_mfma_f32_16x16x32_bf16 v[168:171], a[76:79], a[116:119], v[168:171]\n"
    "ds_read_b64_tr_b16 a[62:63], v231 offset:46592\n"
    "v_mfma_f32_16x16x32_bf16 v[172:175], a[76:79], a[120:123], v[172:175]\n"
    "s_add_u32 m0, s62, 81920\n"
    "ds_read_b64_tr_b16 a[40:41], v230 offset:37888\n"
    "v_mfma_f32_16x16x32_bf16 v[176:179], a[76:79], a[124:127], v[176:179]\n"
    "buffer_load_dwordx4 v236, s[24:27], s28 offen lds\n"
    "v_mfma_f32_16x16x32_bf16 v[180:183], a[76:79], a[128:131], v[180:183]\n"
    "ds_read_b64_tr_b16 a[42:43], v230 offset:38400\n"
    "v_mfma_f32_16x16x32_bf16 a[168:171], a[24:27], a[96:99], a[168:171]\n"
    "ds_read_b64_tr_b16 a[44:45], v231 offset:37888\n"
    "v_mfma_f32_16x16x32_bf16 a[172:175], a[24:27], a[100:103], a[172:175]\n"
    "s_add_u32 m0, s62, 85888\n"
    "ds_read_b64_tr_b16 a[46:47], v231 offset:38400\n"
    "v_mfma_f32_16x16x32_bf16 a[176:179], a[24:27], a[104:107], a[176:179]\n"
    "buffer_load_dwordx4 v236, s[24:27], s28 offen offset:128 lds\n"
    "v_accvgpr_read_b32 v232, a152\n"
    "v_accvgpr_read_b32 v233, a153\n"
    "v_mfma_f32_16x16x32_bf16 a[180:183], a[24:27], a[108:111], a[180:183]\n"
    "s_add_u32 s28, s28, 131072\n"
    "v_accvgpr_read_b32 v234, a154\n"
    "v_accvgpr_read_b32 v235, a155\n"
    "v_mfma_f32_16x16x32_bf16 a[200:203], a[28:31], a[96:99], a[200:203]\n"
    "v_accvgpr_read_b32 v240, a184\n"
    "v_accvgpr_read_b32 v241, a185\n"
    "v_mfma_f32_16x16x32_bf16 a[204:207], a[28:31], a[100:103], a[204:207]\n"
    "s_add_u32 m0, s62, 90112\n"
    "v_accvgpr_read_b32 v242, a186\n"
    "v_accvgpr_read_b32 v243, a187\n"
    "v_mfma_f32_16x16x32_bf16 a[208:211], a[28:31], a[104:107], a[208:211]\n"
    "buffer_load_dwordx4 v236, s[24:27], s29 offen lds\n"
    "v_pk_mul_f32 v[136:137], v[136:137], v[232:233]\n"
    "v_pk_mul_f32 v[138:139], v[138:139], v[234:235]\n"
    "v_mfma_f32_16x16x32_bf16 a[212:215], a[28:31], a[108:111], a[212:215]\n"
    "v_pk_mul_f32 v[168:169], v[168:169], v[240:241]\n"
    "v_pk_mul_f32 v[170:171], v[170:171], v[242:243]\n"
    "v_mfma_f32_16x16x32_bf16 v[152:155], a[32:35], a[96:99], v[152:155]\n"
    "v_cvt_pk_bf16_f32 v168, v168, v169\n"
    "v_cvt_pk_bf16_f32 v169, v170, v171\n"
    "v_mfma_f32_16x16x32_bf16 v[156:159], a[32:35], a[100:103], v[156:159]\n"
    "s_add_u32 m0, s62, 94080\n"
    "v_cvt_pk_bf16_f32 v170, v136, v137\n"
    "v_cvt_pk_bf16_f32 v171, v138, v139\n"
    "v_mfma_f32_16x16x32_bf16 v[160:163], a[32:35], a[104:107], v[160:163]\n"
    "buffer_load_dwordx4 v236, s[24:27], s29 offen offset:128 lds\n"
    "buffer_store_dwordx4 v[168:171], v244, s[80:83], s86 offen sc1\n"
    "v_mfma_f32_16x16x32_bf16 v[164:167], a[32:35], a[108:111], v[164:167]\n"
    "s_add_u32 s29, s29, 131072\n"
    "v_accvgpr_read_b32 v232, a156\n"
    "v_accvgpr_read_b32 v233, a157\n"
    "v_mfma_f32_16x16x32_bf16 v[184:187], a[36:39], a[96:99], v[184:187]\n"
    "v_accvgpr_read_b32 v234, a158\n"
    "v_accvgpr_read_b32 v235, a159\n"
    "v_mfma_f32_16x16x32_bf16 v[188:191], a[36:39], a[100:103], v[188:191]\n"
    "v_accvgpr_read_b32 v240, a188\n"
    "v_accvgpr_read_b32 v241, a189\n"
    "v_mfma_f32_16x16x32_bf16 v[192:195], a[36:39], a[104:107], v[192:195]\n"
    "v_accvgpr_read_b32 v242, a190\n"
    "v_accvgpr_read_b32 v243, a191\n"
    "v_mfma_f32_16x16x32_bf16 v[196:199], a[36:39], a[108:111], v[196:199]\n"
    "v_pk_mul_f32 v[140:141], v[140:141], v[232:233]\n"
    "v_pk_mul_f32 v[142:143], v[142:143], v[234:235]\n"
    "s_waitcnt lgkmcnt(0)\n"
    "s_barrier\n"
    "v_mfma_f32_16x16x32_bf16 a[168:171], a[64:67], a[132:135], a[168:171]\n"
    "v_pk_mul_f32 v[172:173], v[172:173], v[240:241]\n"
    "v_pk_mul_f32 v[174:175], v[174:175], v[242:243]\n"
    "v_mfma_f32_16x16x32_bf16 a[172:175], a[64:67], a[136:139], a[172:175]\n"
    "s_add_u32 m0, s62, 98304\n"
    "v_cvt_pk_bf16_f32 v172, v172, v173\n"
    "v_cvt_pk_bf16_f32 v173, v174, v175\n"
    "v_mfma_f32_16x16x32_bf16 a[176:179], a[64:67], a[140:143], a[176:179]\n"
    "buffer_load_dwordx4 v236, s[24:27], s28 offen lds\n"
    "v_cvt_pk_bf16_f32 v174, v140, v141\n"
    "v_cvt_pk_bf16_f32 v175, v142, v143\n"
    "v_mfma_f32_16x16x32_bf16 a[180:183], a[64:67], a[144:147], a[180:183]\n"
    "s_add_u32 m0, s62, 102272\n"
    "buffer_store_dwordx4 v[172:175], v244, s[80:83], s86 offen offset:1024 sc1\n"
    "v_mfma_f32_16x16x32_bf16 a[200:203], a[68:71], a[132:135], a[200:203]\n"
    "buffer_load_dwordx4 v236, s[24:27], s28 offen offset:128 lds\n"
    "v_accvgpr_read_b32 v232, a160\n"
    "v_accvgpr_read_b32 v233, a161\n"
    "v_mfma_f32_16x16x32_bf16 a[204:207], a[68:71], a[136:139], a[204:207]\n"
    "s_add_u32 s28, s28, 131072\n"
    "s_add_u32 m0, s62, 106496\n"
    "v_accvgpr_read_b32 v234, a162\n"
    "v_accvgpr_read_b32 v235, a163\n"
    "v_mfma_f32_16x16x32_bf16 a[208:211], a[68:71], a[140:143], a[208:211]\n"
    "buffer_load_dwordx4 v236, s[24:27], s29 offen lds\n"
    "v_accvgpr_read_b32 v240, a192\n"
    "v_accvgpr_read_b32 v241, a193\n"
    "v_mfma_f32_16x16x32_bf16 a[212:215], a[68:71], a[144:147], a[212:215]\n"
    "s_add_u32 m0, s62, 110464\n"
    "v_accvgpr_read_b32 v242, a194\n"
    "v_accvgpr_read_b32 v243, a195\n"
    "v_mfma_f32_16x16x32_bf16 v[152:155], a[72:75], a[132:135], v[152:155]\n"
    "buffer_load_dwordx4 v236, s[24:27], s29 offen offset:128 lds\n"
    "v_pk_mul_f32 v[144:145], v[144:145], v[232:233]\n"
    "v_pk_mul_f32 v[146:147], v[146:147], v[234:235]\n"
    "v_mfma_f32_16x16x32_bf16 v[156:159], a[72:75], a[136:139], v[156:159]\n"
    "s_add_u32 s29, s29, 131072\n"
    "s_add_u32 m0, s62, 114688\n"
    "v_pk_mul_f32 v[176:177], v[176:177], v[240:241]\n"
    "v_pk_mul_f32 v[178:179], v[178:179], v[242:243]\n"
    "v_mfma_f32_16x16x32_bf16 v[160:163], a[72:75], a[140:143], v[160:163]\n"
    "buffer_load_dwordx4 v236, s[24:27], s28 offen lds\n"
    "v_cvt_pk_bf16_f32 v176, v176, v177\n"
    "v_cvt_pk_bf16_f32 v177, v178, v179\n"
    "v_mfma_f32_16x16x32_bf16 v[164:167], a[72:75], a[144:147], v[164:167]\n"
    "s_add_u32 m0, s62, 118656\n"
    "v_cvt_pk_bf16_f32 v178, v144, v145\n"
    "v_cvt_pk_bf16_f32 v179, v146, v147\n"
    "v_mfma_f32_16x16x32_bf16 v[184:187], a[76:79], a[132:135], v[184:187]\n"
    "buffer_load_dwordx4 v236, s[24:27], s28 offen offset:128 lds\n"
    "buffer_store_dwordx4 v[176:179], v244, s[80:83], s86 offen offset:2048 sc1\n"
    "v_mfma_f32_16x16x32_bf16 v[188:191], a[76:79], a[136:139], v[188:191]\n"
    "s_add_u32 s28, s28, 131072\n"
    "s_add_u32 m0, s62, 122880\n"
    "v_accvgpr_read_b32 v232, a164\n"
    "v_accvgpr_read_b32 v233, a165\n"
    "v_mfma_f32_16x16x32_bf16 v[192:195], a[76:79], a[140:143], v[192:195]\n"
    "buffer_load_dwordx4 v236, s[24:27], s29 offen lds\n"
    "v_accvgpr_read_b32 v234, a166\n"
    "v_accvgpr_read_b32 v235, a167\n"
    "v_mfma_f32_16x16x32_bf16 v[196:199], a[76:79], a[144:147], v[196:199]\n"
    "s_add_u32 m0, s62, 126848\n"
    "v_accvgpr_read_b32 v240, a196\n"
    "v_accvgpr_read_b32 v241, a197\n"
    "v_mfma_f32_16x16x32_bf16 v[0:3], a[8:11], a[80:83], v[0:3]\n"
    "buffer_load_dwordx4 v236, s[24:27], s29 offen offset:128 lds\n"
    "v_accvgpr_read_b32 v242, a198\n"
    "v_accvgpr_read_b32 v243, a199\n"
    "v_mfma_f32_16x16x32_bf16 v[4:7], a[8:11], a[84:87], v[4:7]\n"
    "s_add_u32 s29, s29, 131072\n"
    "s_add_u32 m0, s62, 131072\n"
    "v_pk_mul_f32 v[148:149], v[148:149], v[232:233]\n"
    "v_pk_mul_f32 v[150:151], v[150:151], v[234:235]\n"
    "v_mfma_f32_16x16x32_bf16 v[8:11], a[8:11], a[88:91], v[8:11]\n"
    "buffer_load_dwordx4 v236, s[24:27], s28 offen lds\n"
    "v_pk_mul_f32 v[180:181], v[180:181], v[240:241]\n"
    "v_pk_mul_f32 v[182:183], v[182:183], v[242:243]\n"
    "v_mfma_f32_16x16x32_bf16 v[12:15], a[8:11], a[92:95], v[12:15]\n"
    "s_add_u32 m0, s62, 135040\n"
    "v_cvt_pk_bf16_f32 v180, v180, v181\n"
    "v_cvt_pk_bf16_f32 v181, v182, v183\n"
    "v_mfma_f32_16x16x32_bf16 v[16:19], a[8:11], a[96:99], v[16:19]\n"
    "buffer_load_dwordx4 v236, s[24:27], s28 offen offset:128 lds\n"
    "v_cvt_pk_bf16_f32 v182, v148, v149\n"
    "v_cvt_pk_bf16_f32 v183, v150, v151\n"
    "v_mfma_f32_16x16x32_bf16 v[20:23], a[8:11], a[100:103], v[20:23]\n"
    "s_add_u32 s28, s28, 131072\n"
    "s_add_u32 m0, s62, 139264\n"
    "buffer_store_dwordx4 v[180:183], v244, s[80:83], s86 offen offset:3072 sc1\n"
    "v_mfma_f32_16x16x32_bf16 v[24:27], a[8:11], a[104:107], v[24:27]\n"
    "buffer_load_dwordx4 v236, s[24:27], s29 offen lds\n"
    "v_accvgpr_read_b32 v232, a168\n"
    "v_accvgpr_read_b32 v233, a169\n"
    "v_mfma_f32_16x16x32_bf16 v[28:31], a[8:11], a[108:111], v[28:31]\n"
    "s_add_u32 m0, s62, 143232\n"
    "v_accvgpr_read_b32 v234, a170\n"
    "v_accvgpr_read_b32 v235, a171\n"
    "v_mfma_f32_16x16x32_bf16 v[132:135], a[8:11], a[112:115], v[132:135]\n"
    "buffer_load_dwordx4 v236, s[24:27], s29 offen offset:128 lds\n"
    "v_accvgpr_read_b32 v240, a200\n"
    "v_accvgpr_read_b32 v241, a201\n"
    "v_mfma_f32_16x16x32_bf16 v[32:35], a[12:15], a[80:83], v[32:35]\n"
    "s_add_u32 s29, s29, 131072\n"
    "v_accvgpr_read_b32 v242, a202\n"
    "v_accvgpr_read_b32 v243, a203\n"
    "v_mfma_f32_16x16x32_bf16 v[36:39], a[12:15], a[84:87], v[36:39]\n"
    "v_pk_mul_f32 v[152:153], v[152:153], v[232:233]\n"
    "v_pk_mul_f32 v[154:155], v[154:155], v[234:235]\n"
    "v_mfma_f32_16x16x32_bf16 v[40:43], a[12:15], a[88:91], v[40:43]\n"
    "v_pk_mul_f32 v[184:185], v[184:185], v[240:241]\n"
    "v_pk_mul_f32 v[186:187], v[186:187], v[242:243]\n"
    "v_mfma_f32_16x16x32_bf16 v[44:47], a[12:15], a[92:95], v[44:47]\n"
    "v_cvt_pk_bf16_f32 v184, v184, v185\n"
    "v_cvt_pk_bf16_f32 v185, v186, v187\n"
    "v_mfma_f32_16x16x32_bf16 v[48:51], a[12:15], a[96:99], v[48:51]\n"
    "v_cvt_pk_bf16_f32 v186, v152, v153\n"
    "v_cvt_pk_bf16_f32 v187, v154, v155\n"
    "v_mfma_f32_16x16x32_bf16 v[52:55], a[12:15], a[100:103], v[52:55]\n"
    "buffer_store_dwordx4 v[184:187], v244, s[80:83], s87 offen sc1\n"
    "v_mfma_f32_16x16x32_bf16 v[56:59], a[12:15], a[104:107], v[56:59]\n"
    "v_accvgpr_read_b32 v232, a172\n"
    "v_accvgpr_read_b32 v233, a173\n"
    "v_mfma_f32_16x16x32_bf16 v[60:63], a[12:15], a[108:111], v[60:63]\n"
    "v_accvgpr_read_b32 v234, a174\n"
    "v_accvgpr_read_b32 v235, a175\n"
    "v_mfma_f32_16x16x32_bf16 v[68:71], a[80:83], a[16:19], v[68:71]\n"
    "v_accvgpr_read_b32 v240, a204\n"
    "v_accvgpr_read_b32 v241, a205\n"
    "v_mfma_f32_16x16x32_bf16 v[72:75], a[84:87], a[16:19], v[72:75]\n"
    "v_accvgpr_read_b32 v242, a206\n"
    "v_accvgpr_read_b32 v243, a207\n"
    "v_mfma_f32_16x16x32_bf16 v[76:79], a[88:91], a[16:19], v[76:79]\n"
    "v_pk_mul_f32 v[156:157], v[156:157], v[232:233]\n"
    "v_pk_mul_f32 v[158:159], v[158:159], v[234:235]\n"
    "v_mfma_f32_16x16x32_bf16 v[80:83], a[92:95], a[16:19], v[80:83]\n"
    "v_pk_mul_f32 v[188:189], v[188:189], v[240:241]\n"
    "v_pk_mul_f32 v[190:191], v[190:191], v[242:243]\n"
    "v_mfma_f32_16x16x32_bf16 v[84:87], a[96:99], a[16:19], v[84:87]\n"
    "v_cvt_pk_bf16_f32 v188, v188, v189\n"
    "v_cvt_pk_bf16_f32 v189, v190, v191\n"
    "v_mfma_f32_16x16x32_bf16 v[88:91], a[100:103], a[16:19], v[88:91]\n"
    "v_cvt_pk_bf16_f32 v190, v156, v157\n"
    "v_cvt_pk_bf16_f32 v191, v158, v159\n"
    "v_mfma_f32_16x16x32_bf16 v[92:95], a[104:107], a[16:19], v[92:95]\n"
    "buffer_store_dwordx4 v[188:191], v244, s[80:83], s87 offen offset:1024 sc1\n"
    "v_mfma_f32_16x16x32_bf16 v[96:99], a[108:111], a[16:19], v[96:99]\n"
    "v_accvgpr_read_b32 v232, a176\n"
    "v_accvgpr_read_b32 v233, a177\n"
    "v_mfma_f32_16x16x32_bf16 v[64:67], a[112:115], a[16:19], v[64:67]\n"
    "v_accvgpr_read_b32 v234, a178\n"
    "v_accvgpr_read_b32 v235, a179\n"
    "v_mfma_f32_16x16x32_bf16 v[100:103], a[80:83], a[20:23], v[100:103]\n"
    "v_accvgpr_read_b32 v240, a208\n"
    "v_accvgpr_read_b32 v241, a209\n"
    "v_mfma_f32_16x16x32_bf16 v[104:107], a[84:87], a[20:23], v[104:107]\n"
    "v_accvgpr_read_b32 v242, a210\n"
    "v_accvgpr_read_b32 v243, a211\n"
    "v_mfma_f32_16x16x32_bf16 v[108:111], a[88:91], a[20:23], v[108:111]\n"
    "v_pk_mul_f32 v[160:161], v[160:161], v[232:233]\n"
    "v_pk_mul_f32 v[162:163], v[162:163], v[234:235]\n"
    "v_mfma_f32_16x16x32_bf16 v[112:115], a[92:95], a[20:23], v[112:115]\n"
    "v_pk_mul_f32 v[192:193], v[192:193], v[240:241]\n"
    "v_pk_mul_f32 v[194:195], v[194:195], v[242:243]\n"
    "v_mfma_f32_16x16x32_bf16 v[116:119], a[96:99], a[20:23], v[116:119]\n"
    "v_cvt_pk_bf16_f32 v192, v192, v193\n"
    "v_cvt_pk_bf16_f32 v193, v194, v195\n"
    "v_mfma_f32_16x16x32_bf16 v[120:123], a[100:103], a[20:23], v[120:123]\n"
    "v_cvt_pk_bf16_f32 v194, v160, v161\n"
    "v_cvt_pk_bf16_f32 v195, v162, v163\n"
    "v_mfma_f32_16x16x32_bf16 v[124:127], a[104:107], a[20:23], v[124:127]\n"
    "buffer_store_dwordx4 v[192:195], v244, s[80:83], s87 offen offset:2048 sc1\n"
    "v_mfma_f32_16x16x32_bf16 v[128:131], a[108:111], a[20:23], v[128:131]\n"
    "v_accvgpr_read_b32 v232, a180\n"
    "v_accvgpr_read_b32 v233, a181\n"
    "v_mfma_f32_16x16x32_bf16 v[0:3], a[48:51], a[116:119], v[0:3]\n"
    "v_accvgpr_read_b32 v234, a182\n"
    "v_accvgpr_read_b32 v235, a183\n"
    "v_mfma_f32_16x16x32_bf16 v[4:7], a[48:51], a[120:123], v[4:7]\n"
    "v_accvgpr_read_b32 v240, a212\n"
    "v_accvgpr_read_b32 v241, a213\n"
    "v_mfma_f32_16x16x32_bf16 v[8:11], a[48:51], a[124:127], v[8:11]\n"
    "v_accvgpr_read_b32 v242, a214\n"
    "v_accvgpr_read_b32 v243, a215\n"
    "v_mfma_f32_16x16x32_bf16 v[12:15], a[48:51], a[128:131], v[12:15]\n"
    "v_pk_mul_f32 v[164:165], v[164:165], v[232:233]\n"
    "v_pk_mul_f32 v[166:167], v[166:167], v[234:235]\n"
    "v_mfma_f32_16x16x32_bf16 v[16:19], a[48:51], a[132:135], v[16:19]\n"
    "v_pk_mul_f32 v[196:197], v[196:197], v[240:241]\n"
    "v_pk_mul_f32 v[198:199], v[198:199], v[242:243]\n"
    "v_mfma_f32_16x16x32_bf16 v[20:23], a[48:51], a[136:139], v[20:23]\n"
    "v_cvt_pk_bf16_f32 v196, v196, v197\n"
    "v_cvt_pk_bf16_f32 v197, v198, v199\n"
    "v_mfma_f32_16x16x32_bf16 v[24:27], a[48:51], a[140:143], v[24:27]\n"
    "v_cvt_pk_bf16_f32 v198, v164, v165\n"
    "v_cvt_pk_bf16_f32 v199, v166, v167\n"
    "v_mfma_f32_16x16x32_bf16 v[28:31], a[48:51], a[144:147], v[28:31]\n"
    "buffer_store_dwordx4 v[196:199], v244, s[80:83], s87 offen offset:3072 sc1\n"
    "v_mfma_f32_16x16x32_bf16 v[132:135], a[48:51], a[148:151], v[132:135]\n"
    "v_mfma_f32_16x16x32_bf16 v[32:35], a[52:55], a[116:119], v[32:35]\n"
    "v_mfma_f32_16x16x32_bf16 v[36:39], a[52:55], a[120:123], v[36:39]\n"
    "v_mfma_f32_16x16x32_bf16 v[40:43], a[52:55], a[124:127], v[40:43]\n"
    "v_mfma_f32_16x16x32_bf16 v[44:47], a[52:55], a[128:131], v[44:47]\n"
    "v_mfma_f32_16x16x32_bf16 v[48:51], a[52:55], a[132:135], v[48:51]\n"
    "v_mfma_f32_16x16x32_bf16 v[52:55], a[52:55], a[136:139], v[52:55]\n"
    "v_mfma_f32_16x16x32_bf16 v[56:59], a[52:55], a[140:143], v[56:59]\n"
    "v_mfma_f32_16x16x32_bf16 v[60:63], a[52:55], a[144:147], v[60:63]\n"
    "v_mfma_f32_16x16x32_bf16 v[68:71], a[116:119], a[56:59], v[68:71]\n"
    "v_mfma_f32_16x16x32_bf16 v[72:75], a[120:123], a[56:59], v[72:75]\n"
    "v_mfma_f32_16x16x32_bf16 v[76:79], a[124:127], a[56:59], v[76:79]\n"
    "v_mfma_f32_16x16x32_bf16 v[80:83], a[128:131], a[56:59], v[80:83]\n"
    "v_mfma_f32_16x16x32_bf16 v[84:87], a[132:135], a[56:59], v[84:87]\n"
    "v_mfma_f32_16x16x32_bf16 v[88:91], a[136:139], a[56:59], v[88:91]\n"
    "v_mfma_f32_16x16x32_bf16 v[92:95], a[140:143], a[56:59], v[92:95]\n"
    "v_mfma_f32_16x16x32_bf16 v[96:99], a[144:147], a[56:59], v[96:99]\n"
    "v_mfma_f32_16x16x32_bf16 v[64:67], a[148:151], a[56:59], v[64:67]\n"
    "v_mfma_f32_16x16x32_bf16 v[100:103], a[116:119], a[60:63], v[100:103]\n"
    "v_mfma_f32_16x16x32_bf16 v[104:107], a[120:123], a[60:63], v[104:107]\n"
    "v_mfma_f32_16x16x32_bf16 v[108:111], a[124:127], a[60:63], v[108:111]\n"
    "v_mfma_f32_16x16x32_bf16 v[112:115], a[128:131], a[60:63], v[112:115]\n"
    "v_mfma_f32_16x16x32_bf16 v[116:119], a[132:135], a[60:63], v[116:119]\n"
    "v_mfma_f32_16x16x32_bf16 v[120:123], a[136:139], a[60:63], v[120:123]\n"
    "v_mfma_f32_16x16x32_bf16 v[124:127], a[140:143], a[60:63], v[124:127]\n"
    "v_mfma_f32_16x16x32_bf16 v[128:131], a[144:147], a[60:63], v[128:131]\n"
    "v_mfma_f32_16x16x32_bf16 v[200:203], a[0:3], a[80:83], v[200:203]\n"
    "v_mfma_f32_16x16x32_bf16 v[208:211], a[0:3], a[84:87], v[208:211]\n"
    "v_mfma_f32_16x16x32_bf16 v[216:219], a[0:3], a[88:91], v[216:219]\n"
    "v_mfma_f32_16x16x32_bf16 a[216:219], a[0:3], a[92:95], a[216:219]\n"
    "v_mfma_f32_16x16x32_bf16 a[224:227], a[0:3], a[96:99], a[224:227]\n"
    "v_mfma_f32_16x16x32_bf16 a[232:235], a[0:3], a[100:103], a[232:235]\n"
    "v_mfma_f32_16x16x32_bf16 a[240:243], a[0:3], a[104:107], a[240:243]\n"
    "v_mfma_f32_16x16x32_bf16 a[248:251], a[0:3], a[108:111], a[248:251]\n"
    "v_mfma_f32_16x16x32_bf16 v[204:207], a[4:7], a[80:83], v[204:207]\n"
    "v_mfma_f32_16x16x32_bf16 v[212:215], a[4:7], a[84:87], v[212:215]\n"
    "v_mfma_f32_16x16x32_bf16 v[220:223], a[4:7], a[88:91], v[220:223]\n"
    "v_mfma_f32_16x16x32_bf16 a[220:223], a[4:7], a[92:95], a[220:223]\n"
    "v_mfma_f32_16x16x32_bf16 a[228:231], a[4:7], a[96:99], a[228:231]\n"
    "v_mfma_f32_16x16x32_bf16 a[236:239], a[4:7], a[100:103], a[236:239]\n"
    "v_mfma_f32_16x16x32_bf16 a[244:247], a[4:7], a[104:107], a[244:247]\n"
    "v_mfma_f32_16x16x32_bf16 a[252:255], a[4:7], a[108:111], a[252:255]\n"
    "v_mfma_f32_16x16x32_bf16 v[200:203], a[40:43], a[116:119], v[200:203]\n"
    "v_mfma_f32_16x16x32_bf16 v[208:211], a[40:43], a[120:123], v[208:211]\n"
    "v_mfma_f32_16x16x32_bf16 v[216:219], a[40:43], a[124:127], v[216:219]\n"
    "v_mfma_f32_16x16x32_bf16 a[216:219], a[40:43], a[128:131], a[216:219]\n"
    "v_mfma_f32_16x16x32_bf16 a[224:227], a[40:43], a[132:135], a[224:227]\n"
    "v_mfma_f32_16x16x32_bf16 a[232:235], a[40:43], a[136:139], a[232:235]\n"
    "v_mfma_f32_16x16x32_bf16 a[240:243], a[40:43], a[140:143], a[240:243]\n"
    "v_mfma_f32_16x16x32_bf16 a[248:251], a[40:43], a[144:147], a[248:251]\n"
    "v_mfma_f32_16x16x32_bf16 v[204:207], a[44:47], a[116:119], v[204:207]\n"
    "v_mfma_f32_16x16x32_bf16 v[212:215], a[44:47], a[120:123], v[212:215]\n"
    "v_mfma_f32_16x16x32_bf16 v[220:223], a[44:47], a[124:127], v[220:223]\n"
    "v_mfma_f32_16x16x32_bf16 a[220:223], a[44:47], a[128:131], a[220:223]\n"
    "v_mfma_f32_16x16x32_bf16 a[228:231], a[44:47], a[132:135], a[228:231]\n"
    "v_mfma_f32_16x16x32_bf16 a[236:239], a[44:47], a[136:139], a[236:239]\n"
    "v_mfma_f32_16x16x32_bf16 a[244:247], a[44:47], a[140:143], a[244:247]\n"
    "v_mfma_f32_16x16x32_bf16 a[252:255], a[44:47], a[144:147], a[252:255]\n"
    "s_nop 7\n"
    "v_lshrrev_b32 v245, 1, v244\n"
    "v_add_u32 v245, s90, v245\n"
    "s_mov_b32 s92, 0x3e8293ee\n"
    "s_mov_b32 s93, 0xbe8293ee\n"
    "v_cvt_pk_bf16_f32 v132, v132, v133\n"
    "v_cvt_pk_bf16_f32 v133, v134, v135\n"
    "v_cvt_pk_bf16_f32 v64, v64, v65\n"
    "ds_write_b64 v245, v[132:133] offset:2048\n"
    "v_cvt_pk_bf16_f32 v65, v66, v67\n"
    "ds_write_b64 v245, v[64:65] offset:2560\n"
    "v_cvt_pk_bf16_f32 v99, v98, v99\n"
    "v_cvt_pk_bf16_f32 v98, v96, v97\n"
    "v_cvt_pk_bf16_f32 v96, v92, v93\n"
    "v_cvt_pk_bf16_f32 v4, v4, v5\n"
    "v_cvt_pk_bf16_f32 v0, v0, v1\n"
    "v_cvt_pk_bf16_f32 v208, v208, v209\n"
    "v_cvt_pk_bf16_f32 v93, v86, v87\n"
    "v_cvt_pk_bf16_f32 v5, v6, v7\n"
    "v_cvt_pk_bf16_f32 v1, v2, v3\n"
    "v_cvt_pk_bf16_f32 v209, v210, v211\n"
    "v_cvt_pk_bf16_f32 v97, v94, v95\n"
    "v_cvt_pk_bf16_f32 v12, v12, v13\n"
    "v_cvt_pk_bf16_f32 v8, v8, v9\n"
    "v_cvt_pk_bf16_f32 v6, v36, v37\n"
    "v_cvt_pk_bf16_f32 v7, v38, v39\n"
    "v_cvt_pk_bf16_f32 v86, v72, v73\n"
    "v_cvt_pk_bf16_f32 v2, v32, v33\n"
    "v_cvt_pk_bf16_f32 v3, v34, v35\n"
    "v_cvt_pk_bf16_f32 v210, v212, v213\n"
    "v_cvt_pk_bf16_f32 v211, v214, v215\n"
    "v_cvt_pk_bf16_f32 v216, v216, v217\n"
    "v_cvt_pk_bf16_f32 v94, v88, v89\n"
    "v_cvt_pk_bf16_f32 v13, v14, v15\n"
    "v_cvt_pk_bf16_f32 v9, v10, v11\n"
    "v_cvt_pk_bf16_f32 v87, v74, v75\n"
    "v_cvt_pk_bf16_f32 v217, v218, v219\n"
    "v_accvgpr_read_b32 v72, a216\n"
    "v_accvgpr_read_b32 v73, a217\n"
    "v_cvt_pk_bf16_f32 v28, v28, v29\n"
    "v_cvt_pk_bf16_f32 v131, v130, v131\n"
    "v_cvt_pk_bf16_f32 v14, v44, v45\n"
    "v_cvt_pk_bf16_f32 v15, v46, v47\n"
    "v_cvt_pk_bf16_f32 v10, v40, v41\n"
    "v_cvt_pk_bf16_f32 v11, v42, v43\n"
    "v_cvt_pk_bf16_f32 v89, v78, v79\n"
    "v_cvt_pk_bf16_f32 v88, v76, v77\n"
    "v_cvt_pk_bf16_f32 v218, v220, v221\n"
    "v_cvt_pk_bf16_f32 v219, v222, v223\n"
    "v_accvgpr_read_b32 v74, a218\n"
    "v_accvgpr_read_b32 v75, a219\n"
    "v_cvt_pk_bf16_f32 v72, v72, v73\n"
    "v_cvt_pk_bf16_f32 v29, v30, v31\n"
    "v_cvt_pk_bf16_f32 v130, v128, v129\n"
    "v_mfma_f32_16x16x32_bf16 v[40:43], v[0:3], v[208:211], v[246:249]\n"
    "v_accvgpr_read_b32 v76, a220\n"
    "v_accvgpr_read_b32 v77, a221\n"
    "v_mfma_f32_16x16x32_bf16 v[44:47], v[4:7], v[208:211], v[250:253]\n"
    "v_accvgpr_read_b32 v78, a222\n"
    "v_accvgpr_read_b32 v79, a223\n"
    "v_cvt_pk_bf16_f32 v73, v74, v75\n"
    "v_cvt_pk_bf16_f32 v30, v60, v61\n"
    "v_cvt_pk_bf16_f32 v31, v62, v63\n"
    "v_cvt_pk_bf16_f32 v128, v124, v125\n"
    "v_cvt_pk_bf16_f32 v74, v76, v77\n"
    "v_cvt_pk_bf16_f32 v75, v78, v79\n"
    "v_cvt_pk_bf16_f32 v125, v118, v119\n"
    "v_cvt_pk_bf16_f32 v124, v116, v117\n"
    "v_mfma_f32_16x16x32_bf16 v[60:63], v[4:7], v[216:219], v[246:249]\n"
    "v_cvt_pk_bf16_f32 v119, v106, v107\n"
    "v_cvt_pk_bf16_f32 v118, v104, v105\n"
    "v_mfma_f32_16x16x32_bf16 v[64:67], v[8:11], v[216:219], v[250:253]\n"
    "v_cvt_pk_bf16_f32 v117, v102, v103\n"
    "v_cvt_pk_bf16_f32 v116, v100, v101\n"
    "v_mfma_f32_16x16x32_bf16 v[100:103], v[8:11], v[72:75], v[246:249]\n"
    "v_max_f32 v40, v40, v41\n"
    "v_max_f32 v42, v42, v43\n"
    "v_mfma_f32_16x16x32_bf16 v[104:107], v[12:15], v[72:75], v[250:253]\n"
    "v_max_f32 v44, v44, v45\n"
    "v_max_f32 v46, v46, v47\n"
    "v_max3_f32 v41, v40, v42, v44\n"
    "v_max_f32 v41, v41, v46\n"
    "v_max_f32 v60, v60, v61\n"
    "v_max_f32 v62, v62, v63\n"
    "v_max_f32 v64, v64, v65\n"
    "v_mov_b32 v43, v41\n"
    "v_max_f32 v66, v66, v67\n"
    "v_max3_f32 v61, v60, v62, v64\n"
    "v_max_f32 v61, v61, v66\n"
    "v_max_f32 v100, v100, v101\n"
    "v_max_f32 v102, v102, v103\n"
    "v_max_f32 v104, v104, v105\n"
    "v_mov_b32 v63, v61\n"
    "v_max_f32 v106, v106, v107\n"
    "v_max3_f32 v101, v100, v102, v104\n"
    "v_permlane16_swap_b32 v41, v43\n"
    "v_max_f32 v101, v101, v106\n"
    "v_max_f32 v41, v41, v43\n"
    "v_mov_b32 v103, v101\n"
    "v_mov_b32 v43, v41\n"
    "v_permlane16_swap_b32 v61, v63\n"
    "v_max_f32 v61, v61, v63\n"
    "v_mov_b32 v63, v61\n"
    "v_permlane16_swap_b32 v101, v103\n"
    "v_permlane32_swap_b32 v41, v43\n"
    "v_max_f32 v101, v101, v103\n"
    "v_max_f32 v41, v41, v43\n"
    "v_mov_b32 v103, v101\n"
    "v_mul_f32 v45, s93, v41\n"
    "v_permlane32_swap_b32 v61, v63\n"
    "v_fma_f32 v40, v40, s92, v45\n"
    "v_fma_f32 v42, v42, s92, v45\n"
    "v_fma_f32 v44, v44, s92, v45\n"
    "v_fma_f32 v46, v46, s92, v45\n"
    "v_max_f32 v61, v61, v63\n"
    "v_exp_f32 v40, v40\n"
    "v_exp_f32 v42, v42\n"
    "v_exp_f32 v44, v44\n"
    "v_exp_f32 v46, v46\n"
    "v_mul_f32 v65, s93, v61\n"
    "v_permlane32_swap_b32 v101, v103\n"
    "v_fma_f32 v60, v60, s92, v65\n"
    "v_fma_f32 v62, v62, s92, v65\n"
    "v_fma_f32 v64, v64, s92, v65\n"
    "v_fma_f32 v66, v66, s92, v65\n"
    "v_max_f32 v101, v101, v103\n"
    "v_exp_f32 v60, v60\n"
    "v_exp_f32 v62, v62\n"
    "v_exp_f32 v64, v64\n"
    "v_exp_f32 v66, v66\n"
    "v_mul_f32 v105, s93, v101\n"
    "v_cvt_pk_bf16_f32 v41, v40, v42\n"
    "v_cvt_pk_bf16_f32 v43, v44, v46\n"
    "v_fma_f32 v100, v100, s92, v105\n"
    "v_fma_f32 v102, v102, s92, v105\n"
    "v_fma_f32 v104, v104, s92, v105\n"
    "v_fma_f32 v106, v106, s92, v105\n"
    "v_cvt_pk_bf16_f32 v16, v16, v17\n"
    "v_mov_b32 v240, 0x3f803f80\n"
    "s_waitcnt lgkmcnt(0)\n"
    "s_barrier\n"
    "ds_read_b64 v[180:181], v245 offset:512\n"
    "ds_read_b64 v[182:183], v245 offset:0\n"
    "v_mov_b32 v241, 0x3f803f80\n"
    "v_mov_b32 v242, 0x3f803f80\n"
    "v_mov_b32 v243, 0x3f803f80\n"
    "v_perm_b32 v208, v41, v41, v254\n"
    "v_perm_b32 v209, v41, v41, v255\n"
    "v_perm_b32 v210, v43, v43, v254\n"
    "v_perm_b32 v211, v43, v43, v255\n"
    "v_exp_f32 v100, v100\n"
    "v_exp_f32 v102, v102\n"
    "v_exp_f32 v104, v104\n"
    "v_exp_f32 v106, v106\n"
    "v_cvt_pk_bf16_f32 v17, v18, v19\n"
    "v_cvt_pk_bf16_f32 v61, v60, v62\n"
    "v_cvt_pk_bf16_f32 v63, v64, v66\n"
    "v_cvt_pk_bf16_f32 v18, v48, v49\n"
    "v_cvt_pk_bf16_f32 v19, v50, v51\n"
    "v_perm_b32 v216, v61, v61, v254\n"
    "v_perm_b32 v217, v61, v61, v255\n"
    "v_perm_b32 v218, v63, v63, v254\n"
    "v_perm_b32 v219, v63, v63, v255\n"
    "v_cvt_pk_bf16_f32 v129, v126, v127\n"
    "v_cvt_pk_bf16_f32 v92, v84, v85\n"
    "v_mfma_f32_16x16x32_bf16 v[48:51], v[240:243], v[208:211], 0\n"
    "v_cvt_pk_bf16_f32 v101, v100, v102\n"
    "v_cvt_pk_bf16_f32 v103, v104, v106\n"
    "v_cvt_pk_bf16_f32 v126, v120, v121\n"
    "v_cvt_pk_bf16_f32 v85, v70, v71\n"
    "v_cvt_pk_bf16_f32 v84, v68, v69\n"
    "v_perm_b32 v72, v101, v101, v254\n"
    "v_perm_b32 v73, v101, v101, v255\n"
    "v_perm_b32 v74, v103, v103, v254\n"
    "v_perm_b32 v75, v103, v103, v255\n"
    "v_cvt_pk_bf16_f32 v121, v110, v111\n"
    "v_cvt_pk_bf16_f32 v120, v108, v109\n"
    "v_mfma_f32_16x16x32_bf16 v[68:71], v[240:243], v[216:219], 0\n"
    "v_cvt_pk_bf16_f32 v127, v122, v123\n"
    "v_cvt_pk_bf16_f32 v123, v114, v115\n"
    "v_mfma_f32_16x16x32_bf16 v[44:47], v[116:119], v[208:211], 0\n"
    "v_cvt_pk_bf16_f32 v122, v112, v113\n"
    "v_cvt_pk_bf16_f32 v95, v90, v91\n"
    "v_mfma_f32_16x16x32_bf16 v[108:111], v[240:243], v[72:75], 0\n"
    "v_cvt_pk_bf16_f32 v91, v82, v83\n"
    "v_cvt_pk_bf16_f32 v90, v80, v81\n"
    "v_mfma_f32_16x16x32_bf16 v[40:43], v[84:87], v[208:211], 0\n"
    "v_rcp_f32 v48, v48\n"
    "v_mfma_f32_16x16x32_bf16 v[64:67], v[118:121], v[216:219], 0\n"
    "v_accvgpr_read_b32 v136, a224\n"
    "v_accvgpr_read_b32 v137, a225\n"
    "v_mfma_f32_16x16x32_bf16 v[60:63], v[86:89], v[216:219], 0\n"
    "v_rcp_f32 v68, v68\n"
    "v_mfma_f32_16x16x32_bf16 v[104:107], v[120:123], v[72:75], 0\n"
    "v_accvgpr_read_b32 v138, a226\n"
    "v_accvgpr_read_b32 v139, a227\n"
    "v_mfma_f32_16x16x32_bf16 v[100:103], v[88:91], v[72:75], 0\n"
    "v_cvt_pk_bf16_f32 v136, v136, v137\n"
    "v_pk_mul_f32 v[44:45], v[44:45], v[48:49] op_sel_hi:[1,0]\n"
    "v_accvgpr_read_b32 v140, a228\n"
    "v_accvgpr_read_b32 v141, a229\n"
    "v_accvgpr_read_b32 v142, a230\n"
    "v_accvgpr_read_b32 v143, a231\n"
    "v_cvt_pk_bf16_f32 v137, v138, v139\n"
    "v_accvgpr_read_b32 v156, a232\n"
    "v_accvgpr_read_b32 v157, a233\n"
    "v_cvt_pk_bf16_f32 v20, v20, v21\n"
    "v_pk_mul_f32 v[46:47], v[46:47], v[48:49] op_sel_hi:[1,0]\n"
    "v_cvt_pk_bf16_f32 v44, v44, v45\n"
    "v_rcp_f32 v108, v108\n"
    "v_cvt_pk_bf16_f32 v138, v140, v141\n"
    "v_cvt_pk_bf16_f32 v139, v142, v143\n"
    "v_accvgpr_read_b32 v158, a234\n"
    "v_accvgpr_read_b32 v159, a235\n"
    "v_cvt_pk_bf16_f32 v156, v156, v157\n"
    "v_cvt_pk_bf16_f32 v21, v22, v23\n"
    "v_pk_mul_f32 v[40:41], v[40:41], v[48:49] op_sel_hi:[1,0]\n"
    "v_pk_mul_f32 v[42:43], v[42:43], v[48:49] op_sel_hi:[1,0]\n"
    "v_cvt_pk_bf16_f32 v45, v46, v47\n"
    "v_pk_mul_f32 v[64:65], v[64:65], v[68:69] op_sel_hi:[1,0]\n"
    "v_accvgpr_read_b32 v160, a236\n"
    "v_accvgpr_read_b32 v161, a237\n"
    "v_accvgpr_read_b32 v162, a238\n"
    "v_accvgpr_read_b32 v163, a239\n"
    "v_cvt_pk_bf16_f32 v157, v158, v159\n"
    "v_accvgpr_read_b32 v32, a240\n"
    "v_accvgpr_read_b32 v33, a241\n"
    "v_cvt_pk_bf16_f32 v24, v24, v25\n"
    "v_cvt_pk_bf16_f32 v22, v52, v53\n"
    "v_cvt_pk_bf16_f32 v23, v54, v55\n"
    "v_cvt_pk_bf16_f32 v46, v40, v41\n"
    "v_cvt_pk_bf16_f32 v47, v42, v43\n"
    "v_pk_mul_f32 v[66:67], v[66:67], v[68:69] op_sel_hi:[1,0]\n"
    "v_cvt_pk_bf16_f32 v64, v64, v65\n"
    "v_cvt_pk_bf16_f32 v158, v160, v161\n"
    "v_cvt_pk_bf16_f32 v159, v162, v163\n"
    "v_accvgpr_read_b32 v34, a242\n"
    "v_accvgpr_read_b32 v35, a243\n"
    "v_cvt_pk_bf16_f32 v32, v32, v33\n"
    "v_cvt_pk_bf16_f32 v25, v26, v27\n"
    "buffer_store_dwordx4 v[44:47], v244, s[80:83], s84 offen offset:1024 sc1\n"
    "v_pk_mul_f32 v[60:61], v[60:61], v[68:69] op_sel_hi:[1,0]\n"
    "v_pk_mul_f32 v[62:63], v[62:63], v[68:69] op_sel_hi:[1,0]\n"
    "v_cvt_pk_bf16_f32 v65, v66, v67\n"
    "v_pk_mul_f32 v[104:105], v[104:105], v[108:109] op_sel_hi:[1,0]\n"
    "v_mfma_f32_16x16x32_bf16 v[144:147], v[12:15], v[136:139], v[246:249]\n"
    "v_accvgpr_read_b32 v36, a244\n"
    "v_accvgpr_read_b32 v37, a245\n"
    "v_mfma_f32_16x16x32_bf16 v[148:151], v[16:19], v[136:139], v[250:253]\n"
    "v_accvgpr_read_b32 v38, a246\n"
    "v_accvgpr_read_b32 v39, a247\n"
    "v_cvt_pk_bf16_f32 v33, v34, v35\n"
    "v_accvgpr_read_b32 v52, a248\n"
    "v_accvgpr_read_b32 v53, a249\n"
    "v_cvt_pk_bf16_f32 v26, v56, v57\n"
    "v_cvt_pk_bf16_f32 v27, v58, v59\n"
    "v_cvt_pk_bf16_f32 v66, v60, v61\n"
    "v_cvt_pk_bf16_f32 v67, v62, v63\n"
    "v_pk_mul_f32 v[106:107], v[106:107], v[108:109] op_sel_hi:[1,0]\n"
    "v_cvt_pk_bf16_f32 v104, v104, v105\n"
    "v_cvt_pk_bf16_f32 v34, v36, v37\n"
    "v_cvt_pk_bf16_f32 v35, v38, v39\n"
    "v_accvgpr_read_b32 v54, a250\n"
    "v_accvgpr_read_b32 v55, a251\n"
    "v_cvt_pk_bf16_f32 v52, v52, v53\n"
    "ds_read_b64 v[82:83], v245 offset:1536\n"
    "buffer_store_dwordx4 v[64:67], v244, s[80:83], s84 offen offset:2048 sc1\n"
    "v_pk_mul_f32 v[100:101], v[100:101], v[108:109] op_sel_hi:[1,0]\n"
    "v_pk_mul_f32 v[102:103], v[102:103], v[108:109] op_sel_hi:[1,0]\n"
    "v_cvt_pk_bf16_f32 v105, v106, v107\n"
    "v_mfma_f32_16x16x32_bf16 v[164:167], v[16:19], v[156:159], v[246:249]\n"
    "v_accvgpr_read_b32 v56, a252\n"
    "v_accvgpr_read_b32 v57, a253\n"
    "v_mfma_f32_16x16x32_bf16 v[168:171], v[20:23], v[156:159], v[250:253]\n"
    "v_accvgpr_read_b32 v58, a254\n"
    "v_accvgpr_read_b32 v59, a255\n"
    "v_cvt_pk_bf16_f32 v53, v54, v55\n"
    "ds_read_b64 v[114:115], v245 offset:1024\n"
    "v_cvt_pk_bf16_f32 v106, v100, v101\n"
    "v_cvt_pk_bf16_f32 v107, v102, v103\n"
    "v_cvt_pk_bf16_f32 v54, v56, v57\n"
    "v_cvt_pk_bf16_f32 v55, v58, v59\n"
    "v_cvt_pk_bf16_f32 v200, v200, v201\n"
    "buffer_store_dwordx4 v[104:107], v244, s[80:83], s84 offen offset:3072 sc1\n"
    "v_mfma_f32_16x16x32_bf16 v[40:43], v[20:23], v[32:35], v[246:249]\n"
    "v_cvt_pk_bf16_f32 v201, v202, v203\n"
    "v_cvt_pk_bf16_f32 v202, v204, v205\n"
    "v_mfma_f32_16x16x32_bf16 v[44:47], v[24:27], v[32:35], v[250:253]\n"
    "v_cvt_pk_bf16_f32 v203, v206, v207\n"
    "v_max_f32 v144, v144, v145\n"
    "v_mfma_f32_16x16x32_bf16 v[60:63], v[24:27], v[52:55], v[246:249]\n"
    "v_max_f32 v146, v146, v147\n"
    "v_max_f32 v148, v148, v149\n"
    "v_mfma_f32_16x16x32_bf16 v[64:67], v[28:31], v[52:55], v[250:253]\n"
    "v_max_f32 v150, v150, v151\n"
    "v_max3_f32 v145, v144, v146, v148\n"
    "s_waitcnt lgkmcnt(2)\n"
    "v_mfma_f32_16x16x32_bf16 v[100:103], v[180:183], v[200:203], v[246:249]\n"
    "v_max_f32 v145, v145, v150\n"
    "v_max_f32 v164, v164, v165\n"
    "v_mfma_f32_16x16x32_bf16 v[104:107], v[0:3], v[200:203], v[250:253]\n"
    "v_max_f32 v166, v166, v167\n"
    "v_max_f32 v168, v168, v169\n"
    "v_mov_b32 v147, v145\n"
    "v_max_f32 v170, v170, v171\n"
    "v_max3_f32 v165, v164, v166, v168\n"
    "v_max_f32 v165, v165, v170\n"
    "v_max_f32 v40, v40, v41\n"
    "v_max_f32 v42, v42, v43\n"
    "v_max_f32 v44, v44, v45\n"
    "v_mov_b32 v167, v165\n"
    "v_max_f32 v46, v46, v47\n"
    "v_max3_f32 v41, v40, v42, v44\n"
    "v_permlane16_swap_b32 v145, v147\n"
    "v_max_f32 v41, v41, v46\n"
    "v_max_f32 v60, v60, v61\n"
    "v_max_f32 v62, v62, v63\n"
    "v_max_f32 v64, v64, v65\n"
    "v_max_f32 v145, v145, v147\n"
    "v_mov_b32 v43, v41\n"
    "v_max_f32 v66, v66, v67\n"
    "v_max3_f32 v61, v60, v62, v64\n"
    "v_mov_b32 v147, v145\n"
    "v_permlane16_swap_b32 v165, v167\n"
    "v_max_f32 v61, v61, v66\n"
    "v_max_f32 v100, v100, v101\n"
    "v_max_f32 v102, v102, v103\n"
    "v_max_f32 v104, v104, v105\n"
    "v_max_f32 v165, v165, v167\n"
    "v_mov_b32 v63, v61\n"
    "v_max_f32 v106, v106, v107\n"
    "v_max3_f32 v101, v100, v102, v104\n"
    "v_mov_b32 v167, v165\n"
    "v_permlane16_swap_b32 v41, v43\n"
    "v_max_f32 v101, v101, v106\n"
    "v_permlane32_swap_b32 v145, v147\n"
    "v_max_f32 v41, v41, v43\n"
    "v_mov_b32 v103, v101\n"
    "v_max_f32 v145, v145, v147\n"
    "v_mov_b32 v43, v41\n"
    "v_permlane16_swap_b32 v61, v63\n"
    "v_mul_f32 v149, s93, v145\n"
    "v_permlane32_swap_b32 v165, v167\n"
    "v_max_f32 v61, v61, v63\n"
    "v_fma_f32 v144, v144, s92, v149\n"
    "v_fma_f32 v146, v146, s92, v149\n"
    "v_fma_f32 v148, v148, s92, v149\n"
    "v_fma_f32 v150, v150, s92, v149\n"
    "v_max_f32 v165, v165, v167\n"
    "v_mov_b32 v63, v61\n"
    "v_permlane16_swap_b32 v101, v103\n"
    "v_exp_f32 v144, v144\n"
    "v_exp_f32 v146, v146\n"
    "v_exp_f32 v148, v148\n"
    "v_exp_f32 v150, v150\n"
    "v_mul_f32 v169, s93, v165\n"
    "v_permlane32_swap_b32 v41, v43\n"
    "v_max_f32 v101, v101, v103\n"
    "v_fma_f32 v164, v164, s92, v169\n"
    "v_fma_f32 v166, v166, s92, v169\n"
    "v_fma_f32 v168, v168, s92, v169\n"
    "v_fma_f32 v170, v170, s92, v169\n"
    "v_max_f32 v41, v41, v43\n"
    "v_mov_b32 v103, v101\n"
    "v_exp_f32 v164, v164\n"
    "v_exp_f32 v166, v166\n"
    "v_exp_f32 v168, v168\n"
    "v_exp_f32 v170, v170\n"
    "v_mul_f32 v45, s93, v41\n"
    "v_permlane32_swap_b32 v61, v63\n"
    "v_cvt_pk_bf16_f32 v145, v144, v146\n"
    "v_cvt_pk_bf16_f32 v147, v148, v150\n"
    "v_fma_f32 v40, v40, s92, v45\n"
    "v_fma_f32 v42, v42, s92, v45\n"
    "v_fma_f32 v44, v44, s92, v45\n"
    "v_fma_f32 v46, v46, s92, v45\n"
    "v_max_f32 v61, v61, v63\n"
    "v_perm_b32 v136, v145, v145, v254\n"
    "v_perm_b32 v137, v145, v145, v255\n"
    "v_perm_b32 v138, v147, v147, v254\n"
    "v_perm_b32 v139, v147, v147, v255\n"
    "v_exp_f32 v40, v40\n"
    "v_exp_f32 v42, v42\n"
    "v_exp_f32 v44, v44\n"
    "v_exp_f32 v46, v46\n"
    "v_mul_f32 v65, s93, v61\n"
    "v_permlane32_swap_b32 v101, v103\n"
    "v_cvt_pk_bf16_f32 v165, v164, v166\n"
    "v_cvt_pk_bf16_f32 v167, v168, v170\n"
    "v_fma_f32 v60, v60, s92, v65\n"
    "v_fma_f32 v62, v62, s92, v65\n"
    "v_fma_f32 v64, v64, s92, v65\n"
    "v_fma_f32 v66, v66, s92, v65\n"
    "v_max_f32 v101, v101, v103\n"
    "v_perm_b32 v156, v165, v165, v254\n"
    "v_perm_b32 v157, v165, v165, v255\n"
    "v_perm_b32 v158, v167, v167, v254\n"
    "v_perm_b32 v159, v167, v167, v255\n"
    "v_exp_f32 v60, v60\n"
    "v_exp_f32 v62, v62\n"
    "v_exp_f32 v64, v64\n"
    "v_exp_f32 v66, v66\n"
    "v_mul_f32 v105, s93, v101\n"
    "v_mfma_f32_16x16x32_bf16 v[152:155], v[240:243], v[136:139], 0\n"
    "v_cvt_pk_bf16_f32 v41, v40, v42\n"
    "v_cvt_pk_bf16_f32 v43, v44, v46\n"
    "v_fma_f32 v100, v100, s92, v105\n"
    "v_fma_f32 v102, v102, s92, v105\n"
    "v_fma_f32 v104, v104, s92, v105\n"
    "v_fma_f32 v106, v106, s92, v105\n"
    "v_perm_b32 v32, v41, v41, v254\n"
    "v_perm_b32 v33, v41, v41, v255\n"
    "v_perm_b32 v34, v43, v43, v254\n"
    "v_perm_b32 v35, v43, v43, v255\n"
    "v_exp_f32 v100, v100\n"
    "v_exp_f32 v102, v102\n"
    "v_exp_f32 v104, v104\n"
    "v_exp_f32 v106, v106\n"
    "v_mfma_f32_16x16x32_bf16 v[172:175], v[240:243], v[156:159], 0\n"
    "v_cvt_pk_bf16_f32 v61, v60, v62\n"
    "v_cvt_pk_bf16_f32 v63, v64, v66\n"
    "v_mfma_f32_16x16x32_bf16 v[148:151], v[122:125], v[136:139], 0\n"
    "v_perm_b32 v52, v61, v61, v254\n"
    "v_perm_b32 v53, v61, v61, v255\n"
    "v_perm_b32 v54, v63, v63, v254\n"
    "v_perm_b32 v55, v63, v63, v255\n"
    "v_mfma_f32_16x16x32_bf16 v[48:51], v[240:243], v[32:35], 0\n"
    "v_cvt_pk_bf16_f32 v101, v100, v102\n"
    "v_cvt_pk_bf16_f32 v103, v104, v106\n"
    "v_mfma_f32_16x16x32_bf16 v[144:147], v[90:93], v[136:139], 0\n"
    "v_perm_b32 v200, v101, v101, v254\n"
    "v_perm_b32 v201, v101, v101, v255\n"
    "v_mfma_f32_16x16x32_bf16 v[168:171], v[124:127], v[156:159], 0\n"
    "v_perm_b32 v202, v103, v103, v254\n"
    "v_perm_b32 v203, v103, v103, v255\n"
    "v_mfma_f32_16x16x32_bf16 v[68:71], v[240:243], v[52:55], 0\n"
    "v_rcp_f32 v152, v152\n"
    "v_mfma_f32_16x16x32_bf16 v[164:167], v[92:95], v[156:159], 0\n"
    "v_rcp_f32 v172, v172\n"
    "v_mfma_f32_16x16x32_bf16 v[44:47], v[126:129], v[32:35], 0\n"
    "v_pk_mul_f32 v[148:149], v[148:149], v[152:153] op_sel_hi:[1,0]\n"
    "v_pk_mul_f32 v[150:151], v[150:151], v[152:153] op_sel_hi:[1,0]\n"
    "v_mfma_f32_16x16x32_bf16 v[108:111], v[240:243], v[200:203], 0\n"
    "v_cvt_pk_bf16_f32 v148, v148, v149\n"
    "v_rcp_f32 v48, v48\n"
    "v_mfma_f32_16x16x32_bf16 v[40:43], v[94:97], v[32:35], 0\n"
    "v_pk_mul_f32 v[144:145], v[144:145], v[152:153] op_sel_hi:[1,0]\n"
    "v_pk_mul_f32 v[146:147], v[146:147], v[152:153] op_sel_hi:[1,0]\n"
    "v_mfma_f32_16x16x32_bf16 v[64:67], v[128:131], v[52:55], 0\n"
    "v_cvt_pk_bf16_f32 v149, v150, v151\n"
    "v_pk_mul_f32 v[168:169], v[168:169], v[172:173] op_sel_hi:[1,0]\n"
    "v_mfma_f32_16x16x32_bf16 v[60:63], v[96:99], v[52:55], 0\n"
    "v_cvt_pk_bf16_f32 v150, v144, v145\n"
    "v_cvt_pk_bf16_f32 v151, v146, v147\n"
    "s_waitcnt lgkmcnt(0)\n"
    "v_mfma_f32_16x16x32_bf16 v[104:107], v[114:117], v[200:203], 0\n"
    "v_pk_mul_f32 v[170:171], v[170:171], v[172:173] op_sel_hi:[1,0]\n"
    "v_cvt_pk_bf16_f32 v168, v168, v169\n"
    "v_mfma_f32_16x16x32_bf16 v[100:103], v[82:85], v[200:203], 0\n"
    "v_rcp_f32 v68, v68\n"
    "buffer_store_dwordx4 v[148:151], v244, s[80:83], s85 offen sc1\n"
    "v_pk_mul_f32 v[164:165], v[164:165], v[172:173] op_sel_hi:[1,0]\n"
    "v_pk_mul_f32 v[166:167], v[166:167], v[172:173] op_sel_hi:[1,0]\n"
    "v_cvt_pk_bf16_f32 v169, v170, v171\n"
    "v_pk_mul_f32 v[44:45], v[44:45], v[48:49] op_sel_hi:[1,0]\n"
    "v_cvt_pk_bf16_f32 v170, v164, v165\n"
    "v_cvt_pk_bf16_f32 v171, v166, v167\n"
    "v_pk_mul_f32 v[46:47], v[46:47], v[48:49] op_sel_hi:[1,0]\n"
    "v_cvt_pk_bf16_f32 v44, v44, v45\n"
    "v_rcp_f32 v108, v108\n"
    "buffer_store_dwordx4 v[168:171], v244, s[80:83], s85 offen offset:1024 sc1\n"
    "v_pk_mul_f32 v[40:41], v[40:41], v[48:49] op_sel_hi:[1,0]\n"
    "v_pk_mul_f32 v[42:43], v[42:43], v[48:49] op_sel_hi:[1,0]\n"
    "v_cvt_pk_bf16_f32 v45, v46, v47\n"
    "v_pk_mul_f32 v[64:65], v[64:65], v[68:69] op_sel_hi:[1,0]\n"
    "v_cvt_pk_bf16_f32 v46, v40, v41\n"
    "v_cvt_pk_bf16_f32 v47, v42, v43\n"
    "v_pk_mul_f32 v[66:67], v[66:67], v[68:69] op_sel_hi:[1,0]\n"
    "v_cvt_pk_bf16_f32 v64, v64, v65\n"
    "buffer_store_dwordx4 v[44:47], v244, s[80:83], s85 offen offset:2048 sc1\n"
    "v_pk_mul_f32 v[60:61], v[60:61], v[68:69] op_sel_hi:[1,0]\n"
    "v_pk_mul_f32 v[62:63], v[62:63], v[68:69] op_sel_hi:[1,0]\n"
    "v_cvt_pk_bf16_f32 v65, v66, v67\n"
    "v_pk_mul_f32 v[104:105], v[104:105], v[108:109] op_sel_hi:[1,0]\n"
    "v_cvt_pk_bf16_f32 v66, v60, v61\n"
    "v_cvt_pk_bf16_f32 v67, v62, v63\n"
    "v_pk_mul_f32 v[106:107], v[106:107], v[108:109] op_sel_hi:[1,0]\n"
    "v_cvt_pk_bf16_f32 v104, v104, v105\n"
    "buffer_store_dwordx4 v[64:67], v244, s[80:83], s85 offen offset:3072 sc1\n"
    "v_pk_mul_f32 v[100:101], v[100:101], v[108:109] op_sel_hi:[1,0]\n"
    "v_pk_mul_f32 v[102:103], v[102:103], v[108:109] op_sel_hi:[1,0]\n"
    "v_cvt_pk_bf16_f32 v105, v106, v107\n"
    "v_cvt_pk_bf16_f32 v106, v100, v101\n"
    "v_cvt_pk_bf16_f32 v107, v102, v103\n"
    "buffer_store_dwordx4 v[104:107], v244, s[80:83], s84 offen sc1\n"
    "EPIEND_%=:\n"
    "s_waitcnt vmcnt(0)\n"
    "s_barrier\n"
    : "+{v[224:235]}"(vin), "+{v[236:239]}"(vinA), "+{s[56:63]}"(soff), "+{s[64:67]}"(smisc), "+{s[68:75]}"(sext), "+{s[44:47]}"(srdA), "+{s[48:51]}"(srdB), "+{s[28:29]}"(sw2)
    : "{s[40:43]}"(srdX), "{s[52:55]}"(srdC), "{s[76:79]}"(srdS), "{s[80:83]}"(srdO), "{s[84:91]}"(sepi), "{s[20:23]}"(srdP), "{s[24:27]}"(srdV), "{v[246:249]}"(cprev), "{v[250:253]}"(cown), "{v[254:255]}"(sel)
    : "memory", "scc", "m0", "s92", "s93", "v0", "v1", "v2", "v3", "v4", "v5", "v6", "v7", "v8", "v9", "v10", "v11", "v12", "v13", "v14", "v15", "v16", "v17", "v18", "v19", "v20", "v21", "v22", "v23", "v24", "v25", "v26", "v27", "v28", "v29", "v30", "v31", "v32", "v33", "v34", "v35", "v36", "v37", "v38", "v39", "v40", "v41", "v42", "v43", "v44", "v45", "v46", "v47", "v48", "v49", "v50", "v51", "v52", "v53", "v54", "v55", "v56", "v57", "v58", "v59", "v60", "v61", "v62", "v63", "v64", "v65", "v66", "v67", "v68", "v69", "v70", "v71", "v72", "v73", "v74", "v75", "v76", "v77", "v78", "v79", "v80", "v81", "v82", "v83", "v84", "v85", "v86", "v87", "v88", "v89", "v90", "v91", "v92", "v93", "v94", "v95", "v96", "v97", "v98", "v99", "v100", "v101", "v102", "v103", "v104", "v105", "v106", "v107", "v108", "v109", "v110", "v111", "v112", "v113", "v114", "v115", "v116", "v117", "v118", "v119", "v120", "v121", "v122", "v123", "v124", "v125", "v126", "v127", "v128", "v129", "v130", "v131", "v132", "v133", "v134", "v135", "v136", "v137", "v138", "v139", "v140", "v141", "v142", "v143", "v144", "v145", "v146", "v147", "v148", "v149", "v150", "v151", "v152", "v153", "v154", "v155", "v156", "v157", "v158", "v159", "v160", "v161", "v162", "v163", "v164", "v165", "v166", "v167", "v168", "v169", "v170", "v171", "v172", "v173", "v174", "v175", "v176", "v177", "v178", "v179", "v180", "v181", "v182", "v183", "v184", "v185", "v186", "v187", "v188", "v189", "v190", "v191", "v192", "v193", "v194", "v195", "v196", "v197", "v198", "v199", "v200", "v201", "v202", "v203", "v204", "v205", "v206", "v207", "v208", "v209", "v210", "v211", "v212", "v213", "v214", "v215", "v216", "v217", "v218", "v219", "v220", "v221", "v222", "v223", "v240", "v241", "v242", "v243", "v244", "v245", "a0", "a1", "a2", "a3", "a4", "a5", "a6", "a7", "a8", "a9", "a10", "a11", "a12", "a13", "a14", "a15", "a16", "a17", "a18", "a19", "a20", "a21", "a22", "a23", "a24", "a25", "a26", "a27", "a28", "a29", "a30", "a31", "a32", "a33", "a34", "a35", "a36", "a37", "a38", "a39", "a40", "a41", "a42", "a43", "a44", "a45", "a46", "a47", "a48", "a49", "a50", "a51", "a52", "a53", "a54", "a55", "a56", "a57", "a58", "a59", "a60", "a61", "a62", "a63", "a64", "a65", "a66", "a67", "a68", "a69", "a70", "a71", "a72", "a73", "a74", "a75", "a76", "a77", "a78", "a79", "a80", "a81", "a82", "a83", "a84", "a85", "a86", "a87", "a88", "a89", "a90", "a91", "a92", "a93", "a94", "a95", "a96", "a97", "a98", "a99", "a100", "a101", "a102", "a103", "a104", "a105", "a106", "a107", "a108", "a109", "a110", "a111", "a112", "a113", "a114", "a115", "a116", "a117", "a118", "a119", "a120", "a121", "a122", "a123", "a124", "a125", "a126", "a127", "a128", "a129", "a130", "a131", "a132", "a133", "a134", "a135", "a136", "a137", "a138", "a139", "a140", "a141", "a142", "a143", "a144", "a145", "a146", "a147", "a148", "a149", "a150", "a151", "a152", "a153", "a154", "a155", "a156", "a157", "a158", "a159", "a160", "a161", "a162", "a163", "a164", "a165", "a166", "a167", "a168", "a169", "a170", "a171", "a172", "a173", "a174", "a175", "a176", "a177", "a178", "a179", "a180", "a181", "a182", "a183", "a184", "a185", "a186", "a187", "a188", "a189", "a190", "a191", "a192", "a193", "a194", "a195", "a196", "a197", "a198", "a199", "a200", "a201", "a202", "a203", "a204", "a205", "a206", "a207", "a208", "a209", "a210", "a211", "a212", "a213", "a214", "a215", "a216", "a217", "a218", "a219", "a220", "a221", "a222", "a223", "a224", "a225", "a226", "a227", "a228", "a229", "a230", "a231", "a232", "a233", "a234", "a235", "a236", "a237", "a238", "a239", "a240", "a241", "a242", "a243", "a244", "a245", "a246", "a247", "a248", "a249", "a250", "a251", "a252", "a253", "a254", "a255");

  } else {
    const int U = 16 + 5 * (cb >> 1), odd = cb & 1;
    const int u0 = U + 3 * odd, u1 = u0 + 1, kgH = 32 + 2 * (U + 2) + odd;
    sepi[0] = ((32 + 2 * u0 + c) * 128 + Tw) * 1024u; sepi[1] = sepi[0] + 4096; sepi[2] = ((32 + 2 * u1 + c) * 128 + Tw) * 1024u; sepi[3] = sepi[2] + 4096;
    sepi[4] = (kgH * 128 + Tw) * 1024u; sepi[5] = sepi[4] + 4096; sepi[6] = 8 * c; sepi[7] = spf[5];
  asm volatile(
    "s_waitcnt lgkmcnt(0)\n"
    "v_mbcnt_lo_u32_b32 v244, -1, 0\n"
    "v_mbcnt_hi_u32_b32 v244, -1, v244\n"
    "v_lshlrev_b32 v244, 4, v244\n"
    "v_mov_b32 v240, -1\n"
    "v_mov_b32 v241, -1\n"
    "v_mov_b32 v242, -1\n"
    "v_mov_b32 v243, -1\n"
    "v_lshlrev_b32 v245, 2, v244\n"
    "s_waitcnt vmcnt(4)\n"
    "s_barrier\n"
    "ds_read_b128 a[80:83], v224 offset:1024\n"
    "ds_read_b128 a[84:87], v224 offset:2048\n"
    "ds_read_b128 a[88:91], v224 offset:3072\n"
    "ds_read_b128 a[92:95], v224 offset:4096\n"
    "ds_read_b128 a[96:99], v224 offset:5120\n"
    "ds_read_b128 a[100:103], v224 offset:6144\n"
    "ds_read_b128 a[104:107], v224 offset:7168\n"
    "ds_read_b128 a[108:111], v224 offset:8192\n"
    "ds_read_b64_tr_b16 a[0:1], v228 offset:0\n"
    "ds_read_b64_tr_b16 a[2:3], v228 offset:512\n"
    "ds_read_b64_tr_b16 a[4:5], v229 offset:0\n"
    "ds_read_b64_tr_b16 a[6:7], v229 offset:512\n"
    "ds_read_b64_tr_b16 a[8:9], v228 offset:4096\n"
    "ds_read_b64_tr_b16 a[10:11], v228 offset:4608\n"
    "ds_read_b64_tr_b16 a[12:13], v229 offset:4096\n"
    "ds_read_b64_tr_b16 a[14:15], v229 offset:4608\n"
    "ds_read_b64_tr_b16 a[16:17], v228 offset:8192\n"
    "ds_read_b64_tr_b16 a[18:19], v228 offset:8704\n"
    "ds_read_b64_tr_b16 a[20:21], v229 offset:8192\n"
    "ds_read_b64_tr_b16 a[22:23], v229 offset:8704\n"
    "ds_read_b64_tr_b16 a[24:25], v228 offset:12288\n"
    "ds_read_b64_tr_b16 a[26:27], v228 offset:12800\n"
    "ds_read_b64_tr_b16 a[28:29], v229 offset:12288\n"
    "ds_read_b64_tr_b16 a[30:31], v229 offset:12800\n"
    "ds_read_b64_tr_b16 a[32:33], v232 offset:0\n"
    "ds_read_b64_tr_b16 a[34:35], v232 offset:512\n"
    "ds_read_b64_tr_b16 a[36:37], v233 offset:0\n"
    "ds_read_b64_tr_b16 a[38:39], v233 offset:512\n"
    "s_waitcnt lgkmcnt(0)\n"
    "v_mfma_f32_16x16x32_bf16 v[0:3], a[0:3], a[80:83], 0\n"
    "s_add_u32 m0, s63, 76800\n"
    "v_mfma_f32_16x16x32_bf16 v[4:7], a[0:3], a[84:87], 0\n"
    "buffer_load_dwordx4 v236, s[40:43], s56 offen sc1 lds\n"
    "v_mfma_f32_16x16x32_bf16 v[8:11], a[0:3], a[88:91], 0\n"
    "v_mfma_f32_16x16x32_bf16 v[12:15], a[0:3], a[92:95], 0\n"
    "v_mfma_f32_16x16x32_bf16 v[16:19], a[0:3], a[96:99], 0\n"
    "v_mfma_f32_16x16x32_bf16 v[20:23], a[0:3], a[100:103], 0\n"
    "buffer_load_dwordx4 v236, s[40:43], s56 offen offset:1024 sc1 lds\n"
    "v_mfma_f32_16x16x32_bf16 v[24:27], a[0:3], a[104:107], 0\n"
    "v_mfma_f32_16x16x32_bf16 v[28:31], a[0:3], a[108:111], 0\n"
    "v_mfma_f32_16x16x32_bf16 v[32:35], a[4:7], a[80:83], 0\n"
    "v_mfma_f32_16x16x32_bf16 v[36:39], a[4:7], a[84:87], 0\n"
    "buffer_load_dwordx4 v236, s[40:43], s56 offen offset:2048 sc1 lds\n"
    "v_mfma_f32_16x16x32_bf16 v[40:43], a[4:7], a[88:91], 0\n"
    "v_mfma_f32_16x16x32_bf16 v[44:47], a[4:7], a[92:95], 0\n"
    "v_mfma_f32_16x16x32_bf16 v[48:51], a[4:7], a[96:99], 0\n"
    "v_mfma_f32_16x16x32_bf16 v[52:55], a[4:7], a[100:103], 0\n"
    "buffer_load_dwordx4 v236, s[40:43], s56 offen offset:3072 sc1 lds\n"
    "v_mfma_f32_16x16x32_bf16 v[56:59], a[4:7], a[104:107], 0\n"
    "s_add_u32 s56, s56, 131072\n"
    "v_mfma_f32_16x16x32_bf16 v[60:63], a[4:7], a[108:111], 0\n"
    "v_mfma_f32_16x16x32_bf16 v[64:67], a[8:11], a[80:83], 0\n"
    "s_add_u32 m0, s62, 93184\n"
    "v_mfma_f32_16x16x32_bf16 v[68:71], a[8:11], a[84:87], 0\n"
    "buffer_load_dwordx4 v237, s[44:47], s57 offen lds\n"
    "v_mfma_f32_16x16x32_bf16 v[72:75], a[8:11], a[88:91], 0\n"
    "v_mfma_f32_16x16x32_bf16 v[76:79], a[8:11], a[92:95], 0\n"
    "v_mfma_f32_16x16x32_bf16 v[80:83], a[8:11], a[96:99], 0\n"
    "s_add_u32 m0, s62, 97280\n"
    "v_mfma_f32_16x16x32_bf16 v[84:87], a[8:11], a[100:103], 0\n"
    "buffer_load_dwordx4 v237, s[48:51], s57 offen lds\n"
    "v_mfma_f32_16x16x32_bf16 v[88:91], a[8:11], a[104:107], 0\n"
    "s_add_u32 s57, s57, 229376\n"
    "v_mfma_f32_16x16x32_bf16 v[92:95], a[8:11], a[108:111], 0\n"
    "v_mfma_f32_16x16x32_bf16 v[96:99], a[12:15], a[80:83], 0\n"
    "s_waitcnt vmcnt(6)\n"
    "s_barrier\n"
    "s_add_u32 m0, s62, 101376\n"
    "v_mfma_f32_16x16x32_bf16 v[100:103], a[12:15], a[84:87], 0\n"
    "buffer_load_dwordx4 v237, s[44:47], s58 offen lds\n"
    "v_mfma_f32_16x16x32_bf16 v[104:107], a[12:15], a[88:91], 0\n"
    "ds_read_b128 a[116:119], v224 offset:38912\n"
    "v_mfma_f32_16x16x32_bf16 v[108:111], a[12:15], a[92:95], 0\n"
    "ds_read_b128 a[120:123], v224 offset:39936\n"
    "v_mfma_f32_16x16x32_bf16 v[112:115], a[12:15], a[96:99], 0\n"
    "s_add_u32 m0, s62, 105472\n"
    "v_mfma_f32_16x16x32_bf16 v[116:119], a[12:15], a[100:103], 0\n"
    "buffer_load_dwordx4 v237, s[48:51], s58 offen lds\n"
    "v_mfma_f32_16x16x32_bf16 v[120:123], a[12:15], a[104:107], 0\n"
    "s_add_u32 s58, s58, 229376\n"
    "v_mfma_f32_16x16x32_bf16 v[124:127], a[12:15], a[108:111], 0\n"
    "ds_read_b128 a[124:127], v224 offset:40960\n"
    "v_mfma_f32_16x16x32_bf16 a[152:155], a[16:19], a[80:83], 0\n"
    "s_add_u32 m0, s62, 109568\n"
    "v_mfma_f32_16x16x32_bf16 a[156:159], a[16:19], a[84:87], 0\n"
    "global_load_lds_dwordx4 v[238:239], off\n"
    "v_mfma_f32_16x16x32_bf16 a[160:163], a[16:19], a[88:91], 0\n"
    "v_lshl_add_u64 v[238:239], v[238:239], 0, s[60:61]\n"
    "v_mfma_f32_16x16x32_bf16 a[164:167], a[16:19], a[92:95], 0\n"
    "ds_read_b128 a[128:131], v224 offset:41984\n"
    "v_mfma_f32_16x16x32_bf16 a[168:171], a[16:19], a[96:99], 0\n"
    "s_add_u32 m0, s63, 114688\n"
    "v_mfma_f32_16x16x32_bf16 a[172:175], a[16:19], a[100:103], 0\n"
    "buffer_load_dwordx4 v236, s[40:43], s56 offen sc1 lds\n"
    "v_mfma_f32_16x16x32_bf16 a[176:179], a[16:19], a[104:107], 0\n"
    "ds_read_b128 a[132:135], v224 offset:43008\n"
    "v_mfma_f32_16x16x32_bf16 a[180:183], a[16:19], a[108:111], 0\n"
    "ds_read_b128 a[136:139], v224 offset:44032\n"
    "v_mfma_f32_16x16x32_bf16 a[184:187], a[20:23], a[80:83], 0\n"
    "ds_read_b128 a[140:143], v224 offset:45056\n"
    "v_mfma_f32_16x16x32_bf16 a[188:191], a[20:23], a[84:87], 0\n"
    "buffer_load_dwordx4 v236, s[40:43], s56 offen offset:1024 sc1 lds\n"
    "v_mfma_f32_16x16x32_bf16 a[192:195], a[20:23], a[88:91], 0\n"
    "ds_read_b128 a[144:147], v224 offset:46080\n"
    "v_mfma_f32_16x16x32_bf16 a[196:199], a[20:23], a[92:95], 0\n"
    "ds_read_b64_tr_b16 a[40:41], v228 offset:37888\n"
    "v_mfma_f32_16x16x32_bf16 a[200:203], a[20:23], a[96:99], 0\n"
    "ds_read_b64_tr_b16 a[42:43], v228 offset:38400\n"
    "v_mfma_f32_16x16x32_bf16 a[204:207], a[20:23], a[100:103], 0\n"
    "buffer_load_dwordx4 v236, s[40:43], s56 offen offset:2048 sc1 lds\n"
    "v_mfma_f32_16x16x32_bf16 a[208:211], a[20:23], a[104:107], 0\n"
    "ds_read_b64_tr_b16 a[44:45], v229 offset:37888\n"
    "v_mfma_f32_16x16x32_bf16 a[212:215], a[20:23], a[108:111], 0\n"
    "ds_read_b64_tr_b16 a[46:47], v229 offset:38400\n"
    "v_mfma_f32_16x16x32_bf16 v[128:131], a[24:27], a[80:83], 0\n"
    "ds_read_b64_tr_b16 a[48:49], v228 offset:41984\n"
    "v_mfma_f32_16x16x32_bf16 v[132:135], a[24:27], a[84:87], 0\n"
    "buffer_load_dwordx4 v236, s[40:43], s56 offen offset:3072 sc1 lds\n"
    "v_mfma_f32_16x16x32_bf16 v[136:139], a[24:27], a[88:91], 0\n"
    "s_add_u32 s56, s56, 131072\n"
    "v_mfma_f32_16x16x32_bf16 v[140:143], a[24:27], a[92:95], 0\n"
    "ds_read_b64_tr_b16 a[50:51], v228 offset:42496\n"
    "v_mfma_f32_16x16x32_bf16 v[144:147], a[24:27], a[96:99], 0\n"
    "s_add_u32 m0, s62, 131072\n"
    "v_mfma_f32_16x16x32_bf16 v[148:151], a[24:27], a[100:103], 0\n"
    "buffer_load_dwordx4 v237, s[44:47], s57 offen lds\n"
    "v_mfma_f32_16x16x32_bf16 v[152:155], a[24:27], a[104:107], 0\n"
    "ds_read_b64_tr_b16 a[52:53], v229 offset:41984\n"
    "v_mfma_f32_16x16x32_bf16 v[156:159], a[24:27], a[108:111], 0\n"
    "ds_read_b64_tr_b16 a[54:55], v229 offset:42496\n"
    "v_mfma_f32_16x16x32_bf16 v[160:163], a[28:31], a[80:83], 0\n"
    "s_add_u32 m0, s62, 135168\n"
    "v_mfma_f32_16x16x32_bf16 v[164:167], a[28:31], a[84:87], 0\n"
    "buffer_load_dwordx4 v237, s[48:51], s57 offen lds\n"
    "v_mfma_f32_16x16x32_bf16 v[168:171], a[28:31], a[88:91], 0\n"
    "s_add_u32 s57, s57, 229376\n"
    "v_mfma_f32_16x16x32_bf16 v[172:175], a[28:31], a[92:95], 0\n"
    "ds_read_b64_tr_b16 a[56:57], v228 offset:46080\n"
    "v_mfma_f32_16x16x32_bf16 v[176:179], a[28:31], a[96:99], 0\n"
    "s_add_u32 m0, s62, 139264\n"
    "v_mfma_f32_16x16x32_bf16 v[180:183], a[28:31], a[100:103], 0\n"
    "buffer_load_dwordx4 v237, s[44:47], s58 offen lds\n"
    "v_mfma_f32_16x16x32_bf16 v[184:187], a[28:31], a[104:107], 0\n"
    "ds_read_b64_tr_b16 a[58:59], v228 offset:46592\n"
    "v_mfma_f32_16x16x32_bf16 v[188:191], a[28:31], a[108:111], 0\n"
    "ds_read_b64_tr_b16 a[60:61], v229 offset:46080\n"
    "v_mfma_f32_16x16x32_bf16 a[216:219], a[32:35], a[80:83], 0\n"
    "s_add_u32 m0, s62, 143360\n"
    "v_mfma_f32_16x16x32_bf16 a[220:223], a[32:35], a[84:87], 0\n"
    "buffer_load_dwordx4 v237, s[48:51], s58 offen lds\n"
    "v_mfma_f32_16x16x32_bf16 a[224:227], a[32:35], a[88:91], 0\n"
    "s_add_u32 s58, s58, 229376\n"
    "v_mfma_f32_16x16x32_bf16 a[228:231], a[32:35], a[92:95], 0\n"
    "ds_read_b64_tr_b16 a[62:63], v229 offset:46592\n"
    "v_mfma_f32_16x16x32_bf16 a[232:235], a[32:35], a[96:99], 0\n"
    "s_add_u32 m0, s62, 147456\n"
    "v_mfma_f32_16x16x32_bf16 a[236:239], a[32:35], a[100:103], 0\n"
    "global_load_lds_dwordx4 v[238:239], off\n"
    "v_mfma_f32_16x16x32_bf16 a[240:243], a[32:35], a[104:107], 0\n"
    "v_lshl_add_u64 v[238:239], v[238:239], 0, s[60:61]\n"
    "v_mfma_f32_16x16x32_bf16 a[244:247], a[32:35], a[108:111], 0\n"
    "ds_read_b64_tr_b16 a[64:65], v228 offset:50176\n"
    "v_mfma_f32_16x16x32_bf16 v[192:195], a[36:39], a[80:83], 0\n"
    "ds_read_b64_tr_b16 a[66:67], v228 offset:50688\n"
    "v_mfma_f32_16x16x32_bf16 v[196:199], a[36:39], a[84:87], 0\n"
    "buffer_load_dword v246, v245, s[20:23], s74 offen\n"
    "v_mfma_f32_16x16x32_bf16 v[200:203], a[36:39], a[88:91], 0\n"
    "s_add_u32 s74, s74, s91\n"
    "v_mfma_f32_16x16x32_bf16 v[204:207], a[36:39], a[92:95], 0\n"
    "ds_read_b64_tr_b16 a[68:69], v229 offset:50176\n"
    "v_mfma_f32_16x16x32_bf16 v[208:211], a[36:39], a[96:99], 0\n"
    "s_mov_b32 m0, 155648\n"
    "v_mfma_f32_16x16x32_bf16 v[212:215], a[36:39], a[100:103], 0\n"
    "global_load_lds_dwordx4 v244, s[70:71]\n"
    "v_mfma_f32_16x16x32_bf16 v[216:219], a[36:39], a[104:107], 0\n"
    "s_add_u32 s70, s70, 1024\n"
    "s_addc_u32 s71, s71, 0\n"
    "v_mfma_f32_16x16x32_bf16 v[220:223], a[36:39], a[108:111], 0\n"
    "ds_read_b64_tr_b16 a[70:71], v229 offset:50688\n"
    "ds_read_b64_tr_b16 a[72:73], v232 offset:37888\n"
    "ds_read_b64_tr_b16 a[74:75], v232 offset:38400\n"
    "ds_read_b64_tr_b16 a[76:77], v233 offset:37888\n"
    "ds_read_b64_tr_b16 a[78:79], v233 offset:38400\n"
    "s_waitcnt vmcnt(11) lgkmcnt(0)\n"
    "s_barrier\n"
    "v_mfma_f32_16x16x32_bf16 v[0:3], a[40:43], a[116:119], v[0:3]\n"
    "ds_read_b128 a[80:83], v225 offset:1024\n"
    "v_mfma_f32_16x16x32_bf16 v[4:7], a[40:43], a[120:123], v[4:7]\n"
    "ds_read_b128 a[84:87], v225 offset:2048\n"
    "v_mfma_f32_16x16x32_bf16 v[8:11], a[40:43], a[124:127], v[8:11]\n"
    "s_add_u32 m0, s63, 1024\n"
    "v_mfma_f32_16x16x32_bf16 v[12:15], a[40:43], a[128:131], v[12:15]\n"
    "buffer_load_dwordx4 v236, s[40:43], s56 offen sc1 lds\n"
    "v_mfma_f32_16x16x32_bf16 v[16:19], a[40:43], a[132:135], v[16:19]\n"
    "ds_read_b128 a[88:91], v225 offset:3072\n"
    "v_mfma_f32_16x16x32_bf16 v[20:23], a[40:43], a[136:139], v[20:23]\n"
    "ds_read_b128 a[92:95], v225 offset:4096\n"
    "v_mfma_f32_16x16x32_bf16 v[24:27], a[40:43], a[140:143], v[24:27]\n"
    "ds_read_b128 a[96:99], v225 offset:5120\n"
    "v_mfma_f32_16x16x32_bf16 v[28:31], a[40:43], a[144:147], v[28:31]\n"
    "buffer_load_dwordx4 v236, s[40:43], s56 offen offset:1024 sc1 lds\n"
    "v_mfma_f32_16x16x32_bf16 v[32:35], a[44:47], a[116:119], v[32:35]\n"
    "ds_read_b128 a[100:103], v225 offset:6144\n"
    "v_mfma_f32_16x16x32_bf16 v[36:39], a[44:47], a[120:123], v[36:39]\n"
    "ds_read_b128 a[104:107], v225 offset:7168\n"
    "v_mfma_f32_16x16x32_bf16 v[40:43], a[44:47], a[124:127], v[40:43]\n"
    "ds_read_b128 a[108:111], v225 offset:8192\n"
    "v_mfma_f32_16x16x32_bf16 v[44:47], a[44:47], a[128:131], v[44:47]\n"
    "buffer_load_dwordx4 v236, s[40:43], s56 offen offset:2048 sc1 lds\n"
    "v_mfma_f32_16x16x32_bf16 v[48:51], a[44:47], a[132:135], v[48:51]\n"
    "ds_read_b64_tr_b16 a[0:1], v230 offset:0\n"
    "v_mfma_f32_16x16x32_bf16 v[52:55], a[44:47], a[136:139], v[52:55]\n"
    "ds_read_b64_tr_b16 a[2:3], v230 offset:512\n"
    "v_mfma_f32_16x16x32_bf16 v[56:59], a[44:47], a[140:143], v[56:59]\n"
    "ds_read_b64_tr_b16 a[4:5], v231 offset:0\n"
    "v_mfma_f32_16x16x32_bf16 v[60:63], a[44:47], a[144:147], v[60:63]\n"
    "buffer_load_dwordx4 v236, s[40:43], s56 offen offset:3072 sc1 lds\n"
    "v_mfma_f32_16x16x32_bf16 v[64:67], a[48:51], a[116:119], v[64:67]\n"
    "s_add_u32 s56, s56, 131072\n"
    "v_mfma_f32_16x16x32_bf16 v[68:71], a[48:51], a[120:123], v[68:71]\n"
    "ds_read_b64_tr_b16 a[6:7], v231 offset:512\n"
    "v_mfma_f32_16x16x32_bf16 v[72:75], a[48:51], a[124:127], v[72:75]\n"
    "s_add_u32 m0, s62, 17408\n"
    "v_mfma_f32_16x16x32_bf16 v[76:79], a[48:51], a[128:131], v[76:79]\n"
    "buffer_load_dwordx4 v237, s[44:47], s57 offen lds\n"
    "v_mfma_f32_16x16x32_bf16 v[80:83], a[48:51], a[132:135], v[80:83]\n"
    "ds_read_b64_tr_b16 a[8:9], v230 offset:4096\n"
    "v_mfma_f32_16x16x32_bf16 v[84:87], a[48:51], a[136:139], v[84:87]\n"
    "ds_read_b64_tr_b16 a[10:11], v230 offset:4608\n"
    "v_mfma_f32_16x16x32_bf16 v[88:91], a[48:51], a[140:143], v[88:91]\n"
    "s_add_u32 m0, s62, 21504\n"
    "v_mfma_f32_16x16x32_bf16 v[92:95], a[48:51], a[144:147], v[92:95]\n"
    "buffer_load_dwordx4 v237, s[48:51], s57 offen lds\n"
    "v_mfma_f32_16x16x32_bf16 v[96:99], a[52:55], a[116:119], v[96:99]\n"
    "s_add_u32 s57, s57, 229376\n"
    "v_mfma_f32_16x16x32_bf16 v[100:103], a[52:55], a[120:123], v[100:103]\n"
    "ds_read_b64_tr_b16 a[12:13], v231 offset:4096\n"
    "v_mfma_f32_16x16x32_bf16 v[104:107], a[52:55], a[124:127], v[104:107]\n"
    "s_add_u32 m0, s62, 25600\n"
    "v_mfma_f32_16x16x32_bf16 v[108:111], a[52:55], a[128:131], v[108:111]\n"
    "buffer_load_dwordx4 v237, s[44:47], s58 offen lds\n"
    "v_mfma_f32_16x16x32_bf16 v[112:115], a[52:55], a[132:135], v[112:115]\n"
    "ds_read_b64_tr_b16 a[14:15], v231 offset:4608\n"
    "v_mfma_f32_16x16x32_bf16 v[116:119], a[52:55], a[136:139], v[116:119]\n"
    "ds_read_b64_tr_b16 a[16:17], v230 offset:8192\n"
    "v_mfma_f32_16x16x32_bf16 v[120:123], a[52:55], a[140:143], v[120:123]\n"
    "s_add_u32 m0, s62, 29696\n"
    "v_mfma_f32_16x16x32_bf16 v[124:127], a[52:55], a[144:147], v[124:127]\n"
    "buffer_load_dwordx4 v237, s[48:51], s58 offen lds\n"
    "v_mfma_f32_16x16x32_bf16 a[152:155], a[56:59], a[116:119], a[152:155]\n"
    "s_add_u32 s58, s58, 229376\n"
    "v_mfma_f32_16x16x32_bf16 a[156:159], a[56:59], a[120:123], a[156:159]\n"
    "ds_read_b64_tr_b16 a[18:19], v230 offset:8704\n"
    "v_mfma_f32_16x16x32_bf16 a[160:163], a[56:59], a[124:127], a[160:163]\n"
    "s_add_u32 m0, s62, 33792\n"
    "v_mfma_f32_16x16x32_bf16 a[164:167], a[56:59], a[128:131], a[164:167]\n"
    "global_load_lds_dwordx4 v[238:239], off\n"
    "v_mfma_f32_16x16x32_bf16 a[168:171], a[56:59], a[132:135], a[168:171]\n"
    "v_lshl_add_u64 v[238:239], v[238:239], 0, s[60:61]\n"
    "v_mfma_f32_16x16x32_bf16 a[172:175], a[56:59], a[136:139], a[172:175]\n"
    "ds_read_b64_tr_b16 a[20:21], v231 offset:8192\n"
    "v_mfma_f32_16x16x32_bf16 a[176:179], a[56:59], a[140:143], a[176:179]\n"
    "s_add_u32 m0, s63, 38912\n"
    "v_mfma_f32_16x16x32_bf16 a[180:183], a[56:59], a[144:147], a[180:183]\n"
    "buffer_load_dwordx4 v236, s[40:43], s56 offen sc1 lds\n"
    "v_mfma_f32_16x16x32_bf16 a[184:187], a[60:63], a[116:119], a[184:187]\n"
    "ds_read_b64_tr_b16 a[22:23], v231 offset:8704\n"
    "v_mfma_f32_16x16x32_bf16 a[188:191], a[60:63], a[120:123], a[188:191]\n"
    "ds_read_b64_tr_b16 a[24:25], v230 offset:12288\n"
    "v_mfma_f32_16x16x32_bf16 a[192:195], a[60:63], a[124:127], a[192:195]\n"
    "ds_read_b64_tr_b16 a[26:27], v230 offset:12800\n"
    "v_mfma_f32_16x16x32_bf16 a[196:199], a[60:63], a[128:131], a[196:199]\n"
    "buffer_load_dwordx4 v236, s[40:43], s56 offen offset:1024 sc1 lds\n"
    "v_mfma_f32_16x16x32_bf16 a[200:203], a[60:63], a[132:135], a[200:203]\n"
    "ds_read_b64_tr_b16 a[28:29], v231 offset:12288\n"
    "v_mfma_f32_16x16x32_bf16 a[204:207], a[60:63], a[136:139], a[204:207]\n"
    "ds_read_b64_tr_b16 a[30:31], v231 offset:12800\n"
    "v_mfma_f32_16x16x32_bf16 a[208:211], a[60:63], a[140:143], a[208:211]\n"
    "ds_read_b64_tr_b16 a[32:33], v234 offset:0\n"
    "v_mfma_f32_16x16x32_bf16 a[212:215], a[60:63], a[144:147], a[212:215]\n"
    "buffer_load_dwordx4 v236, s[40:43], s56 offen offset:2048 sc1 lds\n"
    "v_mfma_f32_16x16x32_bf16 v[128:131], a[64:67], a[116:119], v[128:131]\n"
    "ds_read_b64_tr_b16 a[34:35], v234 offset:512\n"
    "v_mfma_f32_16x16x32_bf16 v[132:135], a[64:67], a[120:123], v[132:135]\n"
    "ds_read_b64_tr_b16 a[36:37], v235 offset:0\n"
    "v_mfma_f32_16x16x32_bf16 v[136:139], a[64:67], a[124:127], v[136:139]\n"
    "ds_read_b64_tr_b16 a[38:39], v235 offset:512\n"
    "v_mfma_f32_16x16x32_bf16 v[140:143], a[64:67], a[128:131], v[140:143]\n"
    "buffer_load_dwordx4 v236, s[40:43], s56 offen offset:3072 sc1 lds\n"
    "v_mfma_f32_16x16x32_bf16 v[144:147], a[64:67], a[132:135], v[144:147]\n"
    "s_add_u32 s56, s56, 131072\n"
    "v_mfma_f32_16x16x32_bf16 v[148:151], a[64:67], a[136:139], v[148:151]\n"
    "v_mfma_f32_16x16x32_bf16 v[152:155], a[64:67], a[140:143], v[152:155]\n"
    "s_add_u32 m0, s62, 55296\n"
    "v_mfma_f32_16x16x32_bf16 v[156:159], a[64:67], a[144:147], v[156:159]\n"
    "buffer_load_dwordx4 v237, s[44:47], s57 offen lds\n"
    "v_mfma_f32_16x16x32_bf16 v[160:163], a[68:71], a[116:119], v[160:163]\n"
    "v_mfma_f32_16x16x32_bf16 v[164:167], a[68:71], a[120:123], v[164:167]\n"
    "v_mfma_f32_16x16x32_bf16 v[168:171], a[68:71], a[124:127], v[168:171]\n"
    "s_add_u32 m0, s62, 59392\n"
    "v_mfma_f32_16x16x32_bf16 v[172:175], a[68:71], a[128:131], v[172:175]\n"
    "buffer_load_dwordx4 v237, s[48:51], s57 offen lds\n"
    "v_mfma_f32_16x16x32_bf16 v[176:179], a[68:71], a[132:135], v[176:179]\n"
    "s_add_u32 s57, s57, 229376\n"
    "v_mfma_f32_16x16x32_bf16 v[180:183], a[68:71], a[136:139], v[180:183]\n"
    "v_mfma_f32_16x16x32_bf16 v[184:187], a[68:71], a[140:143], v[184:187]\n"
    "s_add_u32 m0, s62, 63488\n"
    "v_mfma_f32_16x16x32_bf16 v[188:191], a[68:71], a[144:147], v[188:191]\n"
    "buffer_load_dwordx4 v237, s[44:47], s58 offen lds\n"
    "v_mfma_f32_16x16x32_bf16 a[216:219], a[72:75], a[116:119], a[216:219]\n"
    "v_mfma_f32_16x16x32_bf16 a[220:223], a[72:75], a[120:123], a[220:223]\n"
    "v_mfma_f32_16x16x32_bf16 a[224:227], a[72:75], a[124:127], a[224:227]\n"
    "s_add_u32 m0, s62, 67584\n"
    "v_mfma_f32_16x16x32_bf16 a[228:231], a[72:75], a[128:131], a[228:231]\n"
    "buffer_load_dwordx4 v237, s[48:51], s58 offen lds\n"
    "v_mfma_f32_16x16x32_bf16 a[232:235], a[72:75], a[132:135], a[232:235]\n"
    "s_add_u32 s58, s58, 229376\n"
    "v_mfma_f32_16x16x32_bf16 a[236:239], a[72:75], a[136:139], a[236:239]\n"
    "v_mfma_f32_16x16x32_bf16 a[240:243], a[72:75], a[140:143], a[240:243]\n"
    "s_add_u32 m0, s62, 71680\n"
    "v_mfma_f32_16x16x32_bf16 a[244:247], a[72:75], a[144:147], a[244:247]\n"
    "global_load_lds_dwordx4 v[238:239], off\n"
    "v_mfma_f32_16x16x32_bf16 v[192:195], a[76:79], a[116:119], v[192:195]\n"
    "v_lshl_add_u64 v[238:239], v[238:239], 0, s[60:61]\n"
    "v_mfma_f32_16x16x32_bf16 v[196:199], a[76:79], a[120:123], v[196:199]\n"
    "v_mfma_f32_16x16x32_bf16 v[200:203], a[76:79], a[124:127], v[200:203]\n"
    "v_mfma_f32_16x16x32_bf16 v[204:207], a[76:79], a[128:131], v[204:207]\n"
    "v_mfma_f32_16x16x32_bf16 v[208:211], a[76:79], a[132:135], v[208:211]\n"
    "v_mfma_f32_16x16x32_bf16 v[212:215], a[76:79], a[136:139], v[212:215]\n"
    "v_mfma_f32_16x16x32_bf16 v[216:219], a[76:79], a[140:143], v[216:219]\n"
    "v_mfma_f32_16x16x32_bf16 v[220:223], a[76:79], a[144:147], v[220:223]\n"
    "s_waitcnt vmcnt(20) lgkmcnt(0)\n"
    "s_barrier\n"
    "v_mfma_f32_16x16x32_bf16 v[0:3], a[0:3], a[80:83], v[0:3]\n"
    "ds_read_b128 a[116:119], v225 offset:38912\n"
    "v_mfma_f32_16x16x32_bf16 v[4:7], a[0:3], a[84:87], v[4:7]\n"
    "ds_read_b128 a[120:123], v225 offset:39936\n"
    "v_mfma_f32_16x16x32_bf16 v[8:11], a[0:3], a[88:91], v[8:11]\n"
    "s_add_u32 m0, s63, 76800\n"
    "v_mfma_f32_16x16x32_bf16 v[12:15], a[0:3], a[92:95], v[12:15]\n"
    "buffer_load_dwordx4 v236, s[40:43], s56 offen sc1 lds\n"
    "v_mfma_f32_16x16x32_bf16 v[16:19], a[0:3], a[96:99], v[16:19]\n"
    "ds_read_b128 a[124:127], v225 offset:40960\n"
    "v_mfma_f32_16x16x32_bf16 v[20:23], a[0:3], a[100:103], v[20:23]\n"
    "ds_read_b128 a[128:131], v225 offset:41984\n"
    "v_mfma_f32_16x16x32_bf16 v[24:27], a[0:3], a[104:107], v[24:27]\n"
    "ds_read_b128 a[132:135], v225 offset:43008\n"
    "v_mfma_f32_16x16x32_bf16 v[28:31], a[0:3], a[108:111], v[28:31]\n"
    "ds_read_b128 a[136:139], v225 offset:44032\n"
    "v_mfma_f32_16x16x32_bf16 v[32:35], a[4:7], a[80:83], v[32:35]\n"
    "ds_read_b128 a[140:143], v225 offset:45056\n"
    "v_mfma_f32_16x16x32_bf16 v[36:39], a[4:7], a[84:87], v[36:39]\n"
    "buffer_load_dwordx4 v236, s[40:43], s56 offen offset:1024 sc1 lds\n"
    "v_mfma_f32_16x16x32_bf16 v[40:43], a[4:7], a[88:91], v[40:43]\n"
    "ds_read_b128 a[144:147], v225 offset:46080\n"
    "v_mfma_f32_16x16x32_bf16 v[44:47], a[4:7], a[92:95], v[44:47]\n"
    "ds_read_b64_tr_b16 a[40:41], v230 offset:37888\n"
    "v_mfma_f32_16x16x32_bf16 v[48:51], a[4:7], a[96:99], v[48:51]\n"
    "ds_read_b64_tr_b16 a[42:43], v230 offset:38400\n"
    "v_mfma_f32_16x16x32_bf16 v[52:55], a[4:7], a[100:103], v[52:55]\n"
    "ds_read_b64_tr_b16 a[44:45], v231 offset:37888\n"
    "v_mfma_f32_16x16x32_bf16 v[56:59], a[4:7], a[104:107], v[56:59]\n"
    "ds_read_b64_tr_b16 a[46:47], v231 offset:38400\n"
    "v_mfma_f32_16x16x32_bf16 v[60:63], a[4:7], a[108:111], v[60:63]\n"
    "buffer_load_dwordx4 v236, s[40:43], s56 offen offset:2048 sc1 lds\n"
    "v_mfma_f32_16x16x32_bf16 v[64:67], a[8:11], a[80:83], v[64:67]\n"
    "ds_read_b64_tr_b16 a[48:49], v230 offset:41984\n"
    "v_mfma_f32_16x16x32_bf16 v[68:71], a[8:11], a[84:87], v[68:71]\n"
    "ds_read_b64_tr_b16 a[50:51], v230 offset:42496\n"
    "v_mfma_f32_16x16x32_bf16 v[72:75], a[8:11], a[88:91], v[72:75]\n"
    "ds_read_b64_tr_b16 a[52:53], v231 offset:41984\n"
    "v_mfma_f32_16x16x32_bf16 v[76:79], a[8:11], a[92:95], v[76:79]\n"
    "ds_read_b64_tr_b16 a[54:55], v231 offset:42496\n"
    "v_mfma_f32_16x16x32_bf16 v[80:83], a[8:11], a[96:99], v[80:83]\n"
    "ds_read_b64_tr_b16 a[56:57], v230 offset:46080\n"
    "v_mfma_f32_16x16x32_bf16 v[84:87], a[8:11], a[100:103], v[84:87]\n"
    "buffer_load_dwordx4 v236, s[40:43], s56 offen offset:3072 sc1 lds\n"
    "v_mfma_f32_16x16x32_bf16 v[88:91], a[8:11], a[104:107], v[88:91]\n"
    "s_add_u32 s56, s56, 131072\n"
    "v_mfma_f32_16x16x32_bf16 v[92:95], a[8:11], a[108:111], v[92:95]\n"
    "ds_read_b64_tr_b16 a[58:59], v230 offset:46592\n"
    "v_mfma_f32_16x16x32_bf16 v[96:99], a[12:15], a[80:83], v[96:99]\n"
    "ds_read_b64_tr_b16 a[60:61], v231 offset:46080\n"
    "v_mfma_f32_16x16x32_bf16 v[100:103], a[12:15], a[84:87], v[100:103]\n"
    "ds_read_b64_tr_b16 a[62:63], v231 offset:46592\n"
    "v_mfma_f32_16x16x32_bf16 v[104:107], a[12:15], a[88:91], v[104:107]\n"
    "s_add_u32 m0, s62, 93184\n"
    "v_mfma_f32_16x16x32_bf16 v[108:111], a[12:15], a[92:95], v[108:111]\n"
    "buffer_load_dwordx4 v237, s[44:47], s57 offen lds\n"
    "v_mfma_f32_16x16x32_bf16 v[112:115], a[12:15], a[96:99], v[112:115]\n"
    "ds_read_b64_tr_b16 a[64:65], v230 offset:50176\n"
    "v_mfma_f32_16x16x32_bf16 v[116:119], a[12:15], a[100:103], v[116:119]\n"
    "ds_read_b64_tr_b16 a[66:67], v230 offset:50688\n"
    "v_mfma_f32_16x16x32_bf16 v[120:123], a[12:15], a[104:107], v[120:123]\n"
    "ds_read_b64_tr_b16 a[68:69], v231 offset:50176\n"
    "v_mfma_f32_16x16x32_bf16 v[124:127], a[12:15], a[108:111], v[124:127]\n"
    "ds_read_b64_tr_b16 a[70:71], v231 offset:50688\n"
    "v_mfma_f32_16x16x32_bf16 a[152:155], a[16:19], a[80:83], a[152:155]\n"
    "s_add_u32 m0, s62, 97280\n"
    "v_mfma_f32_16x16x32_bf16 a[156:159], a[16:19], a[84:87], a[156:159]\n"
    "buffer_load_dwordx4 v237, s[48:51], s57 offen lds\n"
    "v_mfma_f32_16x16x32_bf16 a[160:163], a[16:19], a[88:91], a[160:163]\n"
    "s_add_u32 s57, s57, 229376\n"
    "v_mfma_f32_16x16x32_bf16 a[164:167], a[16:19], a[92:95], a[164:167]\n"
    "ds_read_b64_tr_b16 a[72:73], v234 offset:37888\n"
    "v_mfma_f32_16x16x32_bf16 a[168:171], a[16:19], a[96:99], a[168:171]\n"
    "ds_read_b64_tr_b16 a[74:75], v234 offset:38400\n"
    "v_mfma_f32_16x16x32_bf16 a[172:175], a[16:19], a[100:103], a[172:175]\n"
    "ds_read_b64_tr_b16 a[76:77], v235 offset:37888\n"
    "v_mfma_f32_16x16x32_bf16 a[176:179], a[16:19], a[104:107], a[176:179]\n"
    "s_add_u32 m0, s62, 101376\n"
    "v_mfma_f32_16x16x32_bf16 a[180:183], a[16:19], a[108:111], a[180:183]\n"
    "buffer_load_dwordx4 v237, s[44:47], s58 offen lds\n"
    "v_mfma_f32_16x16x32_bf16 a[184:187], a[20:23], a[80:83], a[184:187]\n"
    "ds_read_b64_tr_b16 a[78:79], v235 offset:38400\n"
    "v_mfma_f32_16x16x32_bf16 a[188:191], a[20:23], a[84:87], a[188:191]\n"
    "v_mfma_f32_16x16x32_bf16 a[192:195], a[20:23], a[88:91], a[192:195]\n"
    "v_mfma_f32_16x16x32_bf16 a[196:199], a[20:23], a[92:95], a[196:199]\n"
    "v_mfma_f32_16x16x32_bf16 a[200:203], a[20:23], a[96:99], a[200:203]\n"
    "s_add_u32 m0, s62, 105472\n"
    "v_mfma_f32_16x16x32_bf16 a[204:207], a[20:23], a[100:103], a[204:207]\n"
    "buffer_load_dwordx4 v237, s[48:51], s58 offen lds\n"
    "v_mfma_f32_16x16x32_bf16 a[208:211], a[20:23], a[104:107], a[208:211]\n"
    "s_add_u32 s58, s58, 229376\n"
    "v_mfma_f32_16x16x32_bf16 a[212:215], a[20:23], a[108:111], a[212:215]\n"
    "v_mfma_f32_16x16x32_bf16 v[128:131], a[24:27], a[80:83], v[128:131]\n"
    "v_mfma_f32_16x16x32_bf16 v[132:135], a[24:27], a[84:87], v[132:135]\n"
    "v_mfma_f32_16x16x32_bf16 v[136:139], a[24:27], a[88:91], v[136:139]\n"
    "s_add_u32 m0, s62, 109568\n"
    "v_mfma_f32_16x16x32_bf16 v[140:143], a[24:27], a[92:95], v[140:143]\n"
    "global_load_lds_dwordx4 v[238:239], off\n"
    "v_mfma_f32_16x16x32_bf16 v[144:147], a[24:27], a[96:99], v[144:147]\n"
    "v_lshl_add_u64 v[238:239], v[238:239], 0, s[60:61]\n"
    "v_mfma_f32_16x16x32_bf16 v[148:151], a[24:27], a[100:103], v[148:151]\n"
    "v_mfma_f32_16x16x32_bf16 v[152:155], a[24:27], a[104:107], v[152:155]\n"
    "v_mfma_f32_16x16x32_bf16 v[156:159], a[24:27], a[108:111], v[156:159]\n"
    "v_mfma_f32_16x16x32_bf16 v[160:163], a[28:31], a[80:83], v[160:163]\n"
    "v_mfma_f32_16x16x32_bf16 v[164:167], a[28:31], a[84:87], v[164:167]\n"
    "buffer_store_dwordx4 v[240:243], v244, s[76:79], s75 offen offset:0 sc1\n"
    "v_mfma_f32_16x16x32_bf16 v[168:171], a[28:31], a[88:91], v[168:171]\n"
    "v_mfma_f32_16x16x32_bf16 v[172:175], a[28:31], a[92:95], v[172:175]\n"
    "v_mfma_f32_16x16x32_bf16 v[176:179], a[28:31], a[96:99], v[176:179]\n"
    "v_mfma_f32_16x16x32_bf16 v[180:183], a[28:31], a[100:103], v[180:183]\n"
    "v_mfma_f32_16x16x32_bf16 v[184:187], a[28:31], a[104:107], v[184:187]\n"
    "v_mfma_f32_16x16x32_bf16 v[188:191], a[28:31], a[108:111], v[188:191]\n"
    "buffer_store_dwordx4 v[240:243], v244, s[76:79], s75 offen offset:1024 sc1\n"
    "v_mfma_f32_16x16x32_bf16 a[216:219], a[32:35], a[80:83], a[216:219]\n"
    "v_mfma_f32_16x16x32_bf16 a[220:223], a[32:35], a[84:87], a[220:223]\n"
    "v_mfma_f32_16x16x32_bf16 a[224:227], a[32:35], a[88:91], a[224:227]\n"
    "v_mfma_f32_16x16x32_bf16 a[228:231], a[32:35], a[92:95], a[228:231]\n"
    "v_mfma_f32_16x16x32_bf16 a[232:235], a[32:35], a[96:99], a[232:235]\n"
    "v_mfma_f32_16x16x32_bf16 a[236:239], a[32:35], a[100:103], a[236:239]\n"
    "buffer_store_dwordx4 v[240:243], v244, s[76:79], s75 offen offset:2048 sc1\n"
    "v_mfma_f32_16x16x32_bf16 a[240:243], a[32:35], a[104:107], a[240:243]\n"
    "v_mfma_f32_16x16x32_bf16 a[244:247], a[32:35], a[108:111], a[244:247]\n"
    "v_mfma_f32_16x16x32_bf16 v[192:195], a[36:39], a[80:83], v[192:195]\n"
    "v_mfma_f32_16x16x32_bf16 v[196:199], a[36:39], a[84:87], v[196:199]\n"
    "v_mfma_f32_16x16x32_bf16 v[200:203], a[36:39], a[88:91], v[200:203]\n"
    "v_mfma_f32_16x16x32_bf16 v[204:207], a[36:39], a[92:95], v[204:207]\n"
    "buffer_store_dwordx4 v[240:243], v244, s[76:79], s75 offen offset:3072 sc1\n"
    "v_mfma_f32_16x16x32_bf16 v[208:211], a[36:39], a[96:99], v[208:211]\n"
    "s_add_u32 s75, s75, 4096\n"
    "v_mfma_f32_16x16x32_bf16 v[212:215], a[36:39], a[100:103], v[212:215]\n"
    "v_mfma_f32_16x16x32_bf16 v[216:219], a[36:39], a[104:107], v[216:219]\n"
    "v_mfma_f32_16x16x32_bf16 v[220:223], a[36:39], a[108:111], v[220:223]\n"
    "s_waitcnt vmcnt(22) lgkmcnt(0)\n"
    "s_barrier\n"
    "v_mfma_f32_16x16x32_bf16 v[0:3], a[40:43], a[116:119], v[0:3]\n"
    "ds_read_b128 a[80:83], v224 offset:1024\n"
    "v_mfma_f32_16x16x32_bf16 v[4:7], a[40:43], a[120:123], v[4:7]\n"
    "ds_read_b128 a[84:87], v224 offset:2048\n"
    "v_mfma_f32_16x16x32_bf16 v[8:11], a[40:43], a[124:127], v[8:11]\n"
    "s_add_u32 m0, s63, 114688\n"
    "v_mfma_f32_16x16x32_bf16 v[12:15], a[40:43], a[128:131], v[12:15]\n"
    "buffer_load_dwordx4 v236, s[40:43], s56 offen sc1 lds\n"
    "v_mfma_f32_16x16x32_bf16 v[16:19], a[40:43], a[132:135], v[16:19]\n"
    "ds_read_b128 a[88:91], v224 offset:3072\n"
    "v_mfma_f32_16x16x32_bf16 v[20:23], a[40:43], a[136:139], v[20:23]\n"
    "ds_read_b128 a[92:95], v224 offset:4096\n"
    "v_mfma_f32_16x16x32_bf16 v[24:27], a[40:43], a[140:143], v[24:27]\n"
    "ds_read_b128 a[96:99], v224 offset:5120\n"
    "v_mfma_f32_16x16x32_bf16 v[28:31], a[40:43], a[144:147], v[28:31]\n"
    "ds_read_b128 a[100:103], v224 offset:6144\n"
    "v_mfma_f32_16x16x32_bf16 v[32:35], a[44:47], a[116:119], v[32:35]\n"
    "ds_read_b128 a[104:107], v224 offset:7168\n"
    "v_mfma_f32_16x16x32_bf16 v[36:39], a[44:47], a[120:123], v[36:39]\n"
    "ds_read_b128 a[108:111], v224 offset:8192\n"
    "v_mfma_f32_16x16x32_bf16 v[40:43], a[44:47], a[124:127], v[40:43]\n"
    "buffer_load_dwordx4 v236, s[40:43], s56 offen offset:1024 sc1 lds\n"
    "v_mfma_f32_16x16x32_bf16 v[44:47], a[44:47], a[128:131], v[44:47]\n"
    "ds_read_b64_tr_b16 a[0:1], v228 offset:0\n"
    "v_mfma_f32_16x16x32_bf16 v[48:51], a[44:47], a[132:135], v[48:51]\n"
    "ds_read_b64_tr_b16 a[2:3], v228 offset:512\n"
    "v_mfma_f32_16x16x32_bf16 v[52:55], a[44:47], a[136:139], v[52:55]\n"
    "ds_read_b64_tr_b16 a[4:5], v229 offset:0\n"
    "v_mfma_f32_16x16x32_bf16 v[56:59], a[44:47], a[140:143], v[56:59]\n"
    "ds_read_b64_tr_b16 a[6:7], v229 offset:512\n"
    "v_mfma_f32_16x16x32_bf16 v[60:63], a[44:47], a[144:147], v[60:63]\n"
    "ds_read_b64_tr_b16 a[8:9], v228 offset:4096\n"
    "v_mfma_f32_16x16x32_bf16 v[64:67], a[48:51], a[116:119], v[64:67]\n"
    "ds_read_b64_tr_b16 a[10:11], v228 offset:4608\n"
    "v_mfma_f32_16x16x32_bf16 v[68:71], a[48:51], a[120:123], v[68:71]\n"
    "buffer_load_dwordx4 v236, s[40:43], s56 offen offset:2048 sc1 lds\n"
    "v_mfma_f32_16x16x32_bf16 v[72:75], a[48:51], a[124:127], v[72:75]\n"
    "ds_read_b64_tr_b16 a[12:13], v229 offset:4096\n"
    "v_mfma_f32_16x16x32_bf16 v[76:79], a[48:51], a[128:131], v[76:79]\n"
    "ds_read_b64_tr_b16 a[14:15], v229 offset:4608\n"
    "v_mfma_f32_16x16x32_bf16 v[80:83], a[48:51], a[132:135], v[80:83]\n"
    "ds_read_b64_tr_b16 a[16:17], v228 offset:8192\n"
    "v_mfma_f32_16x16x32_bf16 v[84:87], a[48:51], a[136:139], v[84:87]\n"
    "ds_read_b64_tr_b16 a[18:19], v228 offset:8704\n"
    "v_mfma_f32_16x16x32_bf16 v[88:91], a[48:51], a[140:143], v[88:91]\n"
    "ds_read_b64_tr_b16 a[20:21], v229 offset:8192\n"
    "v_mfma_f32_16x16x32_bf16 v[92:95], a[48:51], a[144:147], v[92:95]\n"
    "ds_read_b64_tr_b16 a[22:23], v229 offset:8704\n"
    "v_mfma_f32_16x16x32_bf16 v[96:99], a[52:55], a[116:119], v[96:99]\n"
    "buffer_load_dwordx4 v236, s[40:43], s56 offen offset:3072 sc1 lds\n"
    "v_mfma_f32_16x16x32_bf16 v[100:103], a[52:55], a[120:123], v[100:103]\n"
    "s_add_u32 s56, s56, 131072\n"
    "v_mfma_f32_16x16x32_bf16 v[104:107], a[52:55], a[124:127], v[104:107]\n"
    "ds_read_b64_tr_b16 a[24:25], v228 offset:12288\n"
    "v_mfma_f32_16x16x32_bf16 v[108:111], a[52:55], a[128:131], v[108:111]\n"
    "ds_read_b64_tr_b16 a[26:27], v228 offset:12800\n"
    "v_mfma_f32_16x16x32_bf16 v[112:115], a[52:55], a[132:135], v[112:115]\n"
    "ds_read_b64_tr_b16 a[28:29], v229 offset:12288\n"
    "v_mfma_f32_16x16x32_bf16 v[116:119], a[52:55], a[136:139], v[116:119]\n"
    "ds_read_b64_tr_b16 a[30:31], v229 offset:12800\n"
    "v_mfma_f32_16x16x32_bf16 v[120:123], a[52:55], a[140:143], v[120:123]\n"
    "s_add_u32 m0, s62, 131072\n"
    "v_mfma_f32_16x16x32_bf16 v[124:127], a[52:55], a[144:147], v[124:127]\n"
    "buffer_load_dwordx4 v237, s[44:47], s57 offen lds\n"
    "v_mfma_f32_16x16x32_bf16 a[152:155], a[56:59], a[116:119], a[152:155]\n"
    "ds_read_b64_tr_b16 a[32:33], v232 offset:0\n"
    "v_mfma_f32_16x16x32_bf16 a[156:159], a[56:59], a[120:123], a[156:159]\n"
    "ds_read_b64_tr_b16 a[34:35], v232 offset:512\n"
    "v_mfma_f32_16x16x32_bf16 a[160:163], a[56:59], a[124:127], a[160:163]\n"
    "ds_read_b64_tr_b16 a[36:37], v233 offset:0\n"
    "v_mfma_f32_16x16x32_bf16 a[164:167], a[56:59], a[128:131], a[164:167]\n"
    "ds_read_b64_tr_b16 a[38:39], v233 offset:512\n"
    "v_mfma_f32_16x16x32_bf16 a[168:171], a[56:59], a[132:135], a[168:171]\n"
    "v_mfma_f32_16x16x32_bf16 a[172:175], a[56:59], a[136:139], a[172:175]\n"
    "s_add_u32 m0, s62, 135168\n"
    "v_mfma_f32_16x16x32_bf16 a[176:179], a[56:59], a[140:143], a[176:179]\n"
    "buffer_load_dwordx4 v237, s[48:51], s57 offen lds\n"
    "v_mfma_f32_16x16x32_bf16 a[180:183], a[56:59], a[144:147], a[180:183]\n"
    "s_add_u32 s57, s57, 229376\n"
    "v_mfma_f32_16x16x32_bf16 a[184:187], a[60:63], a[116:119], a[184:187]\n"
    "v_mfma_f32_16x16x32_bf16 a[188:191], a[60:63], a[120:123], a[188:191]\n"
    "v_mfma_f32_16x16x32_bf16 a[192:195], a[60:63], a[124:127], a[192:195]\n"
    "v_mfma_f32_16x16x32_bf16 a[196:199], a[60:63], a[128:131], a[196:199]\n"
    "v_mfma_f32_16x16x32_bf16 a[200:203], a[60:63], a[132:135], a[200:203]\n"
    "s_add_u32 m0, s62, 139264\n"
    "v_mfma_f32_16x16x32_bf16 a[204:207], a[60:63], a[136:139], a[204:207]\n"
    "buffer_load_dwordx4 v237, s[44:47], s58 offen lds\n"
    "v_mfma_f32_16x16x32_bf16 a[208:211], a[60:63], a[140:143], a[208:211]\n"
    "v_mfma_f32_16x16x32_bf16 a[212:215], a[60:63], a[144:147], a[212:215]\n"
    "v_mfma_f32_16x16x32_bf16 v[128:131], a[64:67], a[116:119], v[128:131]\n"
    "v_mfma_f32_16x16x32_bf16 v[132:135], a[64:67], a[120:123], v[132:135]\n"
    "v_mfma_f32_16x16x32_bf16 v[136:139], a[64:67], a[124:127], v[136:139]\n"
    "v_mfma_f32_16x16x32_bf16 v[140:143], a[64:67], a[128:131], v[140:143]\n"
    "s_add_u32 m0, s62, 143360\n"
    "v_mfma_f32_16x16x32_bf16 v[144:147], a[64:67], a[132:135], v[144:147]\n"
    "buffer_load_dwordx4 v237, s[48:51], s58 offen lds\n"
    "v_mfma_f32_16x16x32_bf16 v[148:151], a[64:67], a[136:139], v[148:151]\n"
    "s_add_u32 s58, s58, 229376\n"
    "v_mfma_f32_16x16x32_bf16 v[152:155], a[64:67], a[140:143], v[152:155]\n"
    "v_mfma_f32_16x16x32_bf16 v[156:159], a[64:67], a[144:147], v[156:159]\n"
    "v_mfma_f32_16x16x32_bf16 v[160:163], a[68:71], a[116:119], v[160:163]\n"
    "v_mfma_f32_16x16x32_bf16 v[164:167], a[68:71], a[120:123], v[164:167]\n"
    "v_mfma_f32_16x16x32_bf16 v[168:171], a[68:71], a[124:127], v[168:171]\n"
    "s_add_u32 m0, s62, 147456\n"
    "v_mfma_f32_16x16x32_bf16 v[172:175], a[68:71], a[128:131], v[172:175]\n"
    "global_load_lds_dwordx4 v[238:239], off\n"
    "v_mfma_f32_16x16x32_bf16 v[176:179], a[68:71], a[132:135], v[176:179]\n"
    "v_lshl_add_u64 v[238:239], v[238:239], 0, s[60:61]\n"
    "v_mfma_f32_16x16x32_bf16 v[180:183], a[68:71], a[136:139], v[180:183]\n"
    "v_mfma_f32_16x16x32_bf16 v[184:187], a[68:71], a[140:143], v[184:187]\n"
    "v_mfma_f32_16x16x32_bf16 v[188:191], a[68:71], a[144:147], v[188:191]\n"
    "v_mfma_f32_16x16x32_bf16 a[216:219], a[72:75], a[116:119], a[216:219]\n"
    "v_mfma_f32_16x16x32_bf16 a[220:223], a[72:75], a[120:123], a[220:223]\n"
    "s_mov_b32 m0, 155648\n"
    "v_mfma_f32_16x16x32_bf16 a[224:227], a[72:75], a[124:127], a[224:227]\n"
    "global_load_lds_dwordx4 v244, s[68:69]\n"
    "v_mfma_f32_16x16x32_bf16 a[228:231], a[72:75], a[128:131], a[228:231]\n"
    "v_mfma_f32_16x16x32_bf16 a[232:235], a[72:75], a[132:135], a[232:235]\n"
    "v_mfma_f32_16x16x32_bf16 a[236:239], a[72:75], a[136:139], a[236:239]\n"
    "v_mfma_f32_16x16x32_bf16 a[240:243], a[72:75], a[140:143], a[240:243]\n"
    "v_mfma_f32_16x16x32_bf16 a[244:247], a[72:75], a[144:147], a[244:247]\n"
    "v_mfma_f32_16x16x32_bf16 v[192:195], a[76:79], a[116:119], v[192:195]\n"
    "v_mfma_f32_16x16x32_bf16 v[196:199], a[76:79], a[120:123], v[196:199]\n"
    "global_load_lds_dwordx4 v244, s[68:69] offset:1024\n"
    "v_mfma_f32_16x16x32_bf16 v[200:203], a[76:79], a[124:127], v[200:203]\n"
    "v_mfma_f32_16x16x32_bf16 v[204:207], a[76:79], a[128:131], v[204:207]\n"
    "v_mfma_f32_16x16x32_bf16 v[208:211], a[76:79], a[132:135], v[208:211]\n"
    "v_mfma_f32_16x16x32_bf16 v[212:215], a[76:79], a[136:139], v[212:215]\n"
    "v_mfma_f32_16x16x32_bf16 v[216:219], a[76:79], a[140:143], v[216:219]\n"
    "v_mfma_f32_16x16x32_bf16 v[220:223], a[76:79], a[144:147], v[220:223]\n"
    "s_mov_b32 s66, 6\n"
    "LOOPf_%=:\n"
    "s_waitcnt vmcnt(19) lgkmcnt(0)\n"
    "s_barrier\n"
    "v_mfma_f32_16x16x32_bf16 v[0:3], a[0:3], a[80:83], v[0:3]\n"
    "ds_read_b128 a[116:119], v224 offset:38912\n"
    "v_mfma_f32_16x16x32_bf16 v[4:7], a[0:3], a[84:87], v[4:7]\n"
    "ds_read_b128 a[120:123], v224 offset:39936\n"
    "v_mfma_f32_16x16x32_bf16 v[8:11], a[0:3], a[88:91], v[8:11]\n"
    "s_add_u32 m0, s63, 1024\n"
    "v_mfma_f32_16x16x32_bf16 v[12:15], a[0:3], a[92:95], v[12:15]\n"
    "buffer_load_dwordx4 v236, s[40:43], s56 offen sc1 lds\n"
    "v_mfma_f32_16x16x32_bf16 v[16:19], a[0:3], a[96:99], v[16:19]\n"
    "ds_read_b128 a[124:127], v224 offset:40960\n"
    "v_mfma_f32_16x16x32_bf16 v[20:23], a[0:3], a[100:103], v[20:23]\n"
    "ds_read_b128 a[128:131], v224 offset:41984\n"
    "v_mfma_f32_16x16x32_bf16 v[24:27], a[0:3], a[104:107], v[24:27]\n"
    "ds_read_b128 a[132:135], v224 offset:43008\n"
    "v_mfma_f32_16x16x32_bf16 v[28:31], a[0:3], a[108:111], v[28:31]\n"
    "ds_read_b128 a[136:139], v224 offset:44032\n"
    "v_mfma_f32_16x16x32_bf16 v[32:35], a[4:7], a[80:83], v[32:35]\n"
    "ds_read_b128 a[140:143], v224 offset:45056\n"
    "v_mfma_f32_16x16x32_bf16 v[36:39], a[4:7], a[84:87], v[36:39]\n"
    "ds_read_b128 a[144:147], v224 offset:46080\n"
    "v_mfma_f32_16x16x32_bf16 v[40:43], a[4:7], a[88:91], v[40:43]\n"
    "buffer_load_dwordx4 v236, s[40:43], s56 offen offset:1024 sc1 lds\n"
    "v_mfma_f32_16x16x32_bf16 v[44:47], a[4:7], a[92:95], v[44:47]\n"
    "ds_read_b64_tr_b16 a[40:41], v228 offset:37888\n"
    "v_mfma_f32_16x16x32_bf16 v[48:51], a[4:7], a[96:99], v[48:51]\n"
    "ds_read_b64_tr_b16 a[42:43], v228 offset:38400\n"
    "v_mfma_f32_16x16x32_bf16 v[52:55], a[4:7], a[100:103], v[52:55]\n"
    "ds_read_b64_tr_b16 a[44:45], v229 offset:37888\n"
    "v_mfma_f32_16x16x32_bf16 v[56:59], a[4:7], a[104:107], v[56:59]\n"
    "ds_read_b64_tr_b16 a[46:47], v229 offset:38400\n"
    "v_mfma_f32_16x16x32_bf16 v[60:63], a[4:7], a[108:111], v[60:63]\n"
    "ds_read_b64_tr_b16 a[48:49], v228 offset:41984\n"
    "v_mfma_f32_16x16x32_bf16 v[64:67], a[8:11], a[80:83], v[64:67]\n"
    "ds_read_b64_tr_b16 a[50:51], v228 offset:42496\n"
    "v_mfma_f32_16x16x32_bf16 v[68:71], a[8:11], a[84:87], v[68:71]\n"
    "buffer_load_dwordx4 v236, s[40:43], s56 offen offset:2048 sc1 lds\n"
    "v_mfma_f32_16x16x32_bf16 v[72:75], a[8:11], a[88:91], v[72:75]\n"
    "ds_read_b64_tr_b16 a[52:53], v229 offset:41984\n"
    "v_mfma_f32_16x16x32_bf16 v[76:79], a[8:11], a[92:95], v[76:79]\n"
    "ds_read_b64_tr_b16 a[54:55], v229 offset:42496\n"
    "v_mfma_f32_16x16x32_bf16 v[80:83], a[8:11], a[96:99], v[80:83]\n"
    "ds_read_b64_tr_b16 a[56:57], v228 offset:46080\n"
    "v_mfma_f32_16x16x32_bf16 v[84:87], a[8:11], a[100:103], v[84:87]\n"
    "ds_read_b64_tr_b16 a[58:59], v228 offset:46592\n"
    "v_mfma_f32_16x16x32_bf16 v[88:91], a[8:11], a[104:107], v[88:91]\n"
    "ds_read_b64_tr_b16 a[60:61], v229 offset:46080\n"
    "v_mfma_f32_16x16x32_bf16 v[92:95], a[8:11], a[108:111], v[92:95]\n"
    "ds_read_b64_tr_b16 a[62:63], v229 offset:46592\n"
    "v_mfma_f32_16x16x32_bf16 v[96:99], a[12:15], a[80:83], v[96:99]\n"
    "buffer_load_dwordx4 v236, s[40:43], s56 offen offset:3072 sc1 lds\n"
    "v_mfma_f32_16x16x32_bf16 v[100:103], a[12:15], a[84:87], v[100:103]\n"
    "s_add_u32 s56, s56, 131072\n"
    "v_mfma_f32_16x16x32_bf16 v[104:107], a[12:15], a[88:91], v[104:107]\n"
    "ds_read_b64_tr_b16 a[64:65], v228 offset:50176\n"
    "v_mfma_f32_16x16x32_bf16 v[108:111], a[12:15], a[92:95], v[108:111]\n"
    "ds_read_b64_tr_b16 a[66:67], v228 offset:50688\n"
    "v_mfma_f32_16x16x32_bf16 v[112:115], a[12:15], a[96:99], v[112:115]\n"
    "ds_read_b64_tr_b16 a[68:69], v229 offset:50176\n"
    "v_mfma_f32_16x16x32_bf16 v[116:119], a[12:15], a[100:103], v[116:119]\n"
    "ds_read_b64_tr_b16 a[70:71], v229 offset:50688\n"
    "v_mfma_f32_16x16x32_bf16 v[120:123], a[12:15], a[104:107], v[120:123]\n"
    "s_add_u32 m0, s62, 17408\n"
    "v_mfma_f32_16x16x32_bf16 v[124:127], a[12:15], a[108:111], v[124:127]\n"
    "buffer_load_dwordx4 v237, s[44:47], s57 offen lds\n"
    "v_mfma_f32_16x16x32_bf16 a[152:155], a[16:19], a[80:83], a[152:155]\n"
    "ds_read_b64_tr_b16 a[72:73], v232 offset:37888\n"
    "v_mfma_f32_16x16x32_bf16 a[156:159], a[16:19], a[84:87], a[156:159]\n"
    "ds_read_b64_tr_b16 a[74:75], v232 offset:38400\n"
    "v_mfma_f32_16x16x32_bf16 a[160:163], a[16:19], a[88:91], a[160:163]\n"
    "ds_read_b64_tr_b16 a[76:77], v233 offset:37888\n"
    "v_mfma_f32_16x16x32_bf16 a[164:167], a[16:19], a[92:95], a[164:167]\n"
    "ds_read_b64_tr_b16 a[78:79], v233 offset:38400\n"
    "v_mfma_f32_16x16x32_bf16 a[168:171], a[16:19], a[96:99], a[168:171]\n"
    "v_mfma_f32_16x16x32_bf16 a[172:175], a[16:19], a[100:103], a[172:175]\n"
    "s_add_u32 m0, s62, 21504\n"
    "v_mfma_f32_16x16x32_bf16 a[176:179], a[16:19], a[104:107], a[176:179]\n"
    "buffer_load_dwordx4 v237, s[48:51], s57 offen lds\n"
    "v_mfma_f32_16x16x32_bf16 a[180:183], a[16:19], a[108:111], a[180:183]\n"
    "s_add_u32 s57, s57, 229376\n"
    "v_mfma_f32_16x16x32_bf16 a[184:187], a[20:23], a[80:83], a[184:187]\n"
    "v_mfma_f32_16x16x32_bf16 a[188:191], a[20:23], a[84:87], a[188:191]\n"
    "v_mfma_f32_16x16x32_bf16 a[192:195], a[20:23], a[88:91], a[192:195]\n"
    "v_mfma_f32_16x16x32_bf16 a[196:199], a[20:23], a[92:95], a[196:199]\n"
    "v_mfma_f32_16x16x32_bf16 a[200:203], a[20:23], a[96:99], a[200:203]\n"
    "s_add_u32 m0, s62, 25600\n"
    "v_mfma_f32_16x16x32_bf16 a[204:207], a[20:23], a[100:103], a[204:207]\n"
    "buffer_load_dwordx4 v237, s[44:47], s58 offen lds\n"
    "v_mfma_f32_16x16x32_bf16 a[208:211], a[20:23], a[104:107], a[208:211]\n"
    "v_mfma_f32_16x16x32_bf16 a[212:215], a[20:23], a[108:111], a[212:215]\n"
    "v_mfma_f32_16x16x32_bf16 v[128:131], a[24:27], a[80:83], v[128:131]\n"
    "v_mfma_f32_16x16x32_bf16 v[132:135], a[24:27], a[84:87], v[132:135]\n"
    "v_mfma_f32_16x16x32_bf16 v[136:139], a[24:27], a[88:91], v[136:139]\n"
    "v_mfma_f32_16x16x32_bf16 v[140:143], a[24:27], a[92:95], v[140:143]\n"
    "s_add_u32 m0, s62, 29696\n"
    "v_mfma_f32_16x16x32_bf16 v[144:147], a[24:27], a[96:99], v[144:147]\n"
    "buffer_load_dwordx4 v237, s[48:51], s58 offen lds\n"
    "v_mfma_f32_16x16x32_bf16 v[148:151], a[24:27], a[100:103], v[148:151]\n"
    "s_add_u32 s58, s58, 229376\n"
    "v_mfma_f32_16x16x32_bf16 v[152:155], a[24:27], a[104:107], v[152:155]\n"
    "v_mfma_f32_16x16x32_bf16 v[156:159], a[24:27], a[108:111], v[156:159]\n"
    "v_mfma_f32_16x16x32_bf16 v[160:163], a[28:31], a[80:83], v[160:163]\n"
    "v_mfma_f32_16x16x32_bf16 v[164:167], a[28:31], a[84:87], v[164:167]\n"
    "v_mfma_f32_16x16x32_bf16 v[168:171], a[28:31], a[88:91], v[168:171]\n"
    "s_add_u32 m0, s62, 33792\n"
    "v_mfma_f32_16x16x32_bf16 v[172:175], a[28:31], a[92:95], v[172:175]\n"
    "global_load_lds_dwordx4 v[238:239], off\n"
    "v_mfma_f32_16x16x32_bf16 v[176:179], a[28:31], a[96:99], v[176:179]\n"
    "v_lshl_add_u64 v[238:239], v[238:239], 0, s[60:61]\n"
    "v_mfma_f32_16x16x32_bf16 v[180:183], a[28:31], a[100:103], v[180:183]\n"
    "v_mfma_f32_16x16x32_bf16 v[184:187], a[28:31], a[104:107], v[184:187]\n"
    "v_mfma_f32_16x16x32_bf16 v[188:191], a[28:31], a[108:111], v[188:191]\n"
    "v_mfma_f32_16x16x32_bf16 a[216:219], a[32:35], a[80:83], a[216:219]\n"
    "v_mfma_f32_16x16x32_bf16 a[220:223], a[32:35], a[84:87], a[220:223]\n"
    "v_mfma_f32_16x16x32_bf16 a[224:227], a[32:35], a[88:91], a[224:227]\n"
    "buffer_load_dword v246, v245, s[20:23], s74 offen\n"
    "v_mfma_f32_16x16x32_bf16 a[228:231], a[32:35], a[92:95], a[228:231]\n"
    "s_add_u32 s74, s74, s91\n"
    "v_mfma_f32_16x16x32_bf16 a[232:235], a[32:35], a[96:99], a[232:235]\n"
    "v_mfma_f32_16x16x32_bf16 a[236:239], a[32:35], a[100:103], a[236:239]\n"
    "v_mfma_f32_16x16x32_bf16 a[240:243], a[32:35], a[104:107], a[240:243]\n"
    "v_mfma_f32_16x16x32_bf16 a[244:247], a[32:35], a[108:111], a[244:247]\n"
    "v_mfma_f32_16x16x32_bf16 v[192:195], a[36:39], a[80:83], v[192:195]\n"
    "s_mov_b32 m0, 155648\n"
    "v_mfma_f32_16x16x32_bf16 v[196:199], a[36:39], a[84:87], v[196:199]\n"
    "global_load_lds_dwordx4 v244, s[70:71]\n"
    "v_mfma_f32_16x16x32_bf16 v[200:203], a[36:39], a[88:91], v[200:203]\n"
    "s_add_u32 s70, s70, 1024\n"
    "s_addc_u32 s71, s71, 0\n"
    "v_mfma_f32_16x16x32_bf16 v[204:207], a[36:39], a[92:95], v[204:207]\n"
    "v_mfma_f32_16x16x32_bf16 v[208:211], a[36:39], a[96:99], v[208:211]\n"
    "v_mfma_f32_16x16x32_bf16 v[212:215], a[36:39], a[100:103], v[212:215]\n"
    "v_mfma_f32_16x16x32_bf16 v[216:219], a[36:39], a[104:107], v[216:219]\n"
    "v_mfma_f32_16x16x32_bf16 v[220:223], a[36:39], a[108:111], v[220:223]\n"
    "s_waitcnt vmcnt(21) lgkmcnt(0)\n"
    "s_barrier\n"
    "v_mfma_f32_16x16x32_bf16 v[0:3], a[40:43], a[116:119], v[0:3]\n"
    "ds_read_b128 a[80:83], v225 offset:1024\n"
    "v_mfma_f32_16x16x32_bf16 v[4:7], a[40:43], a[120:123], v[4:7]\n"
    "ds_read_b128 a[84:87], v225 offset:2048\n"
    "v_mfma_f32_16x16x32_bf16 v[8:11], a[40:43], a[124:127], v[8:11]\n"
    "s_add_u32 m0, s63, 38912\n"
    "v_mfma_f32_16x16x32_bf16 v[12:15], a[40:43], a[128:131], v[12:15]\n"
    "buffer_load_dwordx4 v236, s[40:43], s56 offen sc1 lds\n"
    "v_mfma_f32_16x16x32_bf16 v[16:19], a[40:43], a[132:135], v[16:19]\n"
    "ds_read_b128 a[88:91], v225 offset:3072\n"
    "v_mfma_f32_16x16x32_bf16 v[20:23], a[40:43], a[136:139], v[20:23]\n"
    "ds_read_b128 a[92:95], v225 offset:4096\n"
    "v_mfma_f32_16x16x32_bf16 v[24:27], a[40:43], a[140:143], v[24:27]\n"
    "ds_read_b128 a[96:99], v225 offset:5120\n"
    "v_mfma_f32_16x16x32_bf16 v[28:31], a[40:43], a[144:147], v[28:31]\n"
    "ds_read_b128 a[100:103], v225 offset:6144\n"
    "v_mfma_f32_16x16x32_bf16 v[32:35], a[44:47], a[116:119], v[32:35]\n"
    "ds_read_b128 a[104:107], v225 offset:7168\n"
    "v_mfma_f32_16x16x32_bf16 v[36:39], a[44:47], a[120:123], v[36:39]\n"
    "ds_read_b128 a[108:111], v225 offset:8192\n"
    "v_mfma_f32_16x16x32_bf16 v[40:43], a[44:47], a[124:127], v[40:43]\n"
    "ds_read_b64_tr_b16 a[0:1], v230 offset:0\n"
    "v_mfma_f32_16x16x32_bf16 v[44:47], a[44:47], a[128:131], v[44:47]\n"
    "buffer_load_dwordx4 v236, s[40:43], s56 offen offset:1024 sc1 lds\n"
    "v_mfma_f32_16x16x32_bf16 v[48:51], a[44:47], a[132:135], v[48:51]\n"
    "ds_read_b64_tr_b16 a[2:3], v230 offset:512\n"
    "v_mfma_f32_16x16x32_bf16 v[52:55], a[44:47], a[136:139], v[52:55]\n"
    "ds_read_b64_tr_b16 a[4:5], v231 offset:0\n"
    "v_mfma_f32_16x16x32_bf16 v[56:59], a[44:47], a[140:143], v[56:59]\n"
    "ds_read_b64_tr_b16 a[6:7], v231 offset:512\n"
    "v_mfma_f32_16x16x32_bf16 v[60:63], a[44:47], a[144:147], v[60:63]\n"
    "ds_read_b64_tr_b16 a[8:9], v230 offset:4096\n"
    "v_mfma_f32_16x16x32_bf16 v[64:67], a[48:51], a[116:119], v[64:67]\n"
    "ds_read_b64_tr_b16 a[10:11], v230 offset:4608\n"
    "v_mfma_f32_16x16x32_bf16 v[68:71], a[48:51], a[120:123], v[68:71]\n"
    "ds_read_b64_tr_b16 a[12:13], v231 offset:4096\n"
    "v_mfma_f32_16x16x32_bf16 v[72:75], a[48:51], a[124:127], v[72:75]\n"
    "ds_read_b64_tr_b16 a[14:15], v231 offset:4608\n"
    "v_mfma_f32_16x16x32_bf16 v[76:79], a[48:51], a[128:131], v[76:79]\n"
    "buffer_load_dwordx4 v236, s[40:43], s56 offen offset:2048 sc1 lds\n"
    "v_mfma_f32_16x16x32_bf16 v[80:83], a[48:51], a[132:135], v[80:83]\n"
    "ds_read_b64_tr_b16 a[16:17], v230 offset:8192\n"
    "v_mfma_f32_16x16x32_bf16 v[84:87], a[48:51], a[136:139], v[84:87]\n"
    "ds_read_b64_tr_b16 a[18:19], v230 offset:8704\n"
    "v_mfma_f32_16x16x32_bf16 v[88:91], a[48:51], a[140:143], v[88:91]\n"
    "ds_read_b64_tr_b16 a[20:21], v231 offset:8192\n"
    "v_mfma_f32_16x16x32_bf16 v[92:95], a[48:51], a[144:147], v[92:95]\n"
    "ds_read_b64_tr_b16 a[22:23], v231 offset:8704\n"
    "v_mfma_f32_16x16x32_bf16 v[96:99], a[52:55], a[116:119], v[96:99]\n"
    "ds_read_b64_tr_b16 a[24:25], v230 offset:12288\n"
    "v_mfma_f32_16x16x32_bf16 v[100:103], a[52:55], a[120:123], v[100:103]\n"
    "ds_read_b64_tr_b16 a[26:27], v230 offset:12800\n"
    "v_mfma_f32_16x16x32_bf16 v[104:107], a[52:55], a[124:127], v[104:107]\n"
    "ds_read_b64_tr_b16 a[28:29], v231 offset:12288\n"
    "v_mfma_f32_16x16x32_bf16 v[108:111], a[52:55], a[128:131], v[108:111]\n"
    "buffer_load_dwordx4 v236, s[40:43], s56 offen offset:3072 sc1 lds\n"
    "v_mfma_f32_16x16x32_bf16 v[112:115], a[52:55], a[132:135], v[112:115]\n"
    "s_add_u32 s56, s56, 131072\n"
    "v_mfma_f32_16x16x32_bf16 v[116:119], a[52:55], a[136:139], v[116:119]\n"
    "ds_read_b64_tr_b16 a[30:31], v231 offset:12800\n"
    "v_mfma_f32_16x16x32_bf16 v[120:123], a[52:55], a[140:143], v[120:123]\n"
    "ds_read_b64_tr_b16 a[32:33], v234 offset:0\n"
    "v_mfma_f32_16x16x32_bf16 v[124:127], a[52:55], a[144:147], v[124:127]\n"
    "ds_read_b64_tr_b16 a[34:35], v234 offset:512\n"
    "v_mfma_f32_16x16x32_bf16 a[152:155], a[56:59], a[116:119], a[152:155]\n"
    "ds_read_b64_tr_b16 a[36:37], v235 offset:0\n"
    "v_mfma_f32_16x16x32_bf16 a[156:159], a[56:59], a[120:123], a[156:159]\n"
    "ds_read_b64_tr_b16 a[38:39], v235 offset:512\n"
    "v_mfma_f32_16x16x32_bf16 a[160:163], a[56:59], a[124:127], a[160:163]\n"
    "s_add_u32 m0, s62, 55296\n"
    "v_mfma_f32_16x16x32_bf16 a[164:167], a[56:59], a[128:131], a[164:167]\n"
    "buffer_load_dwordx4 v237, s[44:47], s57 offen lds\n"
    "v_mfma_f32_16x16x32_bf16 a[168:171], a[56:59], a[132:135], a[168:171]\n"
    "v_mfma_f32_16x16x32_bf16 a[172:175], a[56:59], a[136:139], a[172:175]\n"
    "v_mfma_f32_16x16x32_bf16 a[176:179], a[56:59], a[140:143], a[176:179]\n"
    "v_mfma_f32_16x16x32_bf16 a[180:183], a[56:59], a[144:147], a[180:183]\n"
    "v_mfma_f32_16x16x32_bf16 a[184:187], a[60:63], a[116:119], a[184:187]\n"
    "v_mfma_f32_16x16x32_bf16 a[188:191], a[60:63], a[120:123], a[188:191]\n"
    "v_mfma_f32_16x16x32_bf16 a[192:195], a[60:63], a[124:127], a[192:195]\n"
    "s_add_u32 m0, s62, 59392\n"
    "v_mfma_f32_16x16x32_bf16 a[196:199], a[60:63], a[128:131], a[196:199]\n"
    "buffer_load_dwordx4 v237, s[48:51], s57 offen lds\n"
    "v_mfma_f32_16x16x32_bf16 a[200:203], a[60:63], a[132:135], a[200:203]\n"
    "s_add_u32 s57, s57, 229376\n"
    "v_mfma_f32_16x16x32_bf16 a[204:207], a[60:63], a[136:139], a[204:207]\n"
    "v_mfma_f32_16x16x32_bf16 a[208:211], a[60:63], a[140:143], a[208:211]\n"
    "v_mfma_f32_16x16x32_bf16 a[212:215], a[60:63], a[144:147], a[212:215]\n"
    "v_mfma_f32_16x16x32_bf16 v[128:131], a[64:67], a[116:119], v[128:131]\n"
    "v_mfma_f32_16x16x32_bf16 v[132:135], a[64:67], a[120:123], v[132:135]\n"
    "v_mfma_f32_16x16x32_bf16 v[136:139], a[64:67], a[124:127], v[136:139]\n"
    "s_add_u32 m0, s62, 63488\n"
    "v_mfma_f32_16x16x32_bf16 v[140:143], a[64:67], a[128:131], v[140:143]\n"
    "buffer_load_dwordx4 v237, s[44:47], s58 offen lds\n"
    "v_mfma_f32_16x16x32_bf16 v[144:147], a[64:67], a[132:135], v[144:147]\n"
    "v_mfma_f32_16x16x32_bf16 v[148:151], a[64:67], a[136:139], v[148:151]\n"
    "v_mfma_f32_16x16x32_bf16 v[152:155], a[64:67], a[140:143], v[152:155]\n"
    "v_mfma_f32_16x16x32_bf16 v[156:159], a[64:67], a[144:147], v[156:159]\n"
    "v_mfma_f32_16x16x32_bf16 v[160:163], a[68:71], a[116:119], v[160:163]\n"
    "v_mfma_f32_16x16x32_bf16 v[164:167], a[68:71], a[120:123], v[164:167]\n"
    "v_mfma_f32_16x16x32_bf16 v[168:171], a[68:71], a[124:127], v[168:171]\n"
    "s_add_u32 m0, s62, 67584\n"
    "v_mfma_f32_16x16x32_bf16 v[172:175], a[68:71], a[128:131], v[172:175]\n"
    "buffer_load_dwordx4 v237, s[48:51], s58 offen lds\n"
    "v_mfma_f32_16x16x32_bf16 v[176:179], a[68:71], a[132:135], v[176:179]\n"
    "s_add_u32 s58, s58, 229376\n"
    "v_mfma_f32_16x16x32_bf16 v[180:183], a[68:71], a[136:139], v[180:183]\n"
    "v_mfma_f32_16x16x32_bf16 v[184:187], a[68:71], a[140:143], v[184:187]\n"
    "v_mfma_f32_16x16x32_bf16 v[188:191], a[68:71], a[144:147], v[188:191]\n"
    "v_mfma_f32_16x16x32_bf16 a[216:219], a[72:75], a[116:119], a[216:219]\n"
    "v_mfma_f32_16x16x32_bf16 a[220:223], a[72:75], a[120:123], a[220:223]\n"
    "v_mfma_f32_16x16x32_bf16 a[224:227], a[72:75], a[124:127], a[224:227]\n"
    "s_add_u32 m0, s62, 71680\n"
    "v_mfma_f32_16x16x32_bf16 a[228:231], a[72:75], a[128:131], a[228:231]\n"
    "global_load_lds_dwordx4 v[238:239], off\n"
    "v_mfma_f32_16x16x32_bf16 a[232:235], a[72:75], a[132:135], a[232:235]\n"
    "v_lshl_add_u64 v[238:239], v[238:239], 0, s[60:61]\n"
    "v_mfma_f32_16x16x32_bf16 a[236:239], a[72:75], a[136:139], a[236:239]\n"
    "v_mfma_f32_16x16x32_bf16 a[240:243], a[72:75], a[140:143], a[240:243]\n"
    "v_mfma_f32_16x16x32_bf16 a[244:247], a[72:75], a[144:147], a[244:247]\n"
    "v_mfma_f32_16x16x32_bf16 v[192:195], a[76:79], a[116:119], v[192:195]\n"
    "v_mfma_f32_16x16x32_bf16 v[196:199], a[76:79], a[120:123], v[196:199]\n"
    "v_mfma_f32_16x16x32_bf16 v[200:203], a[76:79], a[124:127], v[200:203]\n"
    "v_mfma_f32_16x16x32_bf16 v[204:207], a[76:79], a[128:131], v[204:207]\n"
    "v_mfma_f32_16x16x32_bf16 v[208:211], a[76:79], a[132:135], v[208:211]\n"
    "v_mfma_f32_16x16x32_bf16 v[212:215], a[76:79], a[136:139], v[212:215]\n"
    "v_mfma_f32_16x16x32_bf16 v[216:219], a[76:79], a[140:143], v[216:219]\n"
    "v_mfma_f32_16x16x32_bf16 v[220:223], a[76:79], a[144:147], v[220:223]\n"
    "s_waitcnt vmcnt(20) lgkmcnt(0)\n"
    "s_barrier\n"
    "v_mfma_f32_16x16x32_bf16 v[0:3], a[0:3], a[80:83], v[0:3]\n"
    "ds_read_b128 a[116:119], v225 offset:38912\n"
    "v_mfma_f32_16x16x32_bf16 v[4:7], a[0:3], a[84:87], v[4:7]\n"
    "ds_read_b128 a[120:123], v225 offset:39936\n"
    "v_mfma_f32_16x16x32_bf16 v[8:11], a[0:3], a[88:91], v[8:11]\n"
    "s_add_u32 m0, s63, 76800\n"
    "v_mfma_f32_16x16x32_bf16 v[12:15], a[0:3], a[92:95], v[12:15]\n"
    "buffer_load_dwordx4 v236, s[40:43], s56 offen sc1 lds\n"
    "v_mfma_f32_16x16x32_bf16 v[16:19], a[0:3], a[96:99], v[16:19]\n"
    "ds_read_b128 a[124:127], v225 offset:40960\n"
    "v_mfma_f32_16x16x32_bf16 v[20:23], a[0:3], a[100:103], v[20:23]\n"
    "ds_read_b128 a[128:131], v225 offset:41984\n"
    "v_mfma_f32_16x16x32_bf16 v[24:27], a[0:3], a[104:107], v[24:27]\n"
    "ds_read_b128 a[132:135], v225 offset:43008\n"
    "v_mfma_f32_16x16x32_bf16 v[28:31], a[0:3], a[108:111], v[28:31]\n"
    "ds_read_b128 a[136:139], v225 offset:44032\n"
    "v_mfma_f32_16x16x32_bf16 v[32:35], a[4:7], a[80:83], v[32:35]\n"
    "ds_read_b128 a[140:143], v225 offset:45056\n"
    "v_mfma_f32_16x16x32_bf16 v[36:39], a[4:7], a[84:87], v[36:39]\n"
    "ds_read_b128 a[144:147], v225 offset:46080\n"
    "v_mfma_f32_16x16x32_bf16 v[40:43], a[4:7], a[88:91], v[40:43]\n"
    "ds_read_b64_tr_b16 a[40:41], v230 offset:37888\n"
    "v_mfma_f32_16x16x32_bf16 v[44:47], a[4:7], a[92:95], v[44:47]\n"
    "buffer_load_dwordx4 v236, s[40:43], s56 offen offset:1024 sc1 lds\n"
    "v_mfma_f32_16x16x32_bf16 v[48:51], a[4:7], a[96:99], v[48:51]\n"
    "ds_read_b64_tr_b16 a[42:43], v230 offset:38400\n"
    "v_mfma_f32_16x16x32_bf16 v[52:55], a[4:7], a[100:103], v[52:55]\n"
    "ds_read_b64_tr_b16 a[44:45], v231 offset:37888\n"
    "v_mfma_f32_16x16x32_bf16 v[56:59], a[4:7], a[104:107], v[56:59]\n"
    "ds_read_b64_tr_b16 a[46:47], v231 offset:38400\n"
    "v_mfma_f32_16x16x32_bf16 v[60:63], a[4:7], a[108:111], v[60:63]\n"
    "ds_read_b64_tr_b16 a[48:49], v230 offset:41984\n"
    "v_mfma_f32_16x16x32_bf16 v[64:67], a[8:11], a[80:83], v[64:67]\n"
    "ds_read_b64_tr_b16 a[50:51], v230 offset:42496\n"
    "v_mfma_f32_16x16x32_bf16 v[68:71], a[8:11], a[84:87], v[68:71]\n"
    "ds_read_b64_tr_b16 a[52:53], v231 offset:41984\n"
    "v_mfma_f32_16x16x32_bf16 v[72:75], a[8:11], a[88:91], v[72:75]\n"
    "ds_read_b64_tr_b16 a[54:55], v231 offset:42496\n"
    "v_mfma_f32_16x16x32_bf16 v[76:79], a[8:11], a[92:95], v[76:79]\n"
    "buffer_load_dwordx4 v236, s[40:43], s56 offen offset:2048 sc1 lds\n"
    "v_mfma_f32_16x16x32_bf16 v[80:83], a[8:11], a[96:99], v[80:83]\n"
    "ds_read_b64_tr_b16 a[56:57], v230 offset:46080\n"
    "v_mfma_f32_16x16x32_bf16 v[84:87], a[8:11], a[100:103], v[84:87]\n"
    "ds_read_b64_tr_b16 a[58:59], v230 offset:46592\n"
    "v_mfma_f32_16x16x32_bf16 v[88:91], a[8:11], a[104:107], v[88:91]\n"
    "ds_read_b64_tr_b16 a[60:61], v231 offset:46080\n"
    "v_mfma_f32_16x16x32_bf16 v[92:95], a[8:11], a[108:111], v[92:95]\n"
    "ds_read_b64_tr_b16 a[62:63], v231 offset:46592\n"
    "v_mfma_f32_16x16x32_bf16 v[96:99], a[12:15], a[80:83], v[96:99]\n"
    "ds_read_b64_tr_b16 a[64:65], v230 offset:50176\n"
    "v_mfma_f32_16x16x32_bf16 v[100:103], a[12:15], a[84:87], v[100:103]\n"
    "ds_read_b64_tr_b16 a[66:67], v230 offset:50688\n"
    "v_mfma_f32_16x16x32_bf16 v[104:107], a[12:15], a[88:91], v[104:107]\n"
    "ds_read_b64_tr_b16 a[68:69], v231 offset:50176\n"
    "v_mfma_f32_16x16x32_bf16 v[108:111], a[12:15], a[92:95], v[108:111]\n"
    "buffer_load_dwordx4 v236, s[40:43], s56 offen offset:3072 sc1 lds\n"
    "v_mfma_f32_16x16x32_bf16 v[112:115], a[12:15], a[96:99], v[112:115]\n"
    "s_add_u32 s56, s56, 131072\n"
    "v_mfma_f32_16x16x32_bf16 v[116:119], a[12:15], a[100:103], v[116:119]\n"
    "ds_read_b64_tr_b16 a[70:71], v231 offset:50688\n"
    "v_mfma_f32_16x16x32_bf16 v[120:123], a[12:15], a[104:107], v[120:123]\n"
    "ds_read_b64_tr_b16 a[72:73], v234 offset:37888\n"
    "v_mfma_f32_16x16x32_bf16 v[124:127], a[12:15], a[108:111], v[124:127]\n"
    "ds_read_b64_tr_b16 a[74:75], v234 offset:38400\n"
    "v_mfma_f32_16x16x32_bf16 a[152:155], a[16:19], a[80:83], a[152:155]\n"
    "ds_read_b64_tr_b16 a[76:77], v235 offset:37888\n"
    "v_mfma_f32_16x16x32_bf16 a[156:159], a[16:19], a[84:87], a[156:159]\n"
    "ds_read_b64_tr_b16 a[78:79], v235 offset:38400\n"
    "v_mfma_f32_16x16x32_bf16 a[160:163], a[16:19], a[88:91], a[160:163]\n"
    "s_add_u32 m0, s62, 93184\n"
    "v_mfma_f32_16x16x32_bf16 a[164:167], a[16:19], a[92:95], a[164:167]\n"
    "buffer_load_dwordx4 v237, s[44:47], s57 offen lds\n"
    "v_mfma_f32_16x16x32_bf16 a[168:171], a[16:19], a[96:99], a[168:171]\n"
    "v_mfma_f32_16x16x32_bf16 a[172:175], a[16:19], a[100:103], a[172:175]\n"
    "v_mfma_f32_16x16x32_bf16 a[176:179], a[16:19], a[104:107], a[176:179]\n"
    "v_mfma_f32_16x16x32_bf16 a[180:183], a[16:19], a[108:111], a[180:183]\n"
    "v_mfma_f32_16x16x32_bf16 a[184:187], a[20:23], a[80:83], a[184:187]\n"
    "v_mfma_f32_16x16x32_bf16 a[188:191], a[20:23], a[84:87], a[188:191]\n"
    "v_mfma_f32_16x16x32_bf16 a[192:195], a[20:23], a[88:91], a[192:195]\n"
    "s_add_u32 m0, s62, 97280\n"
    "v_mfma_f32_16x16x32_bf16 a[196:199], a[20:23], a[92:95], a[196:199]\n"
    "buffer_load_dwordx4 v237, s[48:51], s57 offen lds\n"
    "v_mfma_f32_16x16x32_bf16 a[200:203], a[20:23], a[96:99], a[200:203]\n"
    "s_add_u32 s57, s57, 229376\n"
    "v_mfma_f32_16x16x32_bf16 a[204:207], a[20:23], a[100:103], a[204:207]\n"
    "v_mfma_f32_16x16x32_bf16 a[208:211], a[20:23], a[104:107], a[208:211]\n"
    "v_mfma_f32_16x16x32_bf16 a[212:215], a[20:23], a[108:111], a[212:215]\n"
    "v_mfma_f32_16x16x32_bf16 v[128:131], a[24:27], a[80:83], v[128:131]\n"
    "v_mfma_f32_16x16x32_bf16 v[132:135], a[24:27], a[84:87], v[132:135]\n"
    "v_mfma_f32_16x16x32_bf16 v[136:139], a[24:27], a[88:91], v[136:139]\n"
    "s_add_u32 m0, s62, 101376\n"
    "v_mfma_f32_16x16x32_bf16 v[140:143], a[24:27], a[92:95], v[140:143]\n"
    "buffer_load_dwordx4 v237, s[44:47], s58 offen lds\n"
    "v_mfma_f32_16x16x32_bf16 v[144:147], a[24:27], a[96:99], v[144:147]\n"
    "v_mfma_f32_16x16x32_bf16 v[148:151], a[24:27], a[100:103], v[148:151]\n"
    "v_mfma_f32_16x16x32_bf16 v[152:155], a[24:27], a[104:107], v[152:155]\n"
    "v_mfma_f32_16x16x32_bf16 v[156:159], a[24:27], a[108:111], v[156:159]\n"
    "v_mfma_f32_16x16x32_bf16 v[160:163], a[28:31], a[80:83], v[160:163]\n"
    "v_mfma_f32_16x16x32_bf16 v[164:167], a[28:31], a[84:87], v[164:167]\n"
    "v_mfma_f32_16x16x32_bf16 v[168:171], a[28:31], a[88:91], v[168:171]\n"
    "s_add_u32 m0, s62, 105472\n"
    "v_mfma_f32_16x16x32_bf16 v[172:175], a[28:31], a[92:95], v[172:175]\n"
    "buffer_load_dwordx4 v237, s[48:51], s58 offen lds\n"
    "v_mfma_f32_16x16x32_bf16 v[176:179], a[28:31], a[96:99], v[176:179]\n"
    "s_add_u32 s58, s58, 229376\n"
    "v_mfma_f32_16x16x32_bf16 v[180:183], a[28:31], a[100:103], v[180:183]\n"
    "v_mfma_f32_16x16x32_bf16 v[184:187], a[28:31], a[104:107], v[184:187]\n"
    "v_mfma_f32_16x16x32_bf16 v[188:191], a[28:31], a[108:111], v[188:191]\n"
    "v_mfma_f32_16x16x32_bf16 a[216:219], a[32:35], a[80:83], a[216:219]\n"
    "v_mfma_f32_16x16x32_bf16 a[220:223], a[32:35], a[84:87], a[220:223]\n"
    "v_mfma_f32_16x16x32_bf16 a[224:227], a[32:35], a[88:91], a[224:227]\n"
    "s_add_u32 m0, s62, 109568\n"
    "v_mfma_f32_16x16x32_bf16 a[228:231], a[32:35], a[92:95], a[228:231]\n"
    "global_load_lds_dwordx4 v[238:239], off\n"
    "v_mfma_f32_16x16x32_bf16 a[232:235], a[32:35], a[96:99], a[232:235]\n"
    "v_lshl_add_u64 v[238:239], v[238:239], 0, s[60:61]\n"
    "v_mfma_f32_16x16x32_bf16 a[236:239], a[32:35], a[100:103], a[236:239]\n"
    "v_mfma_f32_16x16x32_bf16 a[240:243], a[32:35], a[104:107], a[240:243]\n"
    "v_mfma_f32_16x16x32_bf16 a[244:247], a[32:35], a[108:111], a[244:247]\n"
    "v_mfma_f32_16x16x32_bf16 v[192:195], a[36:39], a[80:83], v[192:195]\n"
    "v_mfma_f32_16x16x32_bf16 v[196:199], a[36:39], a[84:87], v[196:199]\n"
    "v_mfma_f32_16x16x32_bf16 v[200:203], a[36:39], a[88:91], v[200:203]\n"
    "s_sub_u32 s92, 6, s66\n"
    "s_min_u32 m0, s92, 5\n"
    "s_nop 0\n"
    "s_movrels_b32 s92, s84\n"
    "v_mfma_f32_16x16x32_bf16 v[204:207], a[36:39], a[92:95], v[204:207]\n"
    "buffer_load_dword v246, v245, s[80:83], s92 offen\n"
    "v_mfma_f32_16x16x32_bf16 v[208:211], a[36:39], a[96:99], v[208:211]\n"
    "v_mfma_f32_16x16x32_bf16 v[212:215], a[36:39], a[100:103], v[212:215]\n"
    "v_mfma_f32_16x16x32_bf16 v[216:219], a[36:39], a[104:107], v[216:219]\n"
    "v_mfma_f32_16x16x32_bf16 v[220:223], a[36:39], a[108:111], v[220:223]\n"
    "s_waitcnt vmcnt(21) lgkmcnt(0)\n"
    "s_barrier\n"
    "v_mfma_f32_16x16x32_bf16 v[0:3], a[40:43], a[116:119], v[0:3]\n"
    "ds_read_b128 a[80:83], v224 offset:1024\n"
    "v_mfma_f32_16x16x32_bf16 v[4:7], a[40:43], a[120:123], v[4:7]\n"
    "ds_read_b128 a[84:87], v224 offset:2048\n"
    "v_mfma_f32_16x16x32_bf16 v[8:11], a[40:43], a[124:127], v[8:11]\n"
    "s_add_u32 m0, s63, 114688\n"
    "v_mfma_f32_16x16x32_bf16 v[12:15], a[40:43], a[128:131], v[12:15]\n"
    "buffer_load_dwordx4 v236, s[40:43], s56 offen sc1 lds\n"
    "v_mfma_f32_16x16x32_bf16 v[16:19], a[40:43], a[132:135], v[16:19]\n"
    "ds_read_b128 a[88:91], v224 offset:3072\n"
    "v_mfma_f32_16x16x32_bf16 v[20:23], a[40:43], a[136:139], v[20:23]\n"
    "ds_read_b128 a[92:95], v224 offset:4096\n"
    "v_mfma_f32_16x16x32_bf16 v[24:27], a[40:43], a[140:143], v[24:27]\n"
    "ds_read_b128 a[96:99], v224 offset:5120\n"
    "v_mfma_f32_16x16x32_bf16 v[28:31], a[40:43], a[144:147], v[28:31]\n"
    "ds_read_b128 a[100:103], v224 offset:6144\n"
    "v_mfma_f32_16x16x32_bf16 v[32:35], a[44:47], a[116:119], v[32:35]\n"
    "ds_read_b128 a[104:107], v224 offset:7168\n"
    "v_mfma_f32_16x16x32_bf16 v[36:39], a[44:47], a[120:123], v[36:39]\n"
    "ds_read_b128 a[108:111], v224 offset:8192\n"
    "v_mfma_f32_16x16x32_bf16 v[40:43], a[44:47], a[124:127], v[40:43]\n"
    "ds_read_b64_tr_b16 a[0:1], v228 offset:0\n"
    "v_mfma_f32_16x16x32_bf16 v[44:47], a[44:47], a[128:131], v[44:47]\n"
    "buffer_load_dwordx4 v236, s[40:43], s56 offen offset:1024 sc1 lds\n"
    "v_mfma_f32_16x16x32_bf16 v[48:51], a[44:47], a[132:135], v[48:51]\n"
    "ds_read_b64_tr_b16 a[2:3], v228 offset:512\n"
    "v_mfma_f32_16x16x32_bf16 v[52:55], a[44:47], a[136:139], v[52:55]\n"
    "ds_read_b64_tr_b16 a[4:5], v229 offset:0\n"
    "v_mfma_f32_16x16x32_bf16 v[56:59], a[44:47], a[140:143], v[56:59]\n"
    "ds_read_b64_tr_b16 a[6:7], v229 offset:512\n"
    "v_mfma_f32_16x16x32_bf16 v[60:63], a[44:47], a[144:147], v[60:63]\n"
    "ds_read_b64_tr_b16 a[8:9], v228 offset:4096\n"
    "v_mfma_f32_16x16x32_bf16 v[64:67], a[48:51], a[116:119], v[64:67]\n"
    "ds_read_b64_tr_b16 a[10:11], v228 offset:4608\n"
    "v_mfma_f32_16x16x32_bf16 v[68:71], a[48:51], a[120:123], v[68:71]\n"
    "ds_read_b64_tr_b16 a[12:13], v229 offset:4096\n"
    "v_mfma_f32_16x16x32_bf16 v[72:75], a[48:51], a[124:127], v[72:75]\n"
    "ds_read_b64_tr_b16 a[14:15], v229 offset:4608\n"
    "v_mfma_f32_16x16x32_bf16 v[76:79], a[48:51], a[128:131], v[76:79]\n"
    "buffer_load_dwordx4 v236, s[40:43], s56 offen offset:2048 sc1 lds\n"
    "v_mfma_f32_16x16x32_bf16 v[80:83], a[48:51], a[132:135], v[80:83]\n"
    "ds_read_b64_tr_b16 a[16:17], v228 offset:8192\n"
    "v_mfma_f32_16x16x32_bf16 v[84:87], a[48:51], a[136:139], v[84:87]\n"
    "ds_read_b64_tr_b16 a[18:19], v228 offset:8704\n"
    "v_mfma_f32_16x16x32_bf16 v[88:91], a[48:51], a[140:143], v[88:91]\n"
    "ds_read_b64_tr_b16 a[20:21], v229 offset:8192\n"
    "v_mfma_f32_16x16x32_bf16 v[92:95], a[48:51], a[144:147], v[92:95]\n"
    "ds_read_b64_tr_b16 a[22:23], v229 offset:8704\n"
    "v_mfma_f32_16x16x32_bf16 v[96:99], a[52:55], a[116:119], v[96:99]\n"
    "ds_read_b64_tr_b16 a[24:25], v228 offset:12288\n"
    "v_mfma_f32_16x16x32_bf16 v[100:103], a[52:55], a[120:123], v[100:103]\n"
    "ds_read_b64_tr_b16 a[26:27], v228 offset:12800\n"
    "v_mfma_f32_16x16x32_bf16 v[104:107], a[52:55], a[124:127], v[104:107]\n"
    "ds_read_b64_tr_b16 a[28:29], v229 offset:12288\n"
    "v_mfma_f32_16x16x32_bf16 v[108:111], a[52:55], a[128:131], v[108:111]\n"
    "buffer_load_dwordx4 v236, s[40:43], s56 offen offset:3072 sc1 lds\n"
    "v_mfma_f32_16x16x32_bf16 v[112:115], a[52:55], a[132:135], v[112:115]\n"
    "s_add_u32 s56, s56, 131072\n"
    "v_mfma_f32_16x16x32_bf16 v[116:119], a[52:55], a[136:139], v[116:119]\n"
    "ds_read_b64_tr_b16 a[30:31], v229 offset:12800\n"
    "v_mfma_f32_16x16x32_bf16 v[120:123], a[52:55], a[140:143], v[120:123]\n"
    "ds_read_b64_tr_b16 a[32:33], v232 offset:0\n"
    "v_mfma_f32_16x16x32_bf16 v[124:127], a[52:55], a[144:147], v[124:127]\n"
    "ds_read_b64_tr_b16 a[34:35], v232 offset:512\n"
    "v_mfma_f32_16x16x32_bf16 a[152:155], a[56:59], a[116:119], a[152:155]\n"
    "ds_read_b64_tr_b16 a[36:37], v233 offset:0\n"
    "v_mfma_f32_16x16x32_bf16 a[156:159], a[56:59], a[120:123], a[156:159]\n"
    "ds_read_b64_tr_b16 a[38:39], v233 offset:512\n"
    "v_mfma_f32_16x16x32_bf16 a[160:163], a[56:59], a[124:127], a[160:163]\n"
    "s_add_u32 m0, s62, 131072\n"
    "v_mfma_f32_16x16x32_bf16 a[164:167], a[56:59], a[128:131], a[164:167]\n"
    "buffer_load_dwordx4 v237, s[44:47], s57 offen lds\n"
    "v_mfma_f32_16x16x32_bf16 a[168:171], a[56:59], a[132:135], a[168:171]\n"
    "v_mfma_f32_16x16x32_bf16 a[172:175], a[56:59], a[136:139], a[172:175]\n"
    "v_mfma_f32_16x16x32_bf16 a[176:179], a[56:59], a[140:143], a[176:179]\n"
    "v_mfma_f32_16x16x32_bf16 a[180:183], a[56:59], a[144:147], a[180:183]\n"
    "v_mfma_f32_16x16x32_bf16 a[184:187], a[60:63], a[116:119], a[184:187]\n"
    "v_mfma_f32_16x16x32_bf16 a[188:191], a[60:63], a[120:123], a[188:191]\n"
    "v_mfma_f32_16x16x32_bf16 a[192:195], a[60:63], a[124:127], a[192:195]\n"
    "s_add_u32 m0, s62, 135168\n"
    "v_mfma_f32_16x16x32_bf16 a[196:199], a[60:63], a[128:131], a[196:199]\n"
    "buffer_load_dwordx4 v237, s[48:51], s57 offen lds\n"
    "v_mfma_f32_16x16x32_bf16 a[200:203], a[60:63], a[132:135], a[200:203]\n"
    "s_add_u32 s57, s57, 229376\n"
    "v_mfma_f32_16x16x32_bf16 a[204:207], a[60:63], a[136:139], a[204:207]\n"
    "v_mfma_f32_16x16x32_bf16 a[208:211], a[60:63], a[140:143], a[208:211]\n"
    "v_mfma_f32_16x16x32_bf16 a[212:215], a[60:63], a[144:147], a[212:215]\n"
    "v_mfma_f32_16x16x32_bf16 v[128:131], a[64:67], a[116:119], v[128:131]\n"
    "v_mfma_f32_16x16x32_bf16 v[132:135], a[64:67], a[120:123], v[132:135]\n"
    "v_mfma_f32_16x16x32_bf16 v[136:139], a[64:67], a[124:127], v[136:139]\n"
    "s_add_u32 m0, s62, 139264\n"
    "v_mfma_f32_16x16x32_bf16 v[140:143], a[64:67], a[128:131], v[140:143]\n"
    "buffer_load_dwordx4 v237, s[44:47], s58 offen lds\n"
    "v_mfma_f32_16x16x32_bf16 v[144:147], a[64:67], a[132:135], v[144:147]\n"
    "v_mfma_f32_16x16x32_bf16 v[148:151], a[64:67], a[136:139], v[148:151]\n"
    "v_mfma_f32_16x16x32_bf16 v[152:155], a[64:67], a[140:143], v[152:155]\n"
    "v_mfma_f32_16x16x32_bf16 v[156:159], a[64:67], a[144:147], v[156:159]\n"
    "v_mfma_f32_16x16x32_bf16 v[160:163], a[68:71], a[116:119], v[160:163]\n"
    "v_mfma_f32_16x16x32_bf16 v[164:167], a[68:71], a[120:123], v[164:167]\n"
    "v_mfma_f32_16x16x32_bf16 v[168:171], a[68:71], a[124:127], v[168:171]\n"
    "s_add_u32 m0, s62, 143360\n"
    "v_mfma_f32_16x16x32_bf16 v[172:175], a[68:71], a[128:131], v[172:175]\n"
    "buffer_load_dwordx4 v237, s[48:51], s58 offen lds\n"
    "v_mfma_f32_16x16x32_bf16 v[176:179], a[68:71], a[132:135], v[176:179]\n"
    "s_add_u32 s58, s58, 229376\n"
    "v_mfma_f32_16x16x32_bf16 v[180:183], a[68:71], a[136:139], v[180:183]\n"
    "v_mfma_f32_16x16x32_bf16 v[184:187], a[68:71], a[140:143], v[184:187]\n"
    "v_mfma_f32_16x16x32_bf16 v[188:191], a[68:71], a[144:147], v[188:191]\n"
    "v_mfma_f32_16x16x32_bf16 a[216:219], a[72:75], a[116:119], a[216:219]\n"
    "v_mfma_f32_16x16x32_bf16 a[220:223], a[72:75], a[120:123], a[220:223]\n"
    "v_mfma_f32_16x16x32_bf16 a[224:227], a[72:75], a[124:127], a[224:227]\n"
    "s_add_u32 m0, s62, 147456\n"
    "v_mfma_f32_16x16x32_bf16 a[228:231], a[72:75], a[128:131], a[228:231]\n"
    "global_load_lds_dwordx4 v[238:239], off\n"
    "v_mfma_f32_16x16x32_bf16 a[232:235], a[72:75], a[132:135], a[232:235]\n"
    "v_lshl_add_u64 v[238:239], v[238:239], 0, s[60:61]\n"
    "v_mfma_f32_16x16x32_bf16 a[236:239], a[72:75], a[136:139], a[236:239]\n"
    "v_mfma_f32_16x16x32_bf16 a[240:243], a[72:75], a[140:143], a[240:243]\n"
    "v_mfma_f32_16x16x32_bf16 a[244:247], a[72:75], a[144:147], a[244:247]\n"
    "v_mfma_f32_16x16x32_bf16 v[192:195], a[76:79], a[116:119], v[192:195]\n"
    "v_mfma_f32_16x16x32_bf16 v[196:199], a[76:79], a[120:123], v[196:199]\n"
    "v_mfma_f32_16x16x32_bf16 v[200:203], a[76:79], a[124:127], v[200:203]\n"
    "v_mfma_f32_16x16x32_bf16 v[204:207], a[76:79], a[128:131], v[204:207]\n"
    "v_mfma_f32_16x16x32_bf16 v[208:211], a[76:79], a[132:135], v[208:211]\n"
    "v_mfma_f32_16x16x32_bf16 v[212:215], a[76:79], a[136:139], v[212:215]\n"
    "v_mfma_f32_16x16x32_bf16 v[216:219], a[76:79], a[140:143], v[216:219]\n"
    "v_mfma_f32_16x16x32_bf16 v[220:223], a[76:79], a[144:147], v[220:223]\n"
    "s_sub_u32 s66, s66, 1\n"
    "s_cmp_lg_u32 s66, 0\n"
    "s_cbranch_scc1 LOOPf_%=\n"
    "s_waitcnt vmcnt(19) lgkmcnt(0)\n"
    "s_barrier\n"
    "s_lshl_b32 s56, s67, 3\n"
    "v_lshrrev_b32 v236, 7, v244\n"
    "v_add_u32 v236, s56, v236\n"
    "v_bfe_u32 v237, v236, 1, 2\n"
    "v_lshlrev_b32 v237, 1, v237\n"
    "v_bfe_u32 v238, v244, 4, 3\n"
    "v_xor_b32 v237, v237, v238\n"
    "v_lshlrev_b32 v237, 4, v237\n"
    "v_lshl_add_u32 v236, v236, 11, v237\n"
    "v_mfma_f32_16x16x32_bf16 v[0:3], a[0:3], a[80:83], v[0:3]\n"
    "ds_read_b128 a[116:119], v224 offset:38912\n"
    "v_mfma_f32_16x16x32_bf16 v[4:7], a[0:3], a[84:87], v[4:7]\n"
    "ds_read_b128 a[120:123], v224 offset:39936\n"
    "v_mfma_f32_16x16x32_bf16 v[8:11], a[0:3], a[88:91], v[8:11]\n"
    "s_add_u32 m0, s62, 0\n"
    "v_mfma_f32_16x16x32_bf16 v[12:15], a[0:3], a[92:95], v[12:15]\n"
    "buffer_load_dwordx4 v236, s[24:27], s28 offen lds\n"
    "v_mfma_f32_16x16x32_bf16 v[16:19], a[0:3], a[96:99], v[16:19]\n"
    "ds_read_b128 a[124:127], v224 offset:40960\n"
    "v_mfma_f32_16x16x32_bf16 v[20:23], a[0:3], a[100:103], v[20:23]\n"
    "ds_read_b128 a[128:131], v224 offset:41984\n"
    "v_mfma_f32_16x16x32_bf16 v[24:27], a[0:3], a[104:107], v[24:27]\n"
    "ds_read_b128 a[132:135], v224 offset:43008\n"
    "v_mfma_f32_16x16x32_bf16 v[28:31], a[0:3], a[108:111], v[28:31]\n"
    "ds_read_b128 a[136:139], v224 offset:44032\n"
    "v_mfma_f32_16x16x32_bf16 v[32:35], a[4:7], a[80:83], v[32:35]\n"
    "ds_read_b128 a[140:143], v224 offset:45056\n"
    "v_mfma_f32_16x16x32_bf16 v[36:39], a[4:7], a[84:87], v[36:39]\n"
    "ds_read_b128 a[144:147], v224 offset:46080\n"
    "v_mfma_f32_16x16x32_bf16 v[40:43], a[4:7], a[88:91], v[40:43]\n"
    "s_add_u32 m0, s62, 3968\n"
    "v_mfma_f32_16x16x32_bf16 v[44:47], a[4:7], a[92:95], v[44:47]\n"
    "buffer_load_dwordx4 v236, s[24:27], s28 offen offset:128 lds\n"
    "v_mfma_f32_16x16x32_bf16 v[48:51], a[4:7], a[96:99], v[48:51]\n"
    "s_add_u32 s28, s28, 131072\n"
    "v_mfma_f32_16x16x32_bf16 v[52:55], a[4:7], a[100:103], v[52:55]\n"
    "ds_read_b64_tr_b16 a[40:41], v228 offset:37888\n"
    "v_mfma_f32_16x16x32_bf16 v[56:59], a[4:7], a[104:107], v[56:59]\n"
    "ds_read_b64_tr_b16 a[42:43], v228 offset:38400\n"
    "v_mfma_f32_16x16x32_bf16 v[60:63], a[4:7], a[108:111], v[60:63]\n"
    "ds_read_b64_tr_b16 a[44:45], v229 offset:37888\n"
    "v_mfma_f32_16x16x32_bf16 v[64:67], a[8:11], a[80:83], v[64:67]\n"
    "ds_read_b64_tr_b16 a[46:47], v229 offset:38400\n"
    "v_mfma_f32_16x16x32_bf16 v[68:71], a[8:11], a[84:87], v[68:71]\n"
    "ds_read_b64_tr_b16 a[48:49], v228 offset:41984\n"
    "v_mfma_f32_16x16x32_bf16 v[72:75], a[8:11], a[88:91], v[72:75]\n"
    "s_add_u32 m0, s62, 8192\n"
    "v_mfma_f32_16x16x32_bf16 v[76:79], a[8:11], a[92:95], v[76:79]\n"
    "buffer_load_dwordx4 v236, s[24:27], s29 offen lds\n"
    "v_mfma_f32_16x16x32_bf16 v[80:83], a[8:11], a[96:99], v[80:83]\n"
    "ds_read_b64_tr_b16 a[50:51], v228 offset:42496\n"
    "v_mfma_f32_16x16x32_bf16 v[84:87], a[8:11], a[100:103], v[84:87]\n"
    "ds_read_b64_tr_b16 a[52:53], v229 offset:41984\n"
    "v_mfma_f32_16x16x32_bf16 v[88:91], a[8:11], a[104:107], v[88:91]\n"
    "ds_read_b64_tr_b16 a[54:55], v229 offset:42496\n"
    "v_mfma_f32_16x16x32_bf16 v[92:95], a[8:11], a[108:111], v[92:95]\n"
    "ds_read_b64_tr_b16 a[56:57], v228 offset:46080\n"
    "v_mfma_f32_16x16x32_bf16 v[96:99], a[12:15], a[80:83], v[96:99]\n"
    "ds_read_b64_tr_b16 a[58:59], v228 offset:46592\n"
    "v_mfma_f32_16x16x32_bf16 v[100:103], a[12:15], a[84:87], v[100:103]\n"
    "ds_read_b64_tr_b16 a[60:61], v229 offset:46080\n"
    "v_mfma_f32_16x16x32_bf16 v[104:107], a[12:15], a[88:91], v[104:107]\n"
    "s_add_u32 m0, s62, 12160\n"
    "v_mfma_f32_16x16x32_bf16 v[108:111], a[12:15], a[92:95], v[108:111]\n"
    "buffer_load_dwordx4 v236, s[24:27], s29 offen offset:128 lds\n"
    "v_mfma_f32_16x16x32_bf16 v[112:115], a[12:15], a[96:99], v[112:115]\n"
    "s_add_u32 s29, s29, 131072\n"
    "v_mfma_f32_16x16x32_bf16 v[116:119], a[12:15], a[100:103], v[116:119]\n"
    "ds_read_b64_tr_b16 a[62:63], v229 offset:46592\n"
    "v_mfma_f32_16x16x32_bf16 v[120:123], a[12:15], a[104:107], v[120:123]\n"
    "ds_read_b64_tr_b16 a[64:65], v228 offset:50176\n"
    "v_mfma_f32_16x16x32_bf16 v[124:127], a[12:15], a[108:111], v[124:127]\n"
    "ds_read_b64_tr_b16 a[66:67], v228 offset:50688\n"
    "v_mfma_f32_16x16x32_bf16 a[152:155], a[16:19], a[80:83], a[152:155]\n"
    "ds_read_b64_tr_b16 a[68:69], v229 offset:50176\n"
    "v_mfma_f32_16x16x32_bf16 a[156:159], a[16:19], a[84:87], a[156:159]\n"
    "ds_read_b64_tr_b16 a[70:71], v229 offset:50688\n"
    "v_mfma_f32_16x16x32_bf16 a[160:163], a[16:19], a[88:91], a[160:163]\n"
    "s_add_u32 m0, s62, 16384\n"
    "v_mfma_f32_16x16x32_bf16 a[164:167], a[16:19], a[92:95], a[164:167]\n"
    "buffer_load_dwordx4 v236, s[24:27], s28 offen lds\n"
    "v_mfma_f32_16x16x32_bf16 a[168:171], a[16:19], a[96:99], a[168:171]\n"
    "ds_read_b64_tr_b16 a[72:73], v232 offset:37888\n"
    "v_mfma_f32_16x16x32_bf16 a[172:175], a[16:19], a[100:103], a[172:175]\n"
    "ds_read_b64_tr_b16 a[74:75], v232 offset:38400\n"
    "v_mfma_f32_16x16x32_bf16 a[176:179], a[16:19], a[104:107], a[176:179]\n"
    "ds_read_b64_tr_b16 a[76:77], v233 offset:37888\n"
    "v_mfma_f32_16x16x32_bf16 a[180:183], a[16:19], a[108:111], a[180:183]\n"
    "ds_read_b64_tr_b16 a[78:79], v233 offset:38400\n"
    "v_mfma_f32_16x16x32_bf16 a[184:187], a[20:23], a[80:83], a[184:187]\n"
    "v_mfma_f32_16x16x32_bf16 a[188:191], a[20:23], a[84:87], a[188:191]\n"
    "v_mfma_f32_16x16x32_bf16 a[192:195], a[20:23], a[88:91], a[192:195]\n"
    "s_add_u32 m0, s62, 20352\n"
    "v_mfma_f32_16x16x32_bf16 a[196:199], a[20:23], a[92:95], a[196:199]\n"
    "buffer_load_dwordx4 v236, s[24:27], s28 offen offset:128 lds\n"
    "v_mfma_f32_16x16x32_bf16 a[200:203], a[20:23], a[96:99], a[200:203]\n"
    "s_add_u32 s28, s28, 131072\n"
    "v_mfma_f32_16x16x32_bf16 a[204:207], a[20:23], a[100:103], a[204:207]\n"
    "v_mfma_f32_16x16x32_bf16 a[208:211], a[20:23], a[104:107], a[208:211]\n"
    "v_mfma_f32_16x16x32_bf16 a[212:215], a[20:23], a[108:111], a[212:215]\n"
    "v_mfma_f32_16x16x32_bf16 v[128:131], a[24:27], a[80:83], v[128:131]\n"
    "v_mfma_f32_16x16x32_bf16 v[132:135], a[24:27], a[84:87], v[132:135]\n"
    "v_mfma_f32_16x16x32_bf16 v[136:139], a[24:27], a[88:91], v[136:139]\n"
    "s_add_u32 m0, s62, 24576\n"
    "v_mfma_f32_16x16x32_bf16 v[140:143], a[24:27], a[92:95], v[140:143]\n"
    "buffer_load_dwordx4 v236, s[24:27], s29 offen lds\n"
    "v_mfma_f32_16x16x32_bf16 v[144:147], a[24:27], a[96:99], v[144:147]\n"
    "v_mfma_f32_16x16x32_bf16 v[148:151], a[24:27], a[100:103], v[148:151]\n"
    "v_mfma_f32_16x16x32_bf16 v[152:155], a[24:27], a[104:107], v[152:155]\n"
    "v_mfma_f32_16x16x32_bf16 v[156:159], a[24:27], a[108:111], v[156:159]\n"
    "v_mfma_f32_16x16x32_bf16 v[160:163], a[28:31], a[80:83], v[160:163]\n"
    "v_mfma_f32_16x16x32_bf16 v[164:167], a[28:31], a[84:87], v[164:167]\n"
    "v_mfma_f32_16x16x32_bf16 v[168:171], a[28:31], a[88:91], v[168:171]\n"
    "s_add_u32 m0, s62, 28544\n"
    "v_mfma_f32_16x16x32_bf16 v[172:175], a[28:31], a[92:95], v[172:175]\n"
    "buffer_load_dwordx4 v236, s[24:27], s29 offen offset:128 lds\n"
    "v_mfma_f32_16x16x32_bf16 v[176:179], a[28:31], a[96:99], v[176:179]\n"
    "s_add_u32 s29, s29, 131072\n"
    "v_mfma_f32_16x16x32_bf16 v[180:183], a[28:31], a[100:103], v[180:183]\n"
    "v_mfma_f32_16x16x32_bf16 v[184:187], a[28:31], a[104:107], v[184:187]\n"
    "v_mfma_f32_16x16x32_bf16 v[188:191], a[28:31], a[108:111], v[188:191]\n"
    "v_mfma_f32_16x16x32_bf16 a[216:219], a[32:35], a[80:83], a[216:219]\n"
    "v_mfma_f32_16x16x32_bf16 a[220:223], a[32:35], a[84:87], a[220:223]\n"
    "v_mfma_f32_16x16x32_bf16 a[224:227], a[32:35], a[88:91], a[224:227]\n"
    "v_mfma_f32_16x16x32_bf16 a[228:231], a[32:35], a[92:95], a[228:231]\n"
    "v_mfma_f32_16x16x32_bf16 a[232:235], a[32:35], a[96:99], a[232:235]\n"
    "v_mfma_f32_16x16x32_bf16 a[236:239], a[32:35], a[100:103], a[236:239]\n"
    "v_mfma_f32_16x16x32_bf16 a[240:243], a[32:35], a[104:107], a[240:243]\n"
    "v_mfma_f32_16x16x32_bf16 a[244:247], a[32:35], a[108:111], a[244:247]\n"
    "v_mfma_f32_16x16x32_bf16 v[192:195], a[36:39], a[80:83], v[192:195]\n"
    "v_mfma_f32_16x16x32_bf16 v[196:199], a[36:39], a[84:87], v[196:199]\n"
    "v_mfma_f32_16x16x32_bf16 v[200:203], a[36:39], a[88:91], v[200:203]\n"
    "v_mfma_f32_16x16x32_bf16 v[204:207], a[36:39], a[92:95], v[204:207]\n"
    "v_mfma_f32_16x16x32_bf16 v[208:211], a[36:39], a[96:99], v[208:211]\n"
    "v_mfma_f32_16x16x32_bf16 v[212:215], a[36:39], a[100:103], v[212:215]\n"
    "v_mfma_f32_16x16x32_bf16 v[216:219], a[36:39], a[104:107], v[216:219]\n"
    "v_mfma_f32_16x16x32_bf16 v[220:223], a[36:39], a[108:111], v[220:223]\n"
    "s_waitcnt vmcnt(18) lgkmcnt(0)\n"
    "s_barrier\n"
    "v_mfma_f32_16x16x32_bf16 v[0:3], a[40:43], a[116:119], v[0:3]\n"
    "ds_read_b128 a[80:83], v225 offset:1024\n"
    "v_mfma_f32_16x16x32_bf16 v[4:7], a[40:43], a[120:123], v[4:7]\n"
    "ds_read_b128 a[84:87], v225 offset:2048\n"
    "v_mfma_f32_16x16x32_bf16 v[8:11], a[40:43], a[124:127], v[8:11]\n"
    "s_add_u32 m0, s62, 32768\n"
    "v_mfma_f32_16x16x32_bf16 v[12:15], a[40:43], a[128:131], v[12:15]\n"
    "buffer_load_dwordx4 v236, s[24:27], s28 offen lds\n"
    "v_mfma_f32_16x16x32_bf16 v[16:19], a[40:43], a[132:135], v[16:19]\n"
    "ds_read_b128 a[88:91], v225 offset:3072\n"
    "v_mfma_f32_16x16x32_bf16 v[20:23], a[40:43], a[136:139], v[20:23]\n"
    "ds_read_b128 a[92:95], v225 offset:4096\n"
    "v_mfma_f32_16x16x32_bf16 v[24:27], a[40:43], a[140:143], v[24:27]\n"
    "ds_read_b128 a[96:99], v225 offset:5120\n"
    "v_mfma_f32_16x16x32_bf16 v[28:31], a[40:43], a[144:147], v[28:31]\n"
    "ds_read_b128 a[100:103], v225 offset:6144\n"
    "v_mfma_f32_16x16x32_bf16 v[32:35], a[44:47], a[116:119], v[32:35]\n"
    "ds_read_b128 a[104:107], v225 offset:7168\n"
    "v_mfma_f32_16x16x32_bf16 v[36:39], a[44:47], a[120:123], v[36:39]\n"
    "ds_read_b128 a[108:111], v225 offset:8192\n"
    "v_mfma_f32_16x16x32_bf16 v[40:43], a[44:47], a[124:127], v[40:43]\n"
    "s_add_u32 m0, s62, 36736\n"
    "v_mfma_f32_16x16x32_bf16 v[44:47], a[44:47], a[128:131], v[44:47]\n"
    "buffer_load_dwordx4 v236, s[24:27], s28 offen offset:128 lds\n"
    "v_mfma_f32_16x16x32_bf16 v[48:51], a[44:47], a[132:135], v[48:51]\n"
    "s_add_u32 s28, s28, 131072\n"
    "v_mfma_f32_16x16x32_bf16 v[52:55], a[44:47], a[136:139], v[52:55]\n"
    "ds_read_b64_tr_b16 a[0:1], v230 offset:0\n"
    "v_mfma_f32_16x16x32_bf16 v[56:59], a[44:47], a[140:143], v[56:59]\n"
    "ds_read_b64_tr_b16 a[2:3], v230 offset:512\n"
    "v_mfma_f32_16x16x32_bf16 v[60:63], a[44:47], a[144:147], v[60:63]\n"
    "ds_read_b64_tr_b16 a[4:5], v231 offset:0\n"
    "v_mfma_f32_16x16x32_bf16 v[64:67], a[48:51], a[116:119], v[64:67]\n"
    "ds_read_b64_tr_b16 a[6:7], v231 offset:512\n"
    "v_mfma_f32_16x16x32_bf16 v[68:71], a[48:51], a[120:123], v[68:71]\n"
    "ds_read_b64_tr_b16 a[8:9], v230 offset:4096\n"
    "v_mfma_f32_16x16x32_bf16 v[72:75], a[48:51], a[124:127], v[72:75]\n"
    "s_add_u32 m0, s62, 40960\n"
    "v_mfma_f32_16x16x32_bf16 v[76:79], a[48:51], a[128:131], v[76:79]\n"
    "buffer_load_dwordx4 v236, s[24:27], s29 offen lds\n"
    "v_mfma_f32_16x16x32_bf16 v[80:83], a[48:51], a[132:135], v[80:83]\n"
    "ds_read_b64_tr_b16 a[10:11], v230 offset:4608\n"
    "v_mfma_f32_16x16x32_bf16 v[84:87], a[48:51], a[136:139], v[84:87]\n"
    "ds_read_b64_tr_b16 a[12:13], v231 offset:4096\n"
    "v_mfma_f32_16x16x32_bf16 v[88:91], a[48:51], a[140:143], v[88:91]\n"
    "ds_read_b64_tr_b16 a[14:15], v231 offset:4608\n"
    "v_mfma_f32_16x16x32_bf16 v[92:95], a[48:51], a[144:147], v[92:95]\n"
    "ds_read_b64_tr_b16 a[16:17], v230 offset:8192\n"
    "v_mfma_f32_16x16x32_bf16 v[96:99], a[52:55], a[116:119], v[96:99]\n"
    "ds_read_b64_tr_b16 a[18:19], v230 offset:8704\n"
    "v_mfma_f32_16x16x32_bf16 v[100:103], a[52:55], a[120:123], v[100:103]\n"
    "ds_read_b64_tr_b16 a[20:21], v231 offset:8192\n"
    "v_mfma_f32_16x16x32_bf16 v[104:107], a[52:55], a[124:127], v[104:107]\n"
    "s_add_u32 m0, s62, 44928\n"
    "v_mfma_f32_16x16x32_bf16 v[108:111], a[52:55], a[128:131], v[108:111]\n"
    "buffer_load_dwordx4 v236, s[24:27], s29 offen offset:128 lds\n"
    "v_mfma_f32_16x16x32_bf16 v[112:115], a[52:55], a[132:135], v[112:115]\n"
    "s_add_u32 s29, s29, 131072\n"
    "v_mfma_f32_16x16x32_bf16 v[116:119], a[52:55], a[136:139], v[116:119]\n"
    "ds_read_b64_tr_b16 a[22:23], v231 offset:8704\n"
    "v_mfma_f32_16x16x32_bf16 v[120:123], a[52:55], a[140:143], v[120:123]\n"
    "ds_read_b64_tr_b16 a[24:25], v230 offset:12288\n"
    "v_mfma_f32_16x16x32_bf16 v[124:127], a[52:55], a[144:147], v[124:127]\n"
    "ds_read_b64_tr_b16 a[26:27], v230 offset:12800\n"
    "v_mfma_f32_16x16x32_bf16 a[152:155], a[56:59], a[116:119], a[152:155]\n"
    "ds_read_b64_tr_b16 a[28:29], v231 offset:12288\n"
    "v_mfma_f32_16x16x32_bf16 a[156:159], a[56:59], a[120:123], a[156:159]\n"
    "ds_read_b64_tr_b16 a[30:31], v231 offset:12800\n"
    "v_mfma_f32_16x16x32_bf16 a[160:163], a[56:59], a[124:127], a[160:163]\n"
    "s_add_u32 m0, s62, 49152\n"
    "v_mfma_f32_16x16x32_bf16 a[164:167], a[56:59], a[128:131], a[164:167]\n"
    "buffer_load_dwordx4 v236, s[24:27], s28 offen lds\n"
    "v_mfma_f32_16x16x32_bf16 a[168:171], a[56:59], a[132:135], a[168:171]\n"
    "ds_read_b64_tr_b16 a[32:33], v234 offset:0\n"
    "v_mfma_f32_16x16x32_bf16 a[172:175], a[56:59], a[136:139], a[172:175]\n"
    "ds_read_b64_tr_b16 a[34:35], v234 offset:512\n"
    "v_mfma_f32_16x16x32_bf16 a[176:179], a[56:59], a[140:143], a[176:179]\n"
    "ds_read_b64_tr_b16 a[36:37], v235 offset:0\n"
    "v_mfma_f32_16x16x32_bf16 a[180:183], a[56:59], a[144:147], a[180:183]\n"
    "ds_read_b64_tr_b16 a[38:39], v235 offset:512\n"
    "v_mfma_f32_16x16x32_bf16 a[184:187], a[60:63], a[116:119], a[184:187]\n"
    "v_mfma_f32_16x16x32_bf16 a[188:191], a[60:63], a[120:123], a[188:191]\n"
    "v_mfma_f32_16x16x32_bf16 a[192:195], a[60:63], a[124:127], a[192:195]\n"
    "s_add_u32 m0, s62, 53120\n"
    "v_mfma_f32_16x16x32_bf16 a[196:199], a[60:63], a[128:131], a[196:199]\n"
    "buffer_load_dwordx4 v236, s[24:27], s28 offen offset:128 lds\n"
    "v_mfma_f32_16x16x32_bf16 a[200:203], a[60:63], a[132:135], a[200:203]\n"
    "s_add_u32 s28, s28, 131072\n"
    "v_mfma_f32_16x16x32_bf16 a[204:207], a[60:63], a[136:139], a[204:207]\n"
    "v_mfma_f32_16x16x32_bf16 a[208:211], a[60:63], a[140:143], a[208:211]\n"
    "v_mfma_f32_16x16x32_bf16 a[212:215], a[60:63], a[144:147], a[212:215]\n"
    "v_mfma_f32_16x16x32_bf16 v[128:131], a[64:67], a[116:119], v[128:131]\n"
    "v_mfma_f32_16x16x32_bf16 v[132:135], a[64:67], a[120:123], v[132:135]\n"
    "v_mfma_f32_16x16x32_bf16 v[136:139], a[64:67], a[124:127], v[136:139]\n"
    "s_add_u32 m0, s62, 57344\n"
    "v_mfma_f32_16x16x32_bf16 v[140:143], a[64:67], a[128:131], v[140:143]\n"
    "buffer_load_dwordx4 v236, s[24:27], s29 offen lds\n"
    "v_mfma_f32_16x16x32_bf16 v[144:147], a[64:67], a[132:135], v[144:147]\n"
    "v_mfma_f32_16x16x32_bf16 v[148:151], a[64:67], a[136:139], v[148:151]\n"
    "v_mfma_f32_16x16x32_bf16 v[152:155], a[64:67], a[140:143], v[152:155]\n"
    "v_mfma_f32_16x16x32_bf16 v[156:159], a[64:67], a[144:147], v[156:159]\n"
    "v_mfma_f32_16x16x32_bf16 v[160:163], a[68:71], a[116:119], v[160:163]\n"
    "v_mfma_f32_16x16x32_bf16 v[164:167], a[68:71], a[120:123], v[164:167]\n"
    "v_mfma_f32_16x16x32_bf16 v[168:171], a[68:71], a[124:127], v[168:171]\n"
    "s_add_u32 m0, s62, 61312\n"
    "v_mfma_f32_16x16x32_bf16 v[172:175], a[68:71], a[128:131], v[172:175]\n"
    "buffer_load_dwordx4 v236, s[24:27], s29 offen offset:128 lds\n"
    "v_mfma_f32_16x16x32_bf16 v[176:179], a[68:71], a[132:135], v[176:179]\n"
    "s_add_u32 s29, s29, 131072\n"
    "v_mfma_f32_16x16x32_bf16 v[180:183], a[68:71], a[136:139], v[180:183]\n"
    "v_mfma_f32_16x16x32_bf16 v[184:187], a[68:71], a[140:143], v[184:187]\n"
    "v_mfma_f32_16x16x32_bf16 v[188:191], a[68:71], a[144:147], v[188:191]\n"
    "v_mfma_f32_16x16x32_bf16 a[216:219], a[72:75], a[116:119], a[216:219]\n"
    "v_mfma_f32_16x16x32_bf16 a[220:223], a[72:75], a[120:123], a[220:223]\n"
    "v_mfma_f32_16x16x32_bf16 a[224:227], a[72:75], a[124:127], a[224:227]\n"
    "v_mfma_f32_16x16x32_bf16 a[228:231], a[72:75], a[128:131], a[228:231]\n"
    "v_mfma_f32_16x16x32_bf16 a[232:235], a[72:75], a[132:135], a[232:235]\n"
    "v_mfma_f32_16x16x32_bf16 a[236:239], a[72:75], a[136:139], a[236:239]\n"
    "v_mfma_f32_16x16x32_bf16 a[240:243], a[72:75], a[140:143], a[240:243]\n"
    "v_mfma_f32_16x16x32_bf16 a[244:247], a[72:75], a[144:147], a[244:247]\n"
    "v_mfma_f32_16x16x32_bf16 v[192:195], a[76:79], a[116:119], v[192:195]\n"
    "v_mfma_f32_16x16x32_bf16 v[196:199], a[76:79], a[120:123], v[196:199]\n"
    "v_mfma_f32_16x16x32_bf16 v[200:203], a[76:79], a[124:127], v[200:203]\n"
    "v_mfma_f32_16x16x32_bf16 v[204:207], a[76:79], a[128:131], v[204:207]\n"
    "v_mfma_f32_16x16x32_bf16 v[208:211], a[76:79], a[132:135], v[208:211]\n"
    "v_mfma_f32_16x16x32_bf16 v[212:215], a[76:79], a[136:139], v[212:215]\n"
    "v_mfma_f32_16x16x32_bf16 v[216:219], a[76:79], a[140:143], v[216:219]\n"
    "v_mfma_f32_16x16x32_bf16 v[220:223], a[76:79], a[144:147], v[220:223]\n"
    "s_waitcnt vmcnt(16) lgkmcnt(0)\n"
    "s_barrier\n"
    "v_mfma_f32_16x16x32_bf16 a[152:155], a[16:19], a[80:83], a[152:155]\n"
    "ds_read_b128 a[116:119], v225 offset:38912\n"
    "v_mfma_f32_16x16x32_bf16 a[156:159], a[16:19], a[84:87], a[156:159]\n"
    "ds_read_b128 a[120:123], v225 offset:39936\n"
    "v_mfma_f32_16x16x32_bf16 a[160:163], a[16:19], a[88:91], a[160:163]\n"
    "ds_read_b128 a[124:127], v225 offset:40960\n"
    "v_mfma_f32_16x16x32_bf16 a[164:167], a[16:19], a[92:95], a[164:167]\n"
    "ds_read_b128 a[128:131], v225 offset:41984\n"
    "v_mfma_f32_16x16x32_bf16 a[184:187], a[20:23], a[80:83], a[184:187]\n"
    "ds_read_b64_tr_b16 a[56:57], v230 offset:46080\n"
    "v_mfma_f32_16x16x32_bf16 a[188:191], a[20:23], a[84:87], a[188:191]\n"
    "ds_read_b64_tr_b16 a[58:59], v230 offset:46592\n"
    "v_mfma_f32_16x16x32_bf16 a[192:195], a[20:23], a[88:91], a[192:195]\n"
    "ds_read_b64_tr_b16 a[60:61], v231 offset:46080\n"
    "v_mfma_f32_16x16x32_bf16 a[196:199], a[20:23], a[92:95], a[196:199]\n"
    "ds_read_b64_tr_b16 a[62:63], v231 offset:46592\n"
    "v_mfma_f32_16x16x32_bf16 v[128:131], a[24:27], a[80:83], v[128:131]\n"
    "ds_read_b64_tr_b16 a[64:65], v230 offset:50176\n"
    "v_mfma_f32_16x16x32_bf16 v[132:135], a[24:27], a[84:87], v[132:135]\n"
    "ds_read_b64_tr_b16 a[66:67], v230 offset:50688\n"
    "v_mfma_f32_16x16x32_bf16 v[136:139], a[24:27], a[88:91], v[136:139]\n"
    "ds_read_b64_tr_b16 a[68:69], v231 offset:50176\n"
    "v_mfma_f32_16x16x32_bf16 v[140:143], a[24:27], a[92:95], v[140:143]\n"
    "ds_read_b64_tr_b16 a[70:71], v231 offset:50688\n"
    "v_mfma_f32_16x16x32_bf16 v[160:163], a[28:31], a[80:83], v[160:163]\n"
    "ds_read_b128 a[132:135], v225 offset:43008\n"
    "v_mfma_f32_16x16x32_bf16 v[164:167], a[28:31], a[84:87], v[164:167]\n"
    "s_add_u32 m0, s62, 65536\n"
    "ds_read_b128 a[136:139], v225 offset:44032\n"
    "v_mfma_f32_16x16x32_bf16 v[168:171], a[28:31], a[88:91], v[168:171]\n"
    "buffer_load_dwordx4 v236, s[24:27], s28 offen lds\n"
    "v_mfma_f32_16x16x32_bf16 v[172:175], a[28:31], a[92:95], v[172:175]\n"
    "ds_read_b128 a[140:143], v225 offset:45056\n"
    "s_waitcnt lgkmcnt(14)\n"
    "s_waitcnt lgkmcnt(9)\n"
    "v_mfma_f32_16x16x32_bf16 a[152:155], a[56:59], a[116:119], a[152:155]\n"
    "ds_read_b128 a[144:147], v225 offset:46080\n"
    "v_mfma_f32_16x16x32_bf16 a[156:159], a[56:59], a[120:123], a[156:159]\n"
    "s_add_u32 m0, s62, 69504\n"
    "ds_read_b64_tr_b16 a[40:41], v230 offset:37888\n"
    "v_mfma_f32_16x16x32_bf16 a[160:163], a[56:59], a[124:127], a[160:163]\n"
    "buffer_load_dwordx4 v236, s[24:27], s28 offen offset:128 lds\n"
    "v_mfma_f32_16x16x32_bf16 a[164:167], a[56:59], a[128:131], a[164:167]\n"
    "s_add_u32 s28, s28, 131072\n"
    "ds_read_b64_tr_b16 a[42:43], v230 offset:38400\n"
    "s_waitcnt lgkmcnt(10)\n"
    "v_mfma_f32_16x16x32_bf16 a[184:187], a[60:63], a[116:119], a[184:187]\n"
    "ds_read_b64_tr_b16 a[44:45], v231 offset:37888\n"
    "v_mfma_f32_16x16x32_bf16 a[188:191], a[60:63], a[120:123], a[188:191]\n"
    "s_add_u32 m0, s62, 73728\n"
    "ds_read_b64_tr_b16 a[46:47], v231 offset:38400\n"
    "v_mfma_f32_16x16x32_bf16 a[192:195], a[60:63], a[124:127], a[192:195]\n"
    "buffer_load_dwordx4 v236, s[24:27], s29 offen lds\n"
    "v_mfma_f32_16x16x32_bf16 a[196:199], a[60:63], a[128:131], a[196:199]\n"
    "ds_read_b64_tr_b16 a[48:49], v230 offset:41984\n"
    "s_waitcnt lgkmcnt(11)\n"
    "v_mfma_f32_16x16x32_bf16 v[128:131], a[64:67], a[116:119], v[128:131]\n"
    "ds_read_b64_tr_b16 a[50:51], v230 offset:42496\n"
    "v_mfma_f32_16x16x32_bf16 v[132:135], a[64:67], a[120:123], v[132:135]\n"
    "s_add_u32 m0, s62, 77696\n"
    "ds_read_b64_tr_b16 a[52:53], v231 offset:41984\n"
    "v_mfma_f32_16x16x32_bf16 v[136:139], a[64:67], a[124:127], v[136:139]\n"
    "buffer_load_dwordx4 v236, s[24:27], s29 offen offset:128 lds\n"
    "v_mfma_f32_16x16x32_bf16 v[140:143], a[64:67], a[128:131], v[140:143]\n"
    "s_add_u32 s29, s29, 131072\n"
    "ds_read_b64_tr_b16 a[54:55], v231 offset:42496\n"
    "s_waitcnt lgkmcnt(12)\n"
    "v_mfma_f32_16x16x32_bf16 v[160:163], a[68:71], a[116:119], v[160:163]\n"
    "ds_read_b64_tr_b16 a[72:73], v234 offset:37888\n"
    "v_mfma_f32_16x16x32_bf16 v[164:167], a[68:71], a[120:123], v[164:167]\n"
    "s_add_u32 m0, s62, 81920\n"
    "ds_read_b64_tr_b16 a[74:75], v234 offset:38400\n"
    "v_mfma_f32_16x16x32_bf16 v[168:171], a[68:71], a[124:127], v[168:171]\n"
    "buffer_load_dwordx4 v236, s[24:27], s28 offen lds\n"
    "v_mfma_f32_16x16x32_bf16 v[172:175], a[68:71], a[128:131], v[172:175]\n"
    "ds_read_b64_tr_b16 a[76:77], v235 offset:37888\n"
    "v_mfma_f32_16x16x32_bf16 a[168:171], a[16:19], a[96:99], a[168:171]\n"
    "ds_read_b64_tr_b16 a[78:79], v235 offset:38400\n"
    "v_mfma_f32_16x16x32_bf16 a[172:175], a[16:19], a[100:103], a[172:175]\n"
    "s_add_u32 m0, s62, 85888\n"
    "v_mfma_f32_16x16x32_bf16 a[176:179], a[16:19], a[104:107], a[176:179]\n"
    "buffer_load_dwordx4 v236, s[24:27], s28 offen offset:128 lds\n"
    "v_accvgpr_read_b32 v246, a152\n"
    "v_accvgpr_read_b32 v247, a153\n"
    "v_mfma_f32_16x16x32_bf16 a[180:183], a[16:19], a[108:111], a[180:183]\n"
    "s_add_u32 s28, s28, 131072\n"
    "v_accvgpr_read_b32 v248, a154\n"
    "v_accvgpr_read_b32 v249, a155\n"
    "v_mfma_f32_16x16x32_bf16 a[200:203], a[20:23], a[96:99], a[200:203]\n"
    "v_accvgpr_read_b32 v250, a184\n"
    "v_accvgpr_read_b32 v251, a185\n"
    "v_mfma_f32_16x16x32_bf16 a[204:207], a[20:23], a[100:103], a[204:207]\n"
    "s_add_u32 m0, s62, 90112\n"
    "v_accvgpr_read_b32 v252, a186\n"
    "v_accvgpr_read_b32 v253, a187\n"
    "v_mfma_f32_16x16x32_bf16 a[208:211], a[20:23], a[104:107], a[208:211]\n"
    "buffer_load_dwordx4 v236, s[24:27], s29 offen lds\n"
    "v_pk_mul_f32 v[128:129], v[128:129], v[246:247]\n"
    "v_pk_mul_f32 v[130:131], v[130:131], v[248:249]\n"
    "v_mfma_f32_16x16x32_bf16 a[212:215], a[20:23], a[108:111], a[212:215]\n"
    "v_pk_mul_f32 v[160:161], v[160:161], v[250:251]\n"
    "v_pk_mul_f32 v[162:163], v[162:163], v[252:253]\n"
    "v_mfma_f32_16x16x32_bf16 v[144:147], a[24:27], a[96:99], v[144:147]\n"
    "v_cvt_pk_bf16_f32 v128, v128, v129\n"
    "v_cvt_pk_bf16_f32 v129, v130, v131\n"
    "v_mfma_f32_16x16x32_bf16 v[148:151], a[24:27], a[100:103], v[148:151]\n"
    "s_add_u32 m0, s62, 94080\n"
    "v_cvt_pk_bf16_f32 v130, v160, v161\n"
    "v_cvt_pk_bf16_f32 v131, v162, v163\n"
    "v_mfma_f32_16x16x32_bf16 v[152:155], a[24:27], a[104:107], v[152:155]\n"
    "buffer_load_dwordx4 v236, s[24:27], s29 offen offset:128 lds\n"
    "buffer_store_dwordx4 v[128:131], v244, s[80:83], s86 offen sc1\n"
    "v_mfma_f32_16x16x32_bf16 v[156:159], a[24:27], a[108:111], v[156:159]\n"
    "s_add_u32 s29, s29, 131072\n"
    "v_accvgpr_read_b32 v246, a156\n"
    "v_accvgpr_read_b32 v247, a157\n"
    "v_mfma_f32_16x16x32_bf16 v[176:179], a[28:31], a[96:99], v[176:179]\n"
    "v_accvgpr_read_b32 v248, a158\n"
    "v_accvgpr_read_b32 v249, a159\n"
    "v_mfma_f32_16x16x32_bf16 v[180:183], a[28:31], a[100:103], v[180:183]\n"
    "v_accvgpr_read_b32 v250, a188\n"
    "v_accvgpr_read_b32 v251, a189\n"
    "v_mfma_f32_16x16x32_bf16 v[184:187], a[28:31], a[104:107], v[184:187]\n"
    "v_accvgpr_read_b32 v252, a190\n"
    "v_accvgpr_read_b32 v253, a191\n"
    "v_mfma_f32_16x16x32_bf16 v[188:191], a[28:31], a[108:111], v[188:191]\n"
    "v_pk_mul_f32 v[132:133], v[132:133], v[246:247]\n"
    "v_pk_mul_f32 v[134:135], v[134:135], v[248:249]\n"
    "s_waitcnt lgkmcnt(0)\n"
    "s_barrier\n"
    "v_mfma_f32_16x16x32_bf16 a[168:171], a[56:59], a[132:135], a[168:171]\n"
    "v_pk_mul_f32 v[164:165], v[164:165], v[250:251]\n"
    "v_pk_mul_f32 v[166:167], v[166:167], v[252:253]\n"
    "v_mfma_f32_16x16x32_bf16 a[172:175], a[56:59], a[136:139], a[172:175]\n"
    "s_add_u32 m0, s62, 98304\n"
    "v_cvt_pk_bf16_f32 v132, v132, v133\n"
    "v_cvt_pk_bf16_f32 v133, v134, v135\n"
    "v_mfma_f32_16x16x32_bf16 a[176:179], a[56:59], a[140:143], a[176:179]\n"
    "buffer_load_dwordx4 v236, s[24:27], s28 offen lds\n"
    "v_cvt_pk_bf16_f32 v134, v164, v165\n"
    "v_cvt_pk_bf16_f32 v135, v166, v167\n"
    "v_mfma_f32_16x16x32_bf16 a[180:183], a[56:59], a[144:147], a[180:183]\n"
    "s_add_u32 m0, s62, 102272\n"
    "buffer_store_dwordx4 v[132:135], v244, s[80:83], s86 offen offset:1024 sc1\n"
    "v_mfma_f32_16x16x32_bf16 a[200:203], a[60:63], a[132:135], a[200:203]\n"
    "buffer_load_dwordx4 v236, s[24:27], s28 offen offset:128 lds\n"
    "v_accvgpr_read_b32 v246, a160\n"
    "v_accvgpr_read_b32 v247, a161\n"
    "v_mfma_f32_16x16x32_bf16 a[204:207], a[60:63], a[136:139], a[204:207]\n"
    "s_add_u32 s28, s28, 131072\n"
    "s_add_u32 m0, s62, 106496\n"
    "v_accvgpr_read_b32 v248, a162\n"
    "v_accvgpr_read_b32 v249, a163\n"
    "v_mfma_f32_16x16x32_bf16 a[208:211], a[60:63], a[140:143], a[208:211]\n"
    "buffer_load_dwordx4 v236, s[24:27], s29 offen lds\n"
    "v_accvgpr_read_b32 v250, a192\n"
    "v_accvgpr_read_b32 v251, a193\n"
    "v_mfma_f32_16x16x32_bf16 a[212:215], a[60:63], a[144:147], a[212:215]\n"
    "s_add_u32 m0, s62, 110464\n"
    "v_accvgpr_read_b32 v252, a194\n"
    "v_accvgpr_read_b32 v253, a195\n"
    "v_mfma_f32_16x16x32_bf16 v[144:147], a[64:67], a[132:135], v[144:147]\n"
    "buffer_load_dwordx4 v236, s[24:27], s29 offen offset:128 lds\n"
    "v_pk_mul_f32 v[136:137], v[136:137], v[246:247]\n"
    "v_pk_mul_f32 v[138:139], v[138:139], v[248:249]\n"
    "v_mfma_f32_16x16x32_bf16 v[148:151], a[64:67], a[136:139], v[148:151]\n"
    "s_add_u32 s29, s29, 131072\n"
    "s_add_u32 m0, s62, 114688\n"
    "v_pk_mul_f32 v[168:169], v[168:169], v[250:251]\n"
    "v_pk_mul_f32 v[170:171], v[170:171], v[252:253]\n"
    "v_mfma_f32_16x16x32_bf16 v[152:155], a[64:67], a[140:143], v[152:155]\n"
    "buffer_load_dwordx4 v236, s[24:27], s28 offen lds\n"
    "v_cvt_pk_bf16_f32 v136, v136, v137\n"
    "v_cvt_pk_bf16_f32 v137, v138, v139\n"
    "v_mfma_f32_16x16x32_bf16 v[156:159], a[64:67], a[144:147], v[156:159]\n"
    "s_add_u32 m0, s62, 118656\n"
    "v_cvt_pk_bf16_f32 v138, v168, v169\n"
    "v_cvt_pk_bf16_f32 v139, v170, v171\n"
    "v_mfma_f32_16x16x32_bf16 v[176:179], a[68:71], a[132:135], v[176:179]\n"
    "buffer_load_dwordx4 v236, s[24:27], s28 offen offset:128 lds\n"
    "buffer_store_dwordx4 v[136:139], v244, s[80:83], s86 offen offset:2048 sc1\n"
    "v_mfma_f32_16x16x32_bf16 v[180:183], a[68:71], a[136:139], v[180:183]\n"
    "s_add_u32 s28, s28, 131072\n"
    "s_add_u32 m0, s62, 122880\n"
    "v_accvgpr_read_b32 v246, a164\n"
    "v_accvgpr_read_b32 v247, a165\n"
    "v_mfma_f32_16x16x32_bf16 v[184:187], a[68:71], a[140:143], v[184:187]\n"
    "buffer_load_dwordx4 v236, s[24:27], s29 offen lds\n"
    "v_accvgpr_read_b32 v248, a166\n"
    "v_accvgpr_read_b32 v249, a167\n"
    "v_mfma_f32_16x16x32_bf16 v[188:191], a[68:71], a[144:147], v[188:191]\n"
    "s_add_u32 m0, s62, 126848\n"
    "v_accvgpr_read_b32 v250, a196\n"
    "v_accvgpr_read_b32 v251, a197\n"
    "v_mfma_f32_16x16x32_bf16 v[0:3], a[0:3], a[80:83], v[0:3]\n"
    "buffer_load_dwordx4 v236, s[24:27], s29 offen offset:128 lds\n"
    "v_accvgpr_read_b32 v252, a198\n"
    "v_accvgpr_read_b32 v253, a199\n"
    "v_mfma_f32_16x16x32_bf16 v[4:7], a[0:3], a[84:87], v[4:7]\n"
    "s_add_u32 s29, s29, 131072\n"
    "s_add_u32 m0, s62, 131072\n"
    "v_pk_mul_f32 v[140:141], v[140:141], v[246:247]\n"
    "v_pk_mul_f32 v[142:143], v[142:143], v[248:249]\n"
    "v_mfma_f32_16x16x32_bf16 v[8:11], a[0:3], a[88:91], v[8:11]\n"
    "buffer_load_dwordx4 v236, s[24:27], s28 offen lds\n"
    "v_pk_mul_f32 v[172:173], v[172:173], v[250:251]\n"
    "v_pk_mul_f32 v[174:175], v[174:175], v[252:253]\n"
    "v_mfma_f32_16x16x32_bf16 v[12:15], a[0:3], a[92:95], v[12:15]\n"
    "s_add_u32 m0, s62, 135040\n"
    "v_cvt_pk_bf16_f32 v140, v140, v141\n"
    "v_cvt_pk_bf16_f32 v141, v142, v143\n"
    "v_mfma_f32_16x16x32_bf16 v[32:35], a[4:7], a[80:83], v[32:35]\n"
    "buffer_load_dwordx4 v236, s[24:27], s28 offen offset:128 lds\n"
    "v_cvt_pk_bf16_f32 v142, v172, v173\n"
    "v_cvt_pk_bf16_f32 v143, v174, v175\n"
    "v_mfma_f32_16x16x32_bf16 v[36:39], a[4:7], a[84:87], v[36:39]\n"
    "s_add_u32 s28, s28, 131072\n"
    "s_add_u32 m0, s62, 139264\n"
    "buffer_store_dwordx4 v[140:143], v244, s[80:83], s86 offen offset:3072 sc1\n"
    "v_mfma_f32_16x16x32_bf16 v[40:43], a[4:7], a[88:91], v[40:43]\n"
    "buffer_load_dwordx4 v236, s[24:27], s29 offen lds\n"
    "v_accvgpr_read_b32 v246, a168\n"
    "v_accvgpr_read_b32 v247, a169\n"
    "v_mfma_f32_16x16x32_bf16 v[44:47], a[4:7], a[92:95], v[44:47]\n"
    "s_add_u32 m0, s62, 143232\n"
    "v_accvgpr_read_b32 v248, a170\n"
    "v_accvgpr_read_b32 v249, a171\n"
    "v_mfma_f32_16x16x32_bf16 v[64:67], a[8:11], a[80:83], v[64:67]\n"
    "buffer_load_dwordx4 v236, s[24:27], s29 offen offset:128 lds\n"
    "v_accvgpr_read_b32 v250, a200\n"
    "v_accvgpr_read_b32 v251, a201\n"
    "v_mfma_f32_16x16x32_bf16 v[68:71], a[8:11], a[84:87], v[68:71]\n"
    "s_add_u32 s29, s29, 131072\n"
    "v_accvgpr_read_b32 v252, a202\n"
    "v_accvgpr_read_b32 v253, a203\n"
    "v_mfma_f32_16x16x32_bf16 v[72:75], a[8:11], a[88:91], v[72:75]\n"
    "v_pk_mul_f32 v[144:145], v[144:145], v[246:247]\n"
    "v_pk_mul_f32 v[146:147], v[146:147], v[248:249]\n"
    "v_mfma_f32_16x16x32_bf16 v[76:79], a[8:11], a[92:95], v[76:79]\n"
    "v_pk_mul_f32 v[176:177], v[176:177], v[250:251]\n"
    "v_pk_mul_f32 v[178:179], v[178:179], v[252:253]\n"
    "v_mfma_f32_16x16x32_bf16 v[96:99], a[12:15], a[80:83], v[96:99]\n"
    "v_cvt_pk_bf16_f32 v144, v144, v145\n"
    "v_cvt_pk_bf16_f32 v145, v146, v147\n"
    "v_mfma_f32_16x16x32_bf16 v[100:103], a[12:15], a[84:87], v[100:103]\n"
    "v_cvt_pk_bf16_f32 v146, v176, v177\n"
    "v_cvt_pk_bf16_f32 v147, v178, v179\n"
    "v_mfma_f32_16x16x32_bf16 v[104:107], a[12:15], a[88:91], v[104:107]\n"
    "buffer_store_dwordx4 v[144:147], v244, s[80:83], s87 offen sc1\n"
    "v_mfma_f32_16x16x32_bf16 v[108:111], a[12:15], a[92:95], v[108:111]\n"
    "v_accvgpr_read_b32 v246, a172\n"
    "v_accvgpr_read_b32 v247, a173\n"
    "v_mfma_f32_16x16x32_bf16 v[0:3], a[40:43], a[116:119], v[0:3]\n"
    "v_accvgpr_read_b32 v248, a174\n"
    "v_accvgpr_read_b32 v249, a175\n"
    "v_mfma_f32_16x16x32_bf16 v[4:7], a[40:43], a[120:123], v[4:7]\n"
    "v_accvgpr_read_b32 v250, a204\n"
    "v_accvgpr_read_b32 v251, a205\n"
    "v_mfma_f32_16x16x32_bf16 v[8:11], a[40:43], a[124:127], v[8:11]\n"
    "v_accvgpr_read_b32 v252, a206\n"
    "v_accvgpr_read_b32 v253, a207\n"
    "v_mfma_f32_16x16x32_bf16 v[12:15], a[40:43], a[128:131], v[12:15]\n"
    "v_pk_mul_f32 v[148:149], v[148:149], v[246:247]\n"
    "v_pk_mul_f32 v[150:151], v[150:151], v[248:249]\n"
    "v_mfma_f32_16x16x32_bf16 v[32:35], a[44:47], a[116:119], v[32:35]\n"
    "v_pk_mul_f32 v[180:181], v[180:181], v[250:251]\n"
    "v_pk_mul_f32 v[182:183], v[182:183], v[252:253]\n"
    "v_mfma_f32_16x16x32_bf16 v[36:39], a[44:47], a[120:123], v[36:39]\n"
    "v_cvt_pk_bf16_f32 v148, v148, v149\n"
    "v_cvt_pk_bf16_f32 v149, v150, v151\n"
    "v_mfma_f32_16x16x32_bf16 v[40:43], a[44:47], a[124:127], v[40:43]\n"
    "v_cvt_pk_bf16_f32 v150, v180, v181\n"
    "v_cvt_pk_bf16_f32 v151, v182, v183\n"
    "v_mfma_f32_16x16x32_bf16 v[44:47], a[44:47], a[128:131], v[44:47]\n"
    "buffer_store_dwordx4 v[148:151], v244, s[80:83], s87 offen offset:1024 sc1\n"
    "v_mfma_f32_16x16x32_bf16 v[64:67], a[48:51], a[116:119], v[64:67]\n"
    "v_accvgpr_read_b32 v246, a176\n"
    "v_accvgpr_read_b32 v247, a177\n"
    "v_mfma_f32_16x16x32_bf16 v[68:71], a[48:51], a[120:123], v[68:71]\n"
    "v_accvgpr_read_b32 v248, a178\n"
    "v_accvgpr_read_b32 v249, a179\n"
    "v_mfma_f32_16x16x32_bf16 v[72:75], a[48:51], a[124:127], v[72:75]\n"
    "v_accvgpr_read_b32 v250, a208\n"
    "v_accvgpr_read_b32 v251, a209\n"
    "v_mfma_f32_16x16x32_bf16 v[76:79], a[48:51], a[128:131], v[76:79]\n"
    "v_accvgpr_read_b32 v252, a210\n"
    "v_accvgpr_read_b32 v253, a211\n"
    "v_mfma_f32_16x16x32_bf16 v[96:99], a[52:55], a[116:119], v[96:99]\n"
    "v_pk_mul_f32 v[152:153], v[152:153], v[246:247]\n"
    "v_pk_mul_f32 v[154:155], v[154:155], v[248:249]\n"
    "v_mfma_f32_16x16x32_bf16 v[100:103], a[52:55], a[120:123], v[100:103]\n"
    "v_pk_mul_f32 v[184:185], v[184:185], v[250:251]\n"
    "v_pk_mul_f32 v[186:187], v[186:187], v[252:253]\n"
    "v_mfma_f32_16x16x32_bf16 v[104:107], a[52:55], a[124:127], v[104:107]\n"
    "v_cvt_pk_bf16_f32 v152, v152, v153\n"
    "v_cvt_pk_bf16_f32 v153, v154, v155\n"
    "v_mfma_f32_16x16x32_bf16 v[108:111], a[52:55], a[128:131], v[108:111]\n"
    "v_cvt_pk_bf16_f32 v154, v184, v185\n"
    "v_cvt_pk_bf16_f32 v155, v186, v187\n"
    "v_mfma_f32_16x16x32_bf16 v[16:19], a[0:3], a[96:99], v[16:19]\n"
    "buffer_store_dwordx4 v[152:155], v244, s[80:83], s87 offen offset:2048 sc1\n"
    "v_mfma_f32_16x16x32_bf16 v[20:23], a[0:3], a[100:103], v[20:23]\n"
    "v_accvgpr_read_b32 v246, a180\n"
    "v_accvgpr_read_b32 v247, a181\n"
    "v_mfma_f32_16x16x32_bf16 v[24:27], a[0:3], a[104:107], v[24:27]\n"
    "v_accvgpr_read_b32 v248, a182\n"
    "v_accvgpr_read_b32 v249, a183\n"
    "v_mfma_f32_16x16x32_bf16 v[28:31], a[0:3], a[108:111], v[28:31]\n"
    "v_accvgpr_read_b32 v250, a212\n"
    "v_accvgpr_read_b32 v251, a213\n"
    "v_mfma_f32_16x16x32_bf16 v[48:51], a[4:7], a[96:99], v[48:51]\n"
    "v_accvgpr_read_b32 v252, a214\n"
    "v_accvgpr_read_b32 v253, a215\n"
    "v_mfma_f32_16x16x32_bf16 v[52:55], a[4:7], a[100:103], v[52:55]\n"
    "v_pk_mul_f32 v[156:157], v[156:157], v[246:247]\n"
    "v_pk_mul_f32 v[158:159], v[158:159], v[248:249]\n"
    "v_mfma_f32_16x16x32_bf16 v[56:59], a[4:7], a[104:107], v[56:59]\n"
    "v_pk_mul_f32 v[188:189], v[188:189], v[250:251]\n"
    "v_pk_mul_f32 v[190:191], v[190:191], v[252:253]\n"
    "v_mfma_f32_16x16x32_bf16 v[60:63], a[4:7], a[108:111], v[60:63]\n"
    "v_cvt_pk_bf16_f32 v156, v156, v157\n"
    "v_cvt_pk_bf16_f32 v157, v158, v159\n"
    "v_mfma_f32_16x16x32_bf16 v[80:83], a[8:11], a[96:99], v[80:83]\n"
    "v_cvt_pk_bf16_f32 v158, v188, v189\n"
    "v_cvt_pk_bf16_f32 v159, v190, v191\n"
    "v_mfma_f32_16x16x32_bf16 v[84:87], a[8:11], a[100:103], v[84:87]\n"
    "buffer_store_dwordx4 v[156:159], v244, s[80:83], s87 offen offset:3072 sc1\n"
    "v_mfma_f32_16x16x32_bf16 v[88:91], a[8:11], a[104:107], v[88:91]\n"
    "v_mfma_f32_16x16x32_bf16 v[92:95], a[8:11], a[108:111], v[92:95]\n"
    "v_pk_mul_f32 v[0:1], v[0:1], v[64:65]\n"
    "v_pk_mul_f32 v[2:3], v[2:3], v[66:67]\n"
    "v_pk_mul_f32 v[32:33], v[32:33], v[96:97]\n"
    "v_mfma_f32_16x16x32_bf16 v[112:115], a[12:15], a[96:99], v[112:115]\n"
    "v_pk_mul_f32 v[34:35], v[34:35], v[98:99]\n"
    "v_cvt_pk_bf16_f32 v0, v0, v1\n"
    "v_cvt_pk_bf16_f32 v1, v2, v3\n"
    "v_mfma_f32_16x16x32_bf16 v[116:119], a[12:15], a[100:103], v[116:119]\n"
    "v_cvt_pk_bf16_f32 v2, v32, v33\n"
    "v_cvt_pk_bf16_f32 v3, v34, v35\n"
    "buffer_store_dwordx4 v[0:3], v244, s[80:83], s84 offen sc1\n"
    "v_mfma_f32_16x16x32_bf16 v[120:123], a[12:15], a[104:107], v[120:123]\n"
    "v_pk_mul_f32 v[4:5], v[4:5], v[68:69]\n"
    "v_pk_mul_f32 v[6:7], v[6:7], v[70:71]\n"
    "v_pk_mul_f32 v[36:37], v[36:37], v[100:101]\n"
    "v_mfma_f32_16x16x32_bf16 v[124:127], a[12:15], a[108:111], v[124:127]\n"
    "v_pk_mul_f32 v[38:39], v[38:39], v[102:103]\n"
    "v_cvt_pk_bf16_f32 v4, v4, v5\n"
    "v_cvt_pk_bf16_f32 v5, v6, v7\n"
    "v_mfma_f32_16x16x32_bf16 v[16:19], a[40:43], a[132:135], v[16:19]\n"
    "v_cvt_pk_bf16_f32 v6, v36, v37\n"
    "v_cvt_pk_bf16_f32 v7, v38, v39\n"
    "buffer_store_dwordx4 v[4:7], v244, s[80:83], s84 offen offset:1024 sc1\n"
    "v_mfma_f32_16x16x32_bf16 v[20:23], a[40:43], a[136:139], v[20:23]\n"
    "v_pk_mul_f32 v[8:9], v[8:9], v[72:73]\n"
    "v_pk_mul_f32 v[10:11], v[10:11], v[74:75]\n"
    "v_pk_mul_f32 v[40:41], v[40:41], v[104:105]\n"
    "v_mfma_f32_16x16x32_bf16 v[24:27], a[40:43], a[140:143], v[24:27]\n"
    "v_pk_mul_f32 v[42:43], v[42:43], v[106:107]\n"
    "v_cvt_pk_bf16_f32 v8, v8, v9\n"
    "v_cvt_pk_bf16_f32 v9, v10, v11\n"
    "v_mfma_f32_16x16x32_bf16 v[28:31], a[40:43], a[144:147], v[28:31]\n"
    "v_cvt_pk_bf16_f32 v10, v40, v41\n"
    "v_cvt_pk_bf16_f32 v11, v42, v43\n"
    "buffer_store_dwordx4 v[8:11], v244, s[80:83], s84 offen offset:2048 sc1\n"
    "v_mfma_f32_16x16x32_bf16 v[48:51], a[44:47], a[132:135], v[48:51]\n"
    "v_pk_mul_f32 v[12:13], v[12:13], v[76:77]\n"
    "v_pk_mul_f32 v[14:15], v[14:15], v[78:79]\n"
    "v_pk_mul_f32 v[44:45], v[44:45], v[108:109]\n"
    "v_mfma_f32_16x16x32_bf16 v[52:55], a[44:47], a[136:139], v[52:55]\n"
    "v_pk_mul_f32 v[46:47], v[46:47], v[110:111]\n"
    "v_cvt_pk_bf16_f32 v12, v12, v13\n"
    "v_cvt_pk_bf16_f32 v13, v14, v15\n"
    "v_mfma_f32_16x16x32_bf16 v[56:59], a[44:47], a[140:143], v[56:59]\n"
    "v_cvt_pk_bf16_f32 v14, v44, v45\n"
    "v_cvt_pk_bf16_f32 v15, v46, v47\n"
    "buffer_store_dwordx4 v[12:15], v244, s[80:83], s84 offen offset:3072 sc1\n"
    "v_mfma_f32_16x16x32_bf16 v[60:63], a[44:47], a[144:147], v[60:63]\n"
    "v_mfma_f32_16x16x32_bf16 v[80:83], a[48:51], a[132:135], v[80:83]\n"
    "v_mfma_f32_16x16x32_bf16 v[84:87], a[48:51], a[136:139], v[84:87]\n"
    "v_mfma_f32_16x16x32_bf16 v[88:91], a[48:51], a[140:143], v[88:91]\n"
    "v_mfma_f32_16x16x32_bf16 v[92:95], a[48:51], a[144:147], v[92:95]\n"
    "v_mfma_f32_16x16x32_bf16 v[112:115], a[52:55], a[132:135], v[112:115]\n"
    "v_mfma_f32_16x16x32_bf16 v[116:119], a[52:55], a[136:139], v[116:119]\n"
    "v_mfma_f32_16x16x32_bf16 v[120:123], a[52:55], a[140:143], v[120:123]\n"
    "v_mfma_f32_16x16x32_bf16 v[124:127], a[52:55], a[144:147], v[124:127]\n"
    "v_mfma_f32_16x16x32_bf16 a[216:219], a[32:35], a[80:83], a[216:219]\n"
    "v_mfma_f32_16x16x32_bf16 a[220:223], a[32:35], a[84:87], a[220:223]\n"
    "v_mfma_f32_16x16x32_bf16 a[224:227], a[32:35], a[88:91], a[224:227]\n"
    "v_pk_mul_f32 v[16:17], v[16:17], v[80:81]\n"
    "v_pk_mul_f32 v[18:19], v[18:19], v[82:83]\n"
    "v_pk_mul_f32 v[48:49], v[48:49], v[112:113]\n"
    "v_mfma_f32_16x16x32_bf16 a[228:231], a[32:35], a[92:95], a[228:231]\n"
    "v_pk_mul_f32 v[50:51], v[50:51], v[114:115]\n"
    "v_cvt_pk_bf16_f32 v16, v16, v17\n"
    "v_cvt_pk_bf16_f32 v17, v18, v19\n"
    "v_mfma_f32_16x16x32_bf16 a[232:235], a[32:35], a[96:99], a[232:235]\n"
    "v_cvt_pk_bf16_f32 v18, v48, v49\n"
    "v_cvt_pk_bf16_f32 v19, v50, v51\n"
    "buffer_store_dwordx4 v[16:19], v244, s[80:83], s85 offen sc1\n"
    "v_mfma_f32_16x16x32_bf16 a[236:239], a[32:35], a[100:103], a[236:239]\n"
    "v_pk_mul_f32 v[20:21], v[20:21], v[84:85]\n"
    "v_pk_mul_f32 v[22:23], v[22:23], v[86:87]\n"
    "v_pk_mul_f32 v[52:53], v[52:53], v[116:117]\n"
    "v_mfma_f32_16x16x32_bf16 a[240:243], a[32:35], a[104:107], a[240:243]\n"
    "v_pk_mul_f32 v[54:55], v[54:55], v[118:119]\n"
    "v_cvt_pk_bf16_f32 v20, v20, v21\n"
    "v_cvt_pk_bf16_f32 v21, v22, v23\n"
    "v_mfma_f32_16x16x32_bf16 a[244:247], a[32:35], a[108:111], a[244:247]\n"
    "v_cvt_pk_bf16_f32 v22, v52, v53\n"
    "v_cvt_pk_bf16_f32 v23, v54, v55\n"
    "buffer_store_dwordx4 v[20:23], v244, s[80:83], s85 offen offset:1024 sc1\n"
    "v_mfma_f32_16x16x32_bf16 v[192:195], a[36:39], a[80:83], v[192:195]\n"
    "v_pk_mul_f32 v[24:25], v[24:25], v[88:89]\n"
    "v_pk_mul_f32 v[26:27], v[26:27], v[90:91]\n"
    "v_pk_mul_f32 v[56:57], v[56:57], v[120:121]\n"
    "v_mfma_f32_16x16x32_bf16 v[196:199], a[36:39], a[84:87], v[196:199]\n"
    "v_pk_mul_f32 v[58:59], v[58:59], v[122:123]\n"
    "v_cvt_pk_bf16_f32 v24, v24, v25\n"
    "v_cvt_pk_bf16_f32 v25, v26, v27\n"
    "v_mfma_f32_16x16x32_bf16 v[200:203], a[36:39], a[88:91], v[200:203]\n"
    "v_cvt_pk_bf16_f32 v26, v56, v57\n"
    "v_cvt_pk_bf16_f32 v27, v58, v59\n"
    "buffer_store_dwordx4 v[24:27], v244, s[80:83], s85 offen offset:2048 sc1\n"
    "v_mfma_f32_16x16x32_bf16 v[204:207], a[36:39], a[92:95], v[204:207]\n"
    "v_pk_mul_f32 v[28:29], v[28:29], v[92:93]\n"
    "v_pk_mul_f32 v[30:31], v[30:31], v[94:95]\n"
    "v_pk_mul_f32 v[60:61], v[60:61], v[124:125]\n"
    "v_mfma_f32_16x16x32_bf16 v[208:211], a[36:39], a[96:99], v[208:211]\n"
    "v_pk_mul_f32 v[62:63], v[62:63], v[126:127]\n"
    "v_cvt_pk_bf16_f32 v28, v28, v29\n"
    "v_cvt_pk_bf16_f32 v29, v30, v31\n"
    "v_mfma_f32_16x16x32_bf16 v[212:215], a[36:39], a[100:103], v[212:215]\n"
    "v_cvt_pk_bf16_f32 v30, v60, v61\n"
    "v_cvt_pk_bf16_f32 v31, v62, v63\n"
    "buffer_store_dwordx4 v[28:31], v244, s[80:83], s85 offen offset:3072 sc1\n"
    "v_mfma_f32_16x16x32_bf16 v[216:219], a[36:39], a[104:107], v[216:219]\n"
    "v_mfma_f32_16x16x32_bf16 v[220:223], a[36:39], a[108:111], v[220:223]\n"
    "v_mfma_f32_16x16x32_bf16 a[216:219], a[72:75], a[116:119], a[216:219]\n"
    "v_mfma_f32_16x16x32_bf16 a[220:223], a[72:75], a[120:123], a[220:223]\n"
    "v_mfma_f32_16x16x32_bf16 a[224:227], a[72:75], a[124:127], a[224:227]\n"
    "v_mfma_f32_16x16x32_bf16 a[228:231], a[72:75], a[128:131], a[228:231]\n"
    "v_mfma_f32_16x16x32_bf16 a[232:235], a[72:75], a[132:135], a[232:235]\n"
    "v_mfma_f32_16x16x32_bf16 a[236:239], a[72:75], a[136:139], a[236:239]\n"
    "v_mfma_f32_16x16x32_bf16 a[240:243], a[72:75], a[140:143], a[240:243]\n"
    "v_mfma_f32_16x16x32_bf16 a[244:247], a[72:75], a[144:147], a[244:247]\n"
    "v_mfma_f32_16x16x32_bf16 v[192:195], a[76:79], a[116:119], v[192:195]\n"
    "v_mfma_f32_16x16x32_bf16 v[196:199], a[76:79], a[120:123], v[196:199]\n"
    "v_mfma_f32_16x16x32_bf16 v[200:203], a[76:79], a[124:127], v[200:203]\n"
    "v_mfma_f32_16x16x32_bf16 v[204:207], a[76:79], a[128:131], v[204:207]\n"
    "v_mfma_f32_16x16x32_bf16 v[208:211], a[76:79], a[132:135], v[208:211]\n"
    "v_mfma_f32_16x16x32_bf16 v[212:215], a[76:79], a[136:139], v[212:215]\n"
    "v_mfma_f32_16x16x32_bf16 v[216:219], a[76:79], a[140:143], v[216:219]\n"
    "v_mfma_f32_16x16x32_bf16 v[220:223], a[76:79], a[144:147], v[220:223]\n"
    "s_nop 7\n"
    "v_add_u32 v245, s90, v244\n"
    "s_nop 0\n"
    "v_accvgpr_read_b32 v32, a216\n"
    "v_accvgpr_read_b32 v33, a217\n"
    "v_accvgpr_read_b32 v34, a218\n"
    "v_accvgpr_read_b32 v35, a219\n"
    "v_pk_mul_f32 v[32:33], v[32:33], v[192:193]\n"
    "v_pk_mul_f32 v[34:35], v[34:35], v[194:195]\n"
    "v_cvt_pk_bf16_f32 v32, v32, v33\n"
    "v_accvgpr_read_b32 v36, a220\n"
    "v_accvgpr_read_b32 v37, a221\n"
    "v_cvt_pk_bf16_f32 v33, v34, v35\n"
    "v_accvgpr_read_b32 v38, a222\n"
    "v_accvgpr_read_b32 v39, a223\n"
    "v_pk_mul_f32 v[36:37], v[36:37], v[196:197]\n"
    "buffer_store_dwordx2 v[32:33], v245, s[80:83], s88 offen sc1\n"
    "v_pk_mul_f32 v[38:39], v[38:39], v[198:199]\n"
    "v_cvt_pk_bf16_f32 v36, v36, v37\n"
    "v_accvgpr_read_b32 v40, a224\n"
    "v_accvgpr_read_b32 v41, a225\n"
    "v_cvt_pk_bf16_f32 v37, v38, v39\n"
    "v_accvgpr_read_b32 v42, a226\n"
    "v_accvgpr_read_b32 v43, a227\n"
    "v_pk_mul_f32 v[40:41], v[40:41], v[200:201]\n"
    "buffer_store_dwordx2 v[36:37], v245, s[80:83], s88 offen offset:1024 sc1\n"
    "v_pk_mul_f32 v[42:43], v[42:43], v[202:203]\n"
    "v_cvt_pk_bf16_f32 v40, v40, v41\n"
    "v_accvgpr_read_b32 v44, a228\n"
    "v_accvgpr_read_b32 v45, a229\n"
    "v_cvt_pk_bf16_f32 v41, v42, v43\n"
    "v_accvgpr_read_b32 v46, a230\n"
    "v_accvgpr_read_b32 v47, a231\n"
    "v_pk_mul_f32 v[44:45], v[44:45], v[204:205]\n"
    "buffer_store_dwordx2 v[40:41], v245, s[80:83], s88 offen offset:2048 sc1\n"
    "v_pk_mul_f32 v[46:47], v[46:47], v[206:207]\n"
    "v_cvt_pk_bf16_f32 v44, v44, v45\n"
    "v_accvgpr_read_b32 v48, a232\n"
    "v_accvgpr_read_b32 v49, a233\n"
    "v_cvt_pk_bf16_f32 v45, v46, v47\n"
    "v_accvgpr_read_b32 v50, a234\n"
    "v_accvgpr_read_b32 v51, a235\n"
    "v_pk_mul_f32 v[48:49], v[48:49], v[208:209]\n"
    "buffer_store_dwordx2 v[44:45], v245, s[80:83], s88 offen offset:3072 sc1\n"
    "v_pk_mul_f32 v[50:51], v[50:51], v[210:211]\n"
    "v_cvt_pk_bf16_f32 v48, v48, v49\n"
    "v_accvgpr_read_b32 v52, a236\n"
    "v_accvgpr_read_b32 v53, a237\n"
    "v_cvt_pk_bf16_f32 v49, v50, v51\n"
    "v_accvgpr_read_b32 v54, a238\n"
    "v_accvgpr_read_b32 v55, a239\n"
    "v_pk_mul_f32 v[52:53], v[52:53], v[212:213]\n"
    "buffer_store_dwordx2 v[48:49], v245, s[80:83], s89 offen sc1\n"
    "v_pk_mul_f32 v[54:55], v[54:55], v[214:215]\n"
    "v_cvt_pk_bf16_f32 v52, v52, v53\n"
    "v_accvgpr_read_b32 v56, a240\n"
    "v_accvgpr_read_b32 v57, a241\n"
    "v_cvt_pk_bf16_f32 v53, v54, v55\n"
    "v_accvgpr_read_b32 v58, a242\n"
    "v_accvgpr_read_b32 v59, a243\n"
    "v_pk_mul_f32 v[56:57], v[56:57], v[216:217]\n"
    "buffer_store_dwordx2 v[52:53], v245, s[80:83], s89 offen offset:1024 sc1\n"
    "v_pk_mul_f32 v[58:59], v[58:59], v[218:219]\n"
    "v_cvt_pk_bf16_f32 v56, v56, v57\n"
    "v_accvgpr_read_b32 v60, a244\n"
    "v_accvgpr_read_b32 v61, a245\n"
    "v_cvt_pk_bf16_f32 v57, v58, v59\n"
    "v_accvgpr_read_b32 v62, a246\n"
    "v_accvgpr_read_b32 v63, a247\n"
    "v_pk_mul_f32 v[60:61], v[60:61], v[220:221]\n"
    "buffer_store_dwordx2 v[56:57], v245, s[80:83], s89 offen offset:2048 sc1\n"
    "v_pk_mul_f32 v[62:63], v[62:63], v[222:223]\n"
    "v_cvt_pk_bf16_f32 v60, v60, v61\n"
    "v_cvt_pk_bf16_f32 v61, v62, v63\n"
    "buffer_store_dwordx2 v[60:61], v245, s[80:83], s89 offen offset:3072 sc1\n"
    "s_waitcnt vmcnt(0)\n"
    "s_barrier\n"
    : "+{v[224:235]}"(vin), "+{v[236:239]}"(vinA), "+{s[56:63]}"(soff), "+{s[64:67]}"(smisc), "+{s[68:75]}"(sext), "+{s[44:47]}"(srdA), "+{s[48:51]}"(srdB), "+{s[28:29]}"(sw2)
    : "{s[40:43]}"(srdX), "{s[52:55]}"(srdC), "{s[76:79]}"(srdS), "{s[80:83]}"(srdO), "{s[84:91]}"(sepi), "{s[20:23]}"(srdP), "{s[24:27]}"(srdV)
    : "memory", "scc", "m0", "s92", "s93", "v0", "v1", "v2", "v3", "v4", "v5", "v6", "v7", "v8", "v9", "v10", "v11", "v12", "v13", "v14", "v15", "v16", "v17", "v18", "v19", "v20", "v21", "v22", "v23", "v24", "v25", "v26", "v27", "v28", "v29", "v30", "v31", "v32", "v33", "v34", "v35", "v36", "v37", "v38", "v39", "v40", "v41", "v42", "v43", "v44", "v45", "v46", "v47", "v48", "v49", "v50", "v51", "v52", "v53", "v54", "v55", "v56", "v57", "v58", "v59", "v60", "v61", "v62", "v63", "v64", "v65", "v66", "v67", "v68", "v69", "v70", "v71", "v72", "v73", "v74", "v75", "v76", "v77", "v78", "v79", "v80", "v81", "v82", "v83", "v84", "v85", "v86", "v87", "v88", "v89", "v90", "v91", "v92", "v93", "v94", "v95", "v96", "v97", "v98", "v99", "v100", "v101", "v102", "v103", "v104", "v105", "v106", "v107", "v108", "v109", "v110", "v111", "v112", "v113", "v114", "v115", "v116", "v117", "v118", "v119", "v120", "v121", "v122", "v123", "v124", "v125", "v126", "v127", "v128", "v129", "v130", "v131", "v132", "v133", "v134", "v135", "v136", "v137", "v138", "v139", "v140", "v141", "v142", "v143", "v144", "v145", "v146", "v147", "v148", "v149", "v150", "v151", "v152", "v153", "v154", "v155", "v156", "v157", "v158", "v159", "v160", "v161", "v162", "v163", "v164", "v165", "v166", "v167", "v168", "v169", "v170", "v171", "v172", "v173", "v174", "v175", "v176", "v177", "v178", "v179", "v180", "v181", "v182", "v183", "v184", "v185", "v186", "v187", "v188", "v189", "v190", "v191", "v192", "v193", "v194", "v195", "v196", "v197", "v198", "v199", "v200", "v201", "v202", "v203", "v204", "v205", "v206", "v207", "v208", "v209", "v210", "v211", "v212", "v213", "v214", "v215", "v216", "v217", "v218", "v219", "v220", "v221", "v222", "v223", "v240", "v241", "v242", "v243", "v244", "v245", "v246", "v247", "v248", "v249", "v250", "v251", "v252", "v253", "v254", "v255", "a0", "a1", "a2", "a3", "a4", "a5", "a6", "a7", "a8", "a9", "a10", "a11", "a12", "a13", "a14", "a15", "a16", "a17", "a18", "a19", "a20", "a21", "a22", "a23", "a24", "a25", "a26", "a27", "a28", "a29", "a30", "a31", "a32", "a33", "a34", "a35", "a36", "a37", "a38", "a39", "a40", "a41", "a42", "a43", "a44", "a45", "a46", "a47", "a48", "a49", "a50", "a51", "a52", "a53", "a54", "a55", "a56", "a57", "a58", "a59", "a60", "a61", "a62", "a63", "a64", "a65", "a66", "a67", "a68", "a69", "a70", "a71", "a72", "a73", "a74", "a75", "a76", "a77", "a78", "a79", "a80", "a81", "a82", "a83", "a84", "a85", "a86", "a87", "a88", "a89", "a90", "a91", "a92", "a93", "a94", "a95", "a96", "a97", "a98", "a99", "a100", "a101", "a102", "a103", "a104", "a105", "a106", "a107", "a108", "a109", "a110", "a111", "a112", "a113", "a114", "a115", "a116", "a117", "a118", "a119", "a120", "a121", "a122", "a123", "a124", "a125", "a126", "a127", "a128", "a129", "a130", "a131", "a132", "a133", "a134", "a135", "a136", "a137", "a138", "a139", "a140", "a141", "a142", "a143", "a144", "a145", "a146", "a147", "a148", "a149", "a150", "a151", "a152", "a153", "a154", "a155", "a156", "a157", "a158", "a159", "a160", "a161", "a162", "a163", "a164", "a165", "a166", "a167", "a168", "a169", "a170", "a171", "a172", "a173", "a174", "a175", "a176", "a177", "a178", "a179", "a180", "a181", "a182", "a183", "a184", "a185", "a186", "a187", "a188", "a189", "a190", "a191", "a192", "a193", "a194", "a195", "a196", "a197", "a198", "a199", "a200", "a201", "a202", "a203", "a204", "a205", "a206", "a207", "a208", "a209", "a210", "a211", "a212", "a213", "a214", "a215", "a216", "a217", "a218", "a219", "a220", "a221", "a222", "a223", "a224", "a225", "a226", "a227", "a228", "a229", "a230", "a231", "a232", "a233", "a234", "a235", "a236", "a237", "a238", "a239", "a240", "a241", "a242", "a243", "a244", "a245", "a246", "a247", "a248", "a249", "a250", "a251", "a252", "a253", "a254", "a255");

  }
  TS_K1
  TS_END(0)
}

#define TS_ARG
#define TS_PAR
__device__ __forceinline__ u32 umin32(u32 a, u32 b) { return a < b ? a : b; }
__device__ __forceinline__ u32 umax32(u32 a, u32 b) { return a > b ? a : b; }
template <int J>
__device__ __forceinline__ void k2_epi(const f32x4 (&acc)[16], const u32x4 (&xr)[4], char* ws, u16* __restrict__ out, int grp, int w, int lane, u32 obase TS_PAR) {
  const __amdgpu_buffer_rsrc_t slab = __builtin_amdgcn_make_buffer_rsrc(ws + WS_SLAB, 0, (int)SLAB_BYTES, 0x00020000);
  const u32 voff = w * 4096 + lane * 16;
  {
    const u32 so = (u32)(grp * 2 + (1 - J)) * 16384u;
#pragma unroll
    for (int e = 0; e < 4; ++e) {
      const f32x4 p = acc[(2 * e) * 2 + (1 - J)], q = acc[(2 * e + 1) * 2 + (1 - J)];
      u32x4 val = {umin32(pk(p[0], p[1]), 0xfffffffeu), umin32(pk(p[2], p[3]), 0xfffffffeu), umin32(pk(q[0], q[1]), 0xfffffffeu), umin32(pk(q[2], q[3]), 0xfffffffeu)};
      __builtin_amdgcn_raw_buffer_store_b128(val, slab, voff + e * 1024, so, 16);
    }
  }
  TS(2)
  const u32 si = (u32)(grp * 2 + J) * 16384u;
  u32x4 rv[4];
#pragma unroll
  for (int e = 0; e < 4; ++e) rv[e] = __builtin_amdgcn_raw_buffer_load_b128(slab, voff + e * 1024, si, 16);
  float sum[4][8];
#pragma unroll
  for (int e = 0; e < 4; ++e) {
    const f32x4 p = acc[(2 * e) * 2 + J], q = acc[(2 * e + 1) * 2 + J];
#pragma unroll
    for (int i = 0; i < 4; ++i) {
      auto sv = __builtin_amdgcn_permlane16_swap(__float_as_uint(p[i]), __float_as_uint(q[i]), false, false);
      sum[e][i] = __uint_as_float(sv[0]) + ((i & 1) ? bfhi(xr[e][i >> 1]) : bflo(xr[e][i >> 1]));
      sum[e][4 + i] = __uint_as_float(sv[1]) + ((i & 1) ? bfhi(xr[e][2 + (i >> 1)]) : bflo(xr[e][2 + (i >> 1)]));
    }
  }
  TS(3)
  for (u32 spins = 0; spins < SPIN_MAX; ++spins) {
    u32 mx = 0;
#pragma unroll
    for (int e = 0; e < 4; ++e) mx = umax32(umax32(mx, umax32(rv[e][0], rv[e][1])), umax32(rv[e][2], rv[e][3]));
    if (!__any(mx == 0xffffffffu)) break;
    __builtin_amdgcn_s_sleep(1);
#pragma unroll
    for (int e = 0; e < 4; ++e) rv[e] = __builtin_amdgcn_raw_buffer_load_b128(slab, voff + e * 1024, si, 16);
  }
  TS(4)
  const __amdgpu_buffer_rsrc_t orsrc = __builtin_amdgcn_make_buffer_rsrc(out, 0, S * D * 2, 0x00020000);
#pragma unroll
  for (int e = 0; e < 4; ++e) {
    auto s0 = __builtin_amdgcn_permlane16_swap(rv[e][0], rv[e][2], false, false);
    auto s1 = __builtin_amdgcn_permlane16_swap(rv[e][1], rv[e][3], false, false);
    u32x4 r = {pk(sum[e][0] + bflo(s0[0]), sum[e][1] + bfhi(s0[0])), pk(sum[e][2] + bflo(s1[0]), sum[e][3] + bfhi(s1[0])),
               pk(sum[e][4] + bflo(s0[1]), sum[e][5] + bfhi(s0[1])), pk(sum[e][6] + bflo(s1[1]), sum[e][7] + bfhi(s1[1]))};
    __builtin_amdgcn_raw_buffer_store_b128(r, orsrc, obase + e * 64, 0, OUT_AUX);
  }
}

__device__ __forceinline__ void k2_epi_t(const f32x4 (&acc)[16], const u32x4 (&xr)[4], char* ws, u16* __restrict__ out, u32 si, int w, int lane, u32 obase TS_PAR) {
  const __amdgpu_buffer_rsrc_t slab = __builtin_amdgcn_make_buffer_rsrc(ws + WS_SLAB, 0, (int)SLAB_BYTES, 0x00020000);
  const u32 voff = w * 4096 + lane * 16;
  TS(2)
  u32x4 rv[4];
#pragma unroll
  for (int e = 0; e < 4; ++e) rv[e] = __builtin_amdgcn_raw_buffer_load_b128(slab, voff + e * 1024, si, 16);
  float sum[4][8];
#pragma unroll
  for (int e = 0; e < 4; ++e) {
    const f32x4 p = acc[(2 * e) * 2 + 1], q = acc[(2 * e + 1) * 2 + 1];
#pragma unroll
    for (int i = 0; i < 4; ++i) {
      auto sv = __builtin_amdgcn_permlane16_swap(__float_as_uint(p[i]), __float_as_uint(q[i]), false, false);
      sum[e][i] = __uint_as_float(sv[0]) + ((i & 1) ? bfhi(xr[e][i >> 1]) : bflo(xr[e][i >> 1]));
      sum[e][4 + i] = __uint_as_float(sv[1]) + ((i & 1) ? bfhi(xr[e][2 + (i >> 1)]) : bflo(xr[e][2 + (i >> 1)]));
    }
  }
  TS(3)
  for (u32 spins = 0; spins < SPIN_MAX; ++spins) {
    u32 mx = 0;
#pragma unroll
    for (int e = 0; e < 4; ++e) mx = umax32(umax32(mx, umax32(rv[e][0], rv[e][1])), umax32(rv[e][2], rv[e][3]));
    if (!__any(mx == 0xffffffffu)) break;
    __builtin_amdgcn_s_sleep(1);
#pragma unroll
    for (int e = 0; e < 4; ++e) rv[e] = __builtin_amdgcn_raw_buffer_load_b128(slab, voff + e * 1024, si, 16);
  }
  TS(4)
  const __amdgpu_buffer_rsrc_t orsrc = __builtin_amdgcn_make_buffer_rsrc(out, 0, S * D * 2, 0x00020000);
#pragma unroll
  for (int e = 0; e < 4; ++e) {
    auto s0 = __builtin_amdgcn_permlane16_swap(rv[e][0], rv[e][2], false, false);
    auto s1 = __builtin_amdgcn_permlane16_swap(rv[e][1], rv[e][3], false, false);
    u32x4 r = {pk(sum[e][0] + bflo(s0[0]), sum[e][1] + bfhi(s0[0])), pk(sum[e][2] + bflo(s1[0]), sum[e][3] + bfhi(s1[0])),
               pk(sum[e][4] + bflo(s0[1]), sum[e][5] + bfhi(s0[1])), pk(sum[e][6] + bflo(s1[1]), sum[e][7] + bfhi(s1[1]))};
    __builtin_amdgcn_raw_buffer_store_b128(r, orsrc, obase + e * 64, 0, OUT_AUX);
  }
}

__device__ __forceinline__ void k2_body(char* ws_, char* smem, int w, unsigned long long tag) {
  TS_DECL
  int b = blockIdx.x;
  unsigned long long wsq = (unsigned long long)ws_;
  asm volatile("" : "+s"(b), "+s"(w), "+s"(wsq), "+s"(tag));
  char* ws = (char*)wsq;
  const u32x2 tg = {(u32)tag, (u32)(tag >> 32)};
  const int xcd = b & 7, idx = b >> 3;
  const int j = xcd & 1, rb = (xcd >> 1) * 4 + (idx & 3), cb = idx >> 2;
  const int lane = fresh_lane();
  const __amdgpu_buffer_rsrc_t flg = __builtin_amdgcn_make_buffer_rsrc(ws + WS_FLAG, 0, FL_BYTES, 0x00020000);
  if (w == 0 && lane == 0) __builtin_amdgcn_raw_buffer_store_b64(tg, flg, FL_TILE + b * 128, 0, 16);
  u32x4 ka0; u32x2 ka1, ka2;
  asm volatile("s_load_dwordx2 %0, %3, 0x0\n\ts_load_dwordx4 %1, %3, 0x20\n\ts_load_dwordx2 %2, %3, 0x38\n\ts_waitcnt lgkmcnt(0)"
               : "=&s"(ka1), "=&s"(ka0), "=&s"(ka2) : "s"(__builtin_amdgcn_kernarg_segment_ptr()) : "memory");
  const u16* x = (const u16*)(((unsigned long long)ka1[1] << 32) | ka1[0]);
  const u16* wo = (const u16*)(((unsigned long long)ka0[1] << 32) | ka0[0]);
  const u16* wd = (const u16*)(((unsigned long long)ka0[3] << 32) | ka0[2]);
  u16* out = (u16*)(((unsigned long long)ka2[1] << 32) | ka2[0]);
  const int T0 = 8 * rb;
  constexpr u32 SB = 16384;
  const u32 lds0 = (u32)(unsigned long long)(LAS char*)smem;
  const int g = lane >> 4, q = (lane >> 2) & 3, pp = lane & 3;
  const int rrow = 4 * g + q, f2r = (rrow >> 1) & 3;
  u32x16 vin;
#pragma unroll
  for (int t = 0; t < 4; ++t)
#pragma unroll
    for (int gi = 0; gi < 3; ++gi) vin[3 * t + gi] = lds0 + rrow * 128 + pp * 8 + ((t ^ f2r) * 32) + gi * 3 * SB;
  vin[12] = (T0 + 2 * w) * 1024 + lane * 16;
  const int lrow = 8 * w + (lane >> 3), f2l = (lrow >> 1) & 3, sch = (lane & 7) ^ (f2l << 1);
  vin[13] = lrow * 2048 + sch * 16;
  vin[15] = vin[12] + j * 1024; vin[12] += (1 - j) * 1024;
  u32x4 srdX = mk_srd(ws + WS_A2F, (u32)A2F_BYTES), srdW = mk_srd(j == 0 ? wo : wd, j == 0 ? 1024u * 1024u * 2u : 3584u * 1024u * 2u), srdW2 = mk_srd(wd, 3584u * 1024u * 2u);
  u32x8 soff;
  const u32 r0 = j == 0 ? 0u : 1280u, r1 = j == 0 ? 0u : 2304u;
  soff[0] = (72 * j) * 131072; soff[1] = r0 * 2048 + cb * 256 + 9 * 131072; soff[2] = 0; soff[3] = soff[1] + 65536;
  soff[4] = 0; soff[5] = r1 * 2048 + cb * 256; soff[6] = lds0 + w * 1024; soff[7] = 0;
  const u32 obase = (u32)((16 * (T0 + 2 * w + j) + (lane & 15)) * D + cb * 128 + (g & 1) * 16 + (g >> 1) * 8) * 2u;
  vin[14] = obase;
  u32x4 srdR2 = mk_srd(x, S * D * 2);
  u32x4 xr[4];
  f32x4 acc[16];
  const u32 vsl = w * 4096 + lane * 16, ssl = (u32)((rb * 8 + cb) * 2 + (1 - j)) * 16384u;
  const u32x4 srdSl = mk_srd(ws + WS_SLAB, (u32)SLAB_BYTES);
  asm volatile("" : "+{v[96:111]}"(vin), "+{s[56:63]}"(soff), "+{s[44:47]}"(srdW), "+{s[40:43]}"(srdX), "+{s[48:51]}"(srdW2), "+{s[52:55]}"(srdR2));
  if (w == 0) {
    const int l5 = lane & 31, cj1 = l5 >> 3, x1 = l5 & 7;
    const bool first = cj1 < 2 || x1 == 0, second = cj1 >= 2 && !(x1 == 0 && cj1 == 2);
    const bool need = lane == 32 || (lane < 32 && (j == 0 ? first : second));
    const u32 fo = FL_TILE + (lane == 32 ? (b ^ 1) : (x1 + 8 * (rb >> 1) + 64 * cj1)) * 128;
    for (u32 spins = 0; spins < SPIN_MAX; ++spins) {
      const u32x2 f = __builtin_amdgcn_raw_buffer_load_b64(flg, fo, 0, 16);
      if (!__any(need && (f[0] != tg[0] || f[1] != tg[1]))) break;
      __builtin_amdgcn_s_sleep(1);
    }
  } else if (w == 1 && b == 0) {
    for (u32 spins = 0; spins < SPIN_MAX; ++spins) {
      bool miss = false;
#pragma unroll
      for (int i = 0; i < 4; ++i) { const u32x2 f = __builtin_amdgcn_raw_buffer_load_b64(flg, FL_LN + (lane + 64 * i) * 128, 0, 16); miss = miss || f[0] != tg[0] || f[1] != tg[1]; }
      if (!__any(miss)) break;
      __builtin_amdgcn_s_sleep(1);
    }
    if (lane == 0) __builtin_amdgcn_raw_buffer_store_b64(tg, flg, FL_EPOCH, 0, 16);
  }
  asm volatile("s_barrier" ::: "memory");
  TS_BEGIN(1)
  asm volatile(
    "s_waitcnt lgkmcnt(0)\n"
    "buffer_load_dwordx4 a[0:3], v108, s[40:43], s56 offen sc1\n"
    "buffer_load_dwordx4 a[4:7], v111, s[40:43], s56 offen sc1\n"
    "s_add_u32 s56, s56, 131072\n"
    "buffer_load_dwordx4 a[8:11], v108, s[40:43], s56 offen sc1\n"
    "buffer_load_dwordx4 a[12:15], v111, s[40:43], s56 offen sc1\n"
    "s_add_u32 s56, s56, 131072\n"
    "buffer_load_dwordx4 a[16:19], v108, s[40:43], s56 offen sc1\n"
    "buffer_load_dwordx4 a[20:23], v111, s[40:43], s56 offen sc1\n"
    "s_add_u32 s56, s56, 131072\n"
    "buffer_load_dwordx4 a[24:27], v108, s[40:43], s56 offen sc1\n"
    "buffer_load_dwordx4 a[28:31], v111, s[40:43], s56 offen sc1\n"
    "s_add_u32 s56, s56, 131072\n"
    "buffer_load_dwordx4 a[32:35], v108, s[40:43], s56 offen sc1\n"
    "buffer_load_dwordx4 a[36:39], v111, s[40:43], s56 offen sc1\n"
    "s_add_u32 s56, s56, 131072\n"
    "buffer_load_dwordx4 a[40:43], v108, s[40:43], s56 offen sc1\n"
    "buffer_load_dwordx4 a[44:47], v111, s[40:43], s56 offen sc1\n"
    "s_add_u32 s56, s56, 131072\n"
    "ds_read_b64_tr_b16 v[0:1], v96 offset:0\n"
    "ds_read_b64_tr_b16 v[2:3], v96 offset:2048\n"
    "ds_read_b64_tr_b16 v[4:5], v99 offset:0\n"
    "ds_read_b64_tr_b16 v[6:7], v99 offset:2048\n"
    "ds_read_b64_tr_b16 v[8:9], v102 offset:0\n"
    "ds_read_b64_tr_b16 v[10:11], v102 offset:2048\n"
    "ds_read_b64_tr_b16 v[12:13], v105 offset:0\n"
    "ds_read_b64_tr_b16 v[14:15], v105 offset:2048\n"
    "ds_read_b64_tr_b16 v[16:17], v96 offset:4096\n"
    "ds_read_b64_tr_b16 v[18:19], v96 offset:6144\n"
    "ds_read_b64_tr_b16 v[20:21], v99 offset:4096\n"
    "ds_read_b64_tr_b16 v[22:23], v99 offset:6144\n"
    "ds_read_b64_tr_b16 v[24:25], v102 offset:4096\n"
    "ds_read_b64_tr_b16 v[26:27], v102 offset:6144\n"
    "ds_read_b64_tr_b16 v[28:29], v105 offset:4096\n"
    "ds_read_b64_tr_b16 v[30:31], v105 offset:6144\n"
    "s_waitcnt vmcnt(8)\n"
    "s_waitcnt lgkmcnt(0)\n"
    "v_mfma_f32_16x16x32_bf16 v[128:131], v[0:3], a[0:3], 0\n"
    "ds_read_b64_tr_b16 v[32:33], v96 offset:8192\n"
    "v_mfma_f32_16x16x32_bf16 v[132:135], v[0:3], a[4:7], 0\n"
    "ds_read_b64_tr_b16 v[34:35], v96 offset:10240\n"
    "buffer_load_dwordx4 a[48:51], v108, s[40:43], s56 offen sc1\n"
    "v_mfma_f32_16x16x32_bf16 v[136:139], v[4:7], a[0:3], 0\n"
    "ds_read_b64_tr_b16 v[36:37], v99 offset:8192\n"
    "v_mfma_f32_16x16x32_bf16 v[140:143], v[4:7], a[4:7], 0\n"
    "ds_read_b64_tr_b16 v[38:39], v99 offset:10240\n"
    "buffer_load_dwordx4 a[52:55], v111, s[40:43], s56 offen sc1\n"
    "s_add_u32 s56, s56, 131072\n"
    "v_mfma_f32_16x16x32_bf16 v[144:147], v[8:11], a[0:3], 0\n"
    "ds_read_b64_tr_b16 v[40:41], v102 offset:8192\n"
    "v_mfma_f32_16x16x32_bf16 v[148:151], v[8:11], a[4:7], 0\n"
    "ds_read_b64_tr_b16 v[42:43], v102 offset:10240\n"
    "buffer_load_dwordx4 a[56:59], v108, s[40:43], s56 offen sc1\n"
    "v_mfma_f32_16x16x32_bf16 v[152:155], v[12:15], a[0:3], 0\n"
    "ds_read_b64_tr_b16 v[44:45], v105 offset:8192\n"
    "v_mfma_f32_16x16x32_bf16 v[156:159], v[12:15], a[4:7], 0\n"
    "ds_read_b64_tr_b16 v[46:47], v105 offset:10240\n"
    "buffer_load_dwordx4 a[60:63], v111, s[40:43], s56 offen sc1\n"
    "s_add_u32 s56, s56, 131072\n"
    "v_mfma_f32_16x16x32_bf16 v[160:163], v[16:19], a[0:3], 0\n"
    "ds_read_b64_tr_b16 v[48:49], v96 offset:12288\n"
    "v_mfma_f32_16x16x32_bf16 v[164:167], v[16:19], a[4:7], 0\n"
    "ds_read_b64_tr_b16 v[50:51], v96 offset:14336\n"
    "buffer_load_dwordx4 a[64:67], v108, s[40:43], s56 offen sc1\n"
    "v_mfma_f32_16x16x32_bf16 v[168:171], v[20:23], a[0:3], 0\n"
    "ds_read_b64_tr_b16 v[52:53], v99 offset:12288\n"
    "v_mfma_f32_16x16x32_bf16 v[172:175], v[20:23], a[4:7], 0\n"
    "ds_read_b64_tr_b16 v[54:55], v99 offset:14336\n"
    "buffer_load_dwordx4 a[68:71], v111, s[40:43], s56 offen sc1\n"
    "s_add_u32 s56, s56, 131072\n"
    "v_mfma_f32_16x16x32_bf16 v[176:179], v[24:27], a[0:3], 0\n"
    "ds_read_b64_tr_b16 v[56:57], v102 offset:12288\n"
    "v_mfma_f32_16x16x32_bf16 v[180:183], v[24:27], a[4:7], 0\n"
    "ds_read_b64_tr_b16 v[58:59], v102 offset:14336\n"
    "buffer_load_dwordx4 a[72:75], v108, s[40:43], s56 offen sc1\n"
    "v_mfma_f32_16x16x32_bf16 v[184:187], v[28:31], a[0:3], 0\n"
    "ds_read_b64_tr_b16 v[60:61], v105 offset:12288\n"
    "v_mfma_f32_16x16x32_bf16 v[188:191], v[28:31], a[4:7], 0\n"
    "ds_read_b64_tr_b16 v[62:63], v105 offset:14336\n"
    "buffer_load_dwordx4 a[76:79], v111, s[40:43], s56 offen sc1\n"
    "s_add_u32 s56, s56, 131072\n"
    "s_waitcnt vmcnt(12) lgkmcnt(12)\n"
    "s_barrier\n"
    "v_mfma_f32_16x16x32_bf16 v[128:131], v[32:35], a[8:11], v[128:131]\n"
    "ds_read_b64_tr_b16 v[0:1], v96 offset:16384\n"
    "v_mfma_f32_16x16x32_bf16 v[132:135], v[32:35], a[12:15], v[132:135]\n"
    "ds_read_b64_tr_b16 v[2:3], v96 offset:18432\n"
    "buffer_load_dwordx4 a[80:83], v108, s[40:43], s56 offen sc1\n"
    "v_mfma_f32_16x16x32_bf16 v[136:139], v[36:39], a[8:11], v[136:139]\n"
    "ds_read_b64_tr_b16 v[4:5], v99 offset:16384\n"
    "v_mfma_f32_16x16x32_bf16 v[140:143], v[36:39], a[12:15], v[140:143]\n"
    "ds_read_b64_tr_b16 v[6:7], v99 offset:18432\n"
    "buffer_load_dwordx4 a[84:87], v111, s[40:43], s56 offen sc1\n"
    "s_add_u32 s56, s56, 131072\n"
    "s_waitcnt lgkmcnt(12)\n"
    "v_mfma_f32_16x16x32_bf16 v[144:147], v[40:43], a[8:11], v[144:147]\n"
    "ds_read_b64_tr_b16 v[8:9], v102 offset:16384\n"
    "v_mfma_f32_16x16x32_bf16 v[148:151], v[40:43], a[12:15], v[148:151]\n"
    "ds_read_b64_tr_b16 v[10:11], v102 offset:18432\n"
    "buffer_load_dwordx4 a[88:91], v108, s[40:43], s56 offen sc1\n"
    "v_mfma_f32_16x16x32_bf16 v[152:155], v[44:47], a[8:11], v[152:155]\n"
    "ds_read_b64_tr_b16 v[12:13], v105 offset:16384\n"
    "v_mfma_f32_16x16x32_bf16 v[156:159], v[44:47], a[12:15], v[156:159]\n"
    "ds_read_b64_tr_b16 v[14:15], v105 offset:18432\n"
    "buffer_load_dwordx4 a[92:95], v111, s[40:43], s56 offen sc1\n"
    "s_add_u32 s56, s56, 131072\n"
    "s_waitcnt lgkmcnt(12)\n"
    "v_mfma_f32_16x16x32_bf16 v[160:163], v[48:51], a[8:11], v[160:163]\n"
    "ds_read_b64_tr_b16 v[16:17], v96 offset:20480\n"
    "v_mfma_f32_16x16x32_bf16 v[164:167], v[48:51], a[12:15], v[164:167]\n"
    "ds_read_b64_tr_b16 v[18:19], v96 offset:22528\n"
    "buffer_load_dwordx4 a[96:99], v108, s[40:43], s56 offen sc1\n"
    "v_mfma_f32_16x16x32_bf16 v[168:171], v[52:55], a[8:11], v[168:171]\n"
    "ds_read_b64_tr_b16 v[20:21], v99 offset:20480\n"
    "v_mfma_f32_16x16x32_bf16 v[172:175], v[52:55], a[12:15], v[172:175]\n"
    "ds_read_b64_tr_b16 v[22:23], v99 offset:22528\n"
    "buffer_load_dwordx4 a[100:103], v111, s[40:43], s56 offen sc1\n"
    "s_add_u32 s56, s56, 131072\n"
    "s_waitcnt lgkmcnt(12)\n"
    "v_mfma_f32_16x16x32_bf16 v[176:179], v[56:59], a[8:11], v[176:179]\n"
    "ds_read_b64_tr_b16 v[24:25], v102 offset:20480\n"
    "v_mfma_f32_16x16x32_bf16 v[180:183], v[56:59], a[12:15], v[180:183]\n"
    "ds_read_b64_tr_b16 v[26:27], v102 offset:22528\n"
    "buffer_load_dwordx4 a[104:107], v108, s[40:43], s56 offen sc1\n"
    "v_mfma_f32_16x16x32_bf16 v[184:187], v[60:63], a[8:11], v[184:187]\n"
    "ds_read_b64_tr_b16 v[28:29], v105 offset:20480\n"
    "v_mfma_f32_16x16x32_bf16 v[188:191], v[60:63], a[12:15], v[188:191]\n"
    "ds_read_b64_tr_b16 v[30:31], v105 offset:22528\n"
    "buffer_load_dwordx4 a[108:111], v111, s[40:43], s56 offen sc1\n"
    "s_add_u32 s56, s56, 131072\n"
    "s_waitcnt lgkmcnt(12)\n"
    "v_mfma_f32_16x16x32_bf16 v[128:131], v[0:3], a[16:19], v[128:131]\n"
    "ds_read_b64_tr_b16 v[32:33], v96 offset:24576\n"
    "v_mfma_f32_16x16x32_bf16 v[132:135], v[0:3], a[20:23], v[132:135]\n"
    "ds_read_b64_tr_b16 v[34:35], v96 offset:26624\n"
    "buffer_load_dwordx4 a[112:115], v108, s[40:43], s56 offen sc1\n"
    "v_mfma_f32_16x16x32_bf16 v[136:139], v[4:7], a[16:19], v[136:139]\n"
    "ds_read_b64_tr_b16 v[36:37], v99 offset:24576\n"
    "v_mfma_f32_16x16x32_bf16 v[140:143], v[4:7], a[20:23], v[140:143]\n"
    "ds_read_b64_tr_b16 v[38:39], v99 offset:26624\n"
    "buffer_load_dwordx4 a[116:119], v111, s[40:43], s56 offen sc1\n"
    "s_add_u32 s56, s56, 131072\n"
    "s_waitcnt lgkmcnt(12)\n"
    "v_mfma_f32_16x16x32_bf16 v[144:147], v[8:11], a[16:19], v[144:147]\n"
    "ds_read_b64_tr_b16 v[40:41], v102 offset:24576\n"
    "v_mfma_f32_16x16x32_bf16 v[148:151], v[8:11], a[20:23], v[148:151]\n"
    "ds_read_b64_tr_b16 v[42:43], v102 offset:26624\n"
    "buffer_load_dwordx4 a[120:123], v108, s[40:43], s56 offen sc1\n"
    "v_mfma_f32_16x16x32_bf16 v[152:155], v[12:15], a[16:19], v[152:155]\n"
    "ds_read_b64_tr_b16 v[44:45], v105 offset:24576\n"
    "v_mfma_f32_16x16x32_bf16 v[156:159], v[12:15], a[20:23], v[156:159]\n"
    "ds_read_b64_tr_b16 v[46:47], v105 offset:26624\n"
    "buffer_load_dwordx4 a[124:127], v111, s[40:43], s56 offen sc1\n"
    "s_add_u32 s56, s56, 131072\n"
    "s_waitcnt lgkmcnt(12)\n"
    "v_mfma_f32_16x16x32_bf16 v[160:163], v[16:19], a[16:19], v[160:163]\n"
    "ds_read_b64_tr_b16 v[48:49], v96 offset:28672\n"
    "v_mfma_f32_16x16x32_bf16 v[164:167], v[16:19], a[20:23], v[164:167]\n"
    "ds_read_b64_tr_b16 v[50:51], v96 offset:30720\n"
    "buffer_load_dwordx4 a[128:131], v108, s[40:43], s56 offen sc1\n"
    "v_mfma_f32_16x16x32_bf16 v[168:171], v[20:23], a[16:19], v[168:171]\n"
    "ds_read_b64_tr_b16 v[52:53], v99 offset:28672\n"
    "v_mfma_f32_16x16x32_bf16 v[172:175], v[20:23], a[20:23], v[172:175]\n"
    "ds_read_b64_tr_b16 v[54:55], v99 offset:30720\n"
    "buffer_load_dwordx4 a[132:135], v111, s[40:43], s56 offen sc1\n"
    "s_add_u32 s56, s56, 131072\n"
    "s_waitcnt lgkmcnt(12)\n"
    "v_mfma_f32_16x16x32_bf16 v[176:179], v[24:27], a[16:19], v[176:179]\n"
    "ds_read_b64_tr_b16 v[56:57], v102 offset:28672\n"
    "v_mfma_f32_16x16x32_bf16 v[180:183], v[24:27], a[20:23], v[180:183]\n"
    "ds_read_b64_tr_b16 v[58:59], v102 offset:30720\n"
    "buffer_load_dwordx4 a[136:139], v108, s[40:43], s56 offen sc1\n"
    "v_mfma_f32_16x16x32_bf16 v[184:187], v[28:31], a[16:19], v[184:187]\n"
    "ds_read_b64_tr_b16 v[60:61], v105 offset:28672\n"
    "v_mfma_f32_16x16x32_bf16 v[188:191], v[28:31], a[20:23], v[188:191]\n"
    "ds_read_b64_tr_b16 v[62:63], v105 offset:30720\n"
    "buffer_load_dwordx4 a[140:143], v111, s[40:43], s56 offen sc1\n"
    "s_add_u32 s56, s56, 131072\n"
    "s_waitcnt vmcnt(24) lgkmcnt(12)\n"
    "s_barrier\n"
    "v_mfma_f32_16x16x32_bf16 v[128:131], v[32:35], a[24:27], v[128:131]\n"
    "ds_read_b64_tr_b16 v[0:1], v96 offset:32768\n"
    "s_add_u32 m0, s62, 0\n"
    "v_mfma_f32_16x16x32_bf16 v[132:135], v[32:35], a[28:31], v[132:135]\n"
    "ds_read_b64_tr_b16 v[2:3], v96 offset:34816\n"
    "buffer_load_dwordx4 v109, s[44:47], s57 offen lds\n"
    "v_mfma_f32_16x16x32_bf16 v[136:139], v[36:39], a[24:27], v[136:139]\n"
    "ds_read_b64_tr_b16 v[4:5], v99 offset:32768\n"
    "v_mfma_f32_16x16x32_bf16 v[140:143], v[36:39], a[28:31], v[140:143]\n"
    "ds_read_b64_tr_b16 v[6:7], v99 offset:34816\n"
    "s_waitcnt lgkmcnt(12)\n"
    "v_mfma_f32_16x16x32_bf16 v[144:147], v[40:43], a[24:27], v[144:147]\n"
    "ds_read_b64_tr_b16 v[8:9], v102 offset:32768\n"
    "s_add_u32 m0, s62, 3968\n"
    "v_mfma_f32_16x16x32_bf16 v[148:151], v[40:43], a[28:31], v[148:151]\n"
    "ds_read_b64_tr_b16 v[10:11], v102 offset:34816\n"
    "buffer_load_dwordx4 v109, s[44:47], s57 offen offset:128 lds\n"
    "v_mfma_f32_16x16x32_bf16 v[152:155], v[44:47], a[24:27], v[152:155]\n"
    "ds_read_b64_tr_b16 v[12:13], v105 offset:32768\n"
    "s_add_u32 s57, s57, 131072\n"
    "v_mfma_f32_16x16x32_bf16 v[156:159], v[44:47], a[28:31], v[156:159]\n"
    "ds_read_b64_tr_b16 v[14:15], v105 offset:34816\n"
    "s_waitcnt lgkmcnt(12)\n"
    "v_mfma_f32_16x16x32_bf16 v[160:163], v[48:51], a[24:27], v[160:163]\n"
    "ds_read_b64_tr_b16 v[16:17], v96 offset:36864\n"
    "s_add_u32 m0, s62, 8192\n"
    "v_mfma_f32_16x16x32_bf16 v[164:167], v[48:51], a[28:31], v[164:167]\n"
    "ds_read_b64_tr_b16 v[18:19], v96 offset:38912\n"
    "buffer_load_dwordx4 v109, s[44:47], s59 offen lds\n"
    "v_mfma_f32_16x16x32_bf16 v[168:171], v[52:55], a[24:27], v[168:171]\n"
    "ds_read_b64_tr_b16 v[20:21], v99 offset:36864\n"
    "v_mfma_f32_16x16x32_bf16 v[172:175], v[52:55], a[28:31], v[172:175]\n"
    "ds_read_b64_tr_b16 v[22:23], v99 offset:38912\n"
    "s_waitcnt lgkmcnt(12)\n"
    "v_mfma_f32_16x16x32_bf16 v[176:179], v[56:59], a[24:27], v[176:179]\n"
    "ds_read_b64_tr_b16 v[24:25], v102 offset:36864\n"
    "s_add_u32 m0, s62, 12160\n"
    "v_mfma_f32_16x16x32_bf16 v[180:183], v[56:59], a[28:31], v[180:183]\n"
    "ds_read_b64_tr_b16 v[26:27], v102 offset:38912\n"
    "buffer_load_dwordx4 v109, s[44:47], s59 offen offset:128 lds\n"
    "v_mfma_f32_16x16x32_bf16 v[184:187], v[60:63], a[24:27], v[184:187]\n"
    "ds_read_b64_tr_b16 v[28:29], v105 offset:36864\n"
    "s_add_u32 s59, s59, 131072\n"
    "v_mfma_f32_16x16x32_bf16 v[188:191], v[60:63], a[28:31], v[188:191]\n"
    "ds_read_b64_tr_b16 v[30:31], v105 offset:38912\n"
    "s_waitcnt lgkmcnt(12)\n"
    "v_mfma_f32_16x16x32_bf16 v[128:131], v[0:3], a[32:35], v[128:131]\n"
    "ds_read_b64_tr_b16 v[32:33], v96 offset:40960\n"
    "v_mfma_f32_16x16x32_bf16 v[132:135], v[0:3], a[36:39], v[132:135]\n"
    "ds_read_b64_tr_b16 v[34:35], v96 offset:43008\n"
    "buffer_load_dwordx4 a[0:3], v108, s[40:43], s56 offen sc1\n"
    "v_mfma_f32_16x16x32_bf16 v[136:139], v[4:7], a[32:35], v[136:139]\n"
    "ds_read_b64_tr_b16 v[36:37], v99 offset:40960\n"
    "v_mfma_f32_16x16x32_bf16 v[140:143], v[4:7], a[36:39], v[140:143]\n"
    "ds_read_b64_tr_b16 v[38:39], v99 offset:43008\n"
    "s_waitcnt lgkmcnt(12)\n"
    "v_mfma_f32_16x16x32_bf16 v[144:147], v[8:11], a[32:35], v[144:147]\n"
    "ds_read_b64_tr_b16 v[40:41], v102 offset:40960\n"
    "v_mfma_f32_16x16x32_bf16 v[148:151], v[8:11], a[36:39], v[148:151]\n"
    "ds_read_b64_tr_b16 v[42:43], v102 offset:43008\n"
    "buffer_load_dwordx4 a[4:7], v111, s[40:43], s56 offen sc1\n"
    "v_mfma_f32_16x16x32_bf16 v[152:155], v[12:15], a[32:35], v[152:155]\n"
    "ds_read_b64_tr_b16 v[44:45], v105 offset:40960\n"
    "s_add_u32 s56, s56, 131072\n"
    "v_mfma_f32_16x16x32_bf16 v[156:159], v[12:15], a[36:39], v[156:159]\n"
    "ds_read_b64_tr_b16 v[46:47], v105 offset:43008\n"
    "s_waitcnt lgkmcnt(12)\n"
    "v_mfma_f32_16x16x32_bf16 v[160:163], v[16:19], a[32:35], v[160:163]\n"
    "ds_read_b64_tr_b16 v[48:49], v96 offset:45056\n"
    "v_mfma_f32_16x16x32_bf16 v[164:167], v[16:19], a[36:39], v[164:167]\n"
    "ds_read_b64_tr_b16 v[50:51], v96 offset:47104\n"
    "buffer_load_dwordx4 a[8:11], v108, s[40:43], s56 offen sc1\n"
    "v_mfma_f32_16x16x32_bf16 v[168:171], v[20:23], a[32:35], v[168:171]\n"
    "ds_read_b64_tr_b16 v[52:53], v99 offset:45056\n"
    "v_mfma_f32_16x16x32_bf16 v[172:175], v[20:23], a[36:39], v[172:175]\n"
    "ds_read_b64_tr_b16 v[54:55], v99 offset:47104\n"
    "s_waitcnt lgkmcnt(12)\n"
    "v_mfma_f32_16x16x32_bf16 v[176:179], v[24:27], a[32:35], v[176:179]\n"
    "ds_read_b64_tr_b16 v[56:57], v102 offset:45056\n"
    "v_mfma_f32_16x16x32_bf16 v[180:183], v[24:27], a[36:39], v[180:183]\n"
    "ds_read_b64_tr_b16 v[58:59], v102 offset:47104\n"
    "buffer_load_dwordx4 a[12:15], v111, s[40:43], s56 offen sc1\n"
    "v_mfma_f32_16x16x32_bf16 v[184:187], v[28:31], a[32:35], v[184:187]\n"
    "ds_read_b64_tr_b16 v[60:61], v105 offset:45056\n"
    "s_add_u32 s56, s56, 131072\n"
    "v_mfma_f32_16x16x32_bf16 v[188:191], v[28:31], a[36:39], v[188:191]\n"
    "ds_read_b64_tr_b16 v[62:63], v105 offset:47104\n"
    "s_waitcnt vmcnt(28) lgkmcnt(12)\n"
    "s_barrier\n"
    "v_mfma_f32_16x16x32_bf16 v[128:131], v[32:35], a[40:43], v[128:131]\n"
    "ds_read_b64_tr_b16 v[0:1], v97 offset:0\n"
    "s_add_u32 m0, s62, 16384\n"
    "v_mfma_f32_16x16x32_bf16 v[132:135], v[32:35], a[44:47], v[132:135]\n"
    "ds_read_b64_tr_b16 v[2:3], v97 offset:2048\n"
    "buffer_load_dwordx4 v109, s[44:47], s57 offen lds\n"
    "v_mfma_f32_16x16x32_bf16 v[136:139], v[36:39], a[40:43], v[136:139]\n"
    "ds_read_b64_tr_b16 v[4:5], v100 offset:0\n"
    "v_mfma_f32_16x16x32_bf16 v[140:143], v[36:39], a[44:47], v[140:143]\n"
    "ds_read_b64_tr_b16 v[6:7], v100 offset:2048\n"
    "s_waitcnt lgkmcnt(12)\n"
    "v_mfma_f32_16x16x32_bf16 v[144:147], v[40:43], a[40:43], v[144:147]\n"
    "ds_read_b64_tr_b16 v[8:9], v103 offset:0\n"
    "s_add_u32 m0, s62, 20352\n"
    "v_mfma_f32_16x16x32_bf16 v[148:151], v[40:43], a[44:47], v[148:151]\n"
    "ds_read_b64_tr_b16 v[10:11], v103 offset:2048\n"
    "buffer_load_dwordx4 v109, s[44:47], s57 offen offset:128 lds\n"
    "v_mfma_f32_16x16x32_bf16 v[152:155], v[44:47], a[40:43], v[152:155]\n"
    "ds_read_b64_tr_b16 v[12:13], v106 offset:0\n"
    "s_add_u32 s57, s57, 131072\n"
    "v_mfma_f32_16x16x32_bf16 v[156:159], v[44:47], a[44:47], v[156:159]\n"
    "ds_read_b64_tr_b16 v[14:15], v106 offset:2048\n"
    "s_waitcnt lgkmcnt(12)\n"
    "v_mfma_f32_16x16x32_bf16 v[160:163], v[48:51], a[40:43], v[160:163]\n"
    "ds_read_b64_tr_b16 v[16:17], v97 offset:4096\n"
    "s_add_u32 m0, s62, 24576\n"
    "v_mfma_f32_16x16x32_bf16 v[164:167], v[48:51], a[44:47], v[164:167]\n"
    "ds_read_b64_tr_b16 v[18:19], v97 offset:6144\n"
    "buffer_load_dwordx4 v109, s[44:47], s59 offen lds\n"
    "v_mfma_f32_16x16x32_bf16 v[168:171], v[52:55], a[40:43], v[168:171]\n"
    "ds_read_b64_tr_b16 v[20:21], v100 offset:4096\n"
    "v_mfma_f32_16x16x32_bf16 v[172:175], v[52:55], a[44:47], v[172:175]\n"
    "ds_read_b64_tr_b16 v[22:23], v100 offset:6144\n"
    "s_waitcnt lgkmcnt(12)\n"
    "v_mfma_f32_16x16x32_bf16 v[176:179], v[56:59], a[40:43], v[176:179]\n"
    "ds_read_b64_tr_b16 v[24:25], v103 offset:4096\n"
    "s_add_u32 m0, s62, 28544\n"
    "v_mfma_f32_16x16x32_bf16 v[180:183], v[56:59], a[44:47], v[180:183]\n"
    "ds_read_b64_tr_b16 v[26:27], v103 offset:6144\n"
    "buffer_load_dwordx4 v109, s[44:47], s59 offen offset:128 lds\n"
    "v_mfma_f32_16x16x32_bf16 v[184:187], v[60:63], a[40:43], v[184:187]\n"
    "ds_read_b64_tr_b16 v[28:29], v106 offset:4096\n"
    "s_add_u32 s59, s59, 131072\n"
    "v_mfma_f32_16x16x32_bf16 v[188:191], v[60:63], a[44:47], v[188:191]\n"
    "ds_read_b64_tr_b16 v[30:31], v106 offset:6144\n"
    "s_waitcnt lgkmcnt(12)\n"
    "v_mfma_f32_16x16x32_bf16 v[128:131], v[0:3], a[48:51], v[128:131]\n"
    "ds_read_b64_tr_b16 v[32:33], v97 offset:8192\n"
    "v_mfma_f32_16x16x32_bf16 v[132:135], v[0:3], a[52:55], v[132:135]\n"
    "ds_read_b64_tr_b16 v[34:35], v97 offset:10240\n"
    "buffer_load_dwordx4 a[16:19], v108, s[40:43], s56 offen sc1\n"
    "v_mfma_f32_16x16x32_bf16 v[136:139], v[4:7], a[48:51], v[136:139]\n"
    "ds_read_b64_tr_b16 v[36:37], v100 offset:8192\n"
    "v_mfma_f32_16x16x32_bf16 v[140:143], v[4:7], a[52:55], v[140:143]\n"
    "ds_read_b64_tr_b16 v[38:39], v100 offset:10240\n"
    "s_waitcnt lgkmcnt(12)\n"
    "v_mfma_f32_16x16x32_bf16 v[144:147], v[8:11], a[48:51], v[144:147]\n"
    "ds_read_b64_tr_b16 v[40:41], v103 offset:8192\n"
    "v_mfma_f32_16x16x32_bf16 v[148:151], v[8:11], a[52:55], v[148:151]\n"
    "ds_read_b64_tr_b16 v[42:43], v103 offset:10240\n"
    "buffer_load_dwordx4 a[20:23], v111, s[40:43], s56 offen sc1\n"
    "v_mfma_f32_16x16x32_bf16 v[152:155], v[12:15], a[48:51], v[152:155]\n"
    "ds_read_b64_tr_b16 v[44:45], v106 offset:8192\n"
    "s_add_u32 s56, s56, 131072\n"
    "v_mfma_f32_16x16x32_bf16 v[156:159], v[12:15], a[52:55], v[156:159]\n"
    "ds_read_b64_tr_b16 v[46:47], v106 offset:10240\n"
    "s_waitcnt lgkmcnt(12)\n"
    "v_mfma_f32_16x16x32_bf16 v[160:163], v[16:19], a[48:51], v[160:163]\n"
    "ds_read_b64_tr_b16 v[48:49], v97 offset:12288\n"
    "v_mfma_f32_16x16x32_bf16 v[164:167], v[16:19], a[52:55], v[164:167]\n"
    "ds_read_b64_tr_b16 v[50:51], v97 offset:14336\n"
    "buffer_load_dwordx4 a[24:27], v108, s[40:43], s56 offen sc1\n"
    "v_mfma_f32_16x16x32_bf16 v[168:171], v[20:23], a[48:51], v[168:171]\n"
    "ds_read_b64_tr_b16 v[52:53], v100 offset:12288\n"
    "v_mfma_f32_16x16x32_bf16 v[172:175], v[20:23], a[52:55], v[172:175]\n"
    "ds_read_b64_tr_b16 v[54:55], v100 offset:14336\n"
    "s_waitcnt lgkmcnt(12)\n"
    "v_mfma_f32_16x16x32_bf16 v[176:179], v[24:27], a[48:51], v[176:179]\n"
    "ds_read_b64_tr_b16 v[56:57], v103 offset:12288\n"
    "v_mfma_f32_16x16x32_bf16 v[180:183], v[24:27], a[52:55], v[180:183]\n"
    "ds_read_b64_tr_b16 v[58:59], v103 offset:14336\n"
    "buffer_load_dwordx4 a[28:31], v111, s[40:43], s56 offen sc1\n"
    "v_mfma_f32_16x16x32_bf16 v[184:187], v[28:31], a[48:51], v[184:187]\n"
    "ds_read_b64_tr_b16 v[60:61], v106 offset:12288\n"
    "s_add_u32 s56, s56, 131072\n"
    "v_mfma_f32_16x16x32_bf16 v[188:191], v[28:31], a[52:55], v[188:191]\n"
    "ds_read_b64_tr_b16 v[62:63], v106 offset:14336\n"
    "s_waitcnt vmcnt(32) lgkmcnt(12)\n"
    "s_barrier\n"
    "v_mfma_f32_16x16x32_bf16 v[128:131], v[32:35], a[56:59], v[128:131]\n"
    "ds_read_b64_tr_b16 v[0:1], v97 offset:16384\n"
    "s_add_u32 m0, s62, 32768\n"
    "v_mfma_f32_16x16x32_bf16 v[132:135], v[32:35], a[60:63], v[132:135]\n"
    "ds_read_b64_tr_b16 v[2:3], v97 offset:18432\n"
    "buffer_load_dwordx4 v109, s[44:47], s57 offen lds\n"
    "v_mfma_f32_16x16x32_bf16 v[136:139], v[36:39], a[56:59], v[136:139]\n"
    "ds_read_b64_tr_b16 v[4:5], v100 offset:16384\n"
    "v_mfma_f32_16x16x32_bf16 v[140:143], v[36:39], a[60:63], v[140:143]\n"
    "ds_read_b64_tr_b16 v[6:7], v100 offset:18432\n"
    "s_waitcnt lgkmcnt(12)\n"
    "v_mfma_f32_16x16x32_bf16 v[144:147], v[40:43], a[56:59], v[144:147]\n"
    "ds_read_b64_tr_b16 v[8:9], v103 offset:16384\n"
    "s_add_u32 m0, s62, 36736\n"
    "v_mfma_f32_16x16x32_bf16 v[148:151], v[40:43], a[60:63], v[148:151]\n"
    "ds_read_b64_tr_b16 v[10:11], v103 offset:18432\n"
    "buffer_load_dwordx4 v109, s[44:47], s57 offen offset:128 lds\n"
    "v_mfma_f32_16x16x32_bf16 v[152:155], v[44:47], a[56:59], v[152:155]\n"
    "ds_read_b64_tr_b16 v[12:13], v106 offset:16384\n"
    "s_add_u32 s57, s57, 131072\n"
    "v_mfma_f32_16x16x32_bf16 v[156:159], v[44:47], a[60:63], v[156:159]\n"
    "ds_read_b64_tr_b16 v[14:15], v106 offset:18432\n"
    "s_waitcnt lgkmcnt(12)\n"
    "v_mfma_f32_16x16x32_bf16 v[160:163], v[48:51], a[56:59], v[160:163]\n"
    "ds_read_b64_tr_b16 v[16:17], v97 offset:20480\n"
    "s_add_u32 m0, s62, 40960\n"
    "v_mfma_f32_16x16x32_bf16 v[164:167], v[48:51], a[60:63], v[164:167]\n"
    "ds_read_b64_tr_b16 v[18:19], v97 offset:22528\n"
    "buffer_load_dwordx4 v109, s[44:47], s59 offen lds\n"
    "v_mfma_f32_16x16x32_bf16 v[168:171], v[52:55], a[56:59], v[168:171]\n"
    "ds_read_b64_tr_b16 v[20:21], v100 offset:20480\n"
    "v_mfma_f32_16x16x32_bf16 v[172:175], v[52:55], a[60:63], v[172:175]\n"
    "ds_read_b64_tr_b16 v[22:23], v100 offset:22528\n"
    "s_waitcnt lgkmcnt(12)\n"
    "v_mfma_f32_16x16x32_bf16 v[176:179], v[56:59], a[56:59], v[176:179]\n"
    "ds_read_b64_tr_b16 v[24:25], v103 offset:20480\n"
    "s_add_u32 m0, s62, 44928\n"
    "v_mfma_f32_16x16x32_bf16 v[180:183], v[56:59], a[60:63], v[180:183]\n"
    "ds_read_b64_tr_b16 v[26:27], v103 offset:22528\n"
    "buffer_load_dwordx4 v109, s[44:47], s59 offen offset:128 lds\n"
    "v_mfma_f32_16x16x32_bf16 v[184:187], v[60:63], a[56:59], v[184:187]\n"
    "ds_read_b64_tr_b16 v[28:29], v106 offset:20480\n"
    "s_add_u32 s59, s59, 131072\n"
    "v_mfma_f32_16x16x32_bf16 v[188:191], v[60:63], a[60:63], v[188:191]\n"
    "ds_read_b64_tr_b16 v[30:31], v106 offset:22528\n"
    "s_waitcnt lgkmcnt(12)\n"
    "v_mfma_f32_16x16x32_bf16 v[128:131], v[0:3], a[64:67], v[128:131]\n"
    "ds_read_b64_tr_b16 v[32:33], v97 offset:24576\n"
    "v_mfma_f32_16x16x32_bf16 v[132:135], v[0:3], a[68:71], v[132:135]\n"
    "ds_read_b64_tr_b16 v[34:35], v97 offset:26624\n"
    "buffer_load_dwordx4 a[32:35], v108, s[40:43], s56 offen sc1\n"
    "v_mfma_f32_16x16x32_bf16 v[136:139], v[4:7], a[64:67], v[136:139]\n"
    "ds_read_b64_tr_b16 v[36:37], v100 offset:24576\n"
    "v_mfma_f32_16x16x32_bf16 v[140:143], v[4:7], a[68:71], v[140:143]\n"
    "ds_read_b64_tr_b16 v[38:39], v100 offset:26624\n"
    "s_waitcnt lgkmcnt(12)\n"
    "v_mfma_f32_16x16x32_bf16 v[144:147], v[8:11], a[64:67], v[144:147]\n"
    "ds_read_b64_tr_b16 v[40:41], v103 offset:24576\n"
    "v_mfma_f32_16x16x32_bf16 v[148:151], v[8:11], a[68:71], v[148:151]\n"
    "ds_read_b64_tr_b16 v[42:43], v103 offset:26624\n"
    "buffer_load_dwordx4 a[36:39], v111, s[40:43], s56 offen sc1\n"
    "v_mfma_f32_16x16x32_bf16 v[152:155], v[12:15], a[64:67], v[152:155]\n"
    "ds_read_b64_tr_b16 v[44:45], v106 offset:24576\n"
    "s_add_u32 s56, s56, 131072\n"
    "v_mfma_f32_16x16x32_bf16 v[156:159], v[12:15], a[68:71], v[156:159]\n"
    "ds_read_b64_tr_b16 v[46:47], v106 offset:26624\n"
    "s_waitcnt lgkmcnt(12)\n"
    "v_mfma_f32_16x16x32_bf16 v[160:163], v[16:19], a[64:67], v[160:163]\n"
    "ds_read_b64_tr_b16 v[48:49], v97 offset:28672\n"
    "v_mfma_f32_16x16x32_bf16 v[164:167], v[16:19], a[68:71], v[164:167]\n"
    "ds_read_b64_tr_b16 v[50:51], v97 offset:30720\n"
    "buffer_load_dwordx4 a[40:43], v108, s[40:43], s56 offen sc1\n"
    "v_mfma_f32_16x16x32_bf16 v[168:171], v[20:23], a[64:67], v[168:171]\n"
    "ds_read_b64_tr_b16 v[52:53], v100 offset:28672\n"
    "v_mfma_f32_16x16x32_bf16 v[172:175], v[20:23], a[68:71], v[172:175]\n"
    "ds_read_b64_tr_b16 v[54:55], v100 offset:30720\n"
    "s_waitcnt lgkmcnt(12)\n"
    "v_mfma_f32_16x16x32_bf16 v[176:179], v[24:27], a[64:67], v[176:179]\n"
    "ds_read_b64_tr_b16 v[56:57], v103 offset:28672\n"
    "v_mfma_f32_16x16x32_bf16 v[180:183], v[24:27], a[68:71], v[180:183]\n"
    "ds_read_b64_tr_b16 v[58:59], v103 offset:30720\n"
    "buffer_load_dwordx4 a[44:47], v111, s[40:43], s56 offen sc1\n"
    "v_mfma_f32_16x16x32_bf16 v[184:187], v[28:31], a[64:67], v[184:187]\n"
    "ds_read_b64_tr_b16 v[60:61], v106 offset:28672\n"
    "s_add_u32 s56, s56, 131072\n"
    "v_mfma_f32_16x16x32_bf16 v[188:191], v[28:31], a[68:71], v[188:191]\n"
    "ds_read_b64_tr_b16 v[62:63], v106 offset:30720\n"
    "s_waitcnt vmcnt(36) lgkmcnt(12)\n"
    "s_barrier\n"
    "v_mfma_f32_16x16x32_bf16 v[128:131], v[32:35], a[72:75], v[128:131]\n"
    "ds_read_b64_tr_b16 v[0:1], v97 offset:32768\n"
    "s_add_u32 m0, s62, 49152\n"
    "v_mfma_f32_16x16x32_bf16 v[132:135], v[32:35], a[76:79], v[132:135]\n"
    "ds_read_b64_tr_b16 v[2:3], v97 offset:34816\n"
    "buffer_load_dwordx4 v109, s[44:47], s57 offen lds\n"
    "v_mfma_f32_16x16x32_bf16 v[136:139], v[36:39], a[72:75], v[136:139]\n"
    "ds_read_b64_tr_b16 v[4:5], v100 offset:32768\n"
    "v_mfma_f32_16x16x32_bf16 v[140:143], v[36:39], a[76:79], v[140:143]\n"
    "ds_read_b64_tr_b16 v[6:7], v100 offset:34816\n"
    "s_waitcnt lgkmcnt(12)\n"
    "v_mfma_f32_16x16x32_bf16 v[144:147], v[40:43], a[72:75], v[144:147]\n"
    "ds_read_b64_tr_b16 v[8:9], v103 offset:32768\n"
    "s_add_u32 m0, s62, 53120\n"
    "v_mfma_f32_16x16x32_bf16 v[148:151], v[40:43], a[76:79], v[148:151]\n"
    "ds_read_b64_tr_b16 v[10:11], v103 offset:34816\n"
    "buffer_load_dwordx4 v109, s[44:47], s57 offen offset:128 lds\n"
    "v_mfma_f32_16x16x32_bf16 v[152:155], v[44:47], a[72:75], v[152:155]\n"
    "ds_read_b64_tr_b16 v[12:13], v106 offset:32768\n"
    "s_add_u32 s57, s57, 131072\n"
    "v_mfma_f32_16x16x32_bf16 v[156:159], v[44:47], a[76:79], v[156:159]\n"
    "ds_read_b64_tr_b16 v[14:15], v106 offset:34816\n"
    "s_waitcnt lgkmcnt(12)\n"
    "v_mfma_f32_16x16x32_bf16 v[160:163], v[48:51], a[72:75], v[160:163]\n"
    "ds_read_b64_tr_b16 v[16:17], v97 offset:36864\n"
    "s_add_u32 m0, s62, 57344\n"
    "v_mfma_f32_16x16x32_bf16 v[164:167], v[48:51], a[76:79], v[164:167]\n"
    "ds_read_b64_tr_b16 v[18:19], v97 offset:38912\n"
    "buffer_load_dwordx4 v109, s[44:47], s59 offen lds\n"
    "v_mfma_f32_16x16x32_bf16 v[168:171], v[52:55], a[72:75], v[168:171]\n"
    "ds_read_b64_tr_b16 v[20:21], v100 offset:36864\n"
    "v_mfma_f32_16x16x32_bf16 v[172:175], v[52:55], a[76:79], v[172:175]\n"
    "ds_read_b64_tr_b16 v[22:23], v100 offset:38912\n"
    "s_waitcnt lgkmcnt(12)\n"
    "v_mfma_f32_16x16x32_bf16 v[176:179], v[56:59], a[72:75], v[176:179]\n"
    "ds_read_b64_tr_b16 v[24:25], v103 offset:36864\n"
    "s_add_u32 m0, s62, 61312\n"
    "v_mfma_f32_16x16x32_bf16 v[180:183], v[56:59], a[76:79], v[180:183]\n"
    "ds_read_b64_tr_b16 v[26:27], v103 offset:38912\n"
    "buffer_load_dwordx4 v109, s[44:47], s59 offen offset:128 lds\n"
    "v_mfma_f32_16x16x32_bf16 v[184:187], v[60:63], a[72:75], v[184:187]\n"
    "ds_read_b64_tr_b16 v[28:29], v106 offset:36864\n"
    "s_add_u32 s59, s59, 131072\n"
    "v_mfma_f32_16x16x32_bf16 v[188:191], v[60:63], a[76:79], v[188:191]\n"
    "ds_read_b64_tr_b16 v[30:31], v106 offset:38912\n"
    "s_waitcnt lgkmcnt(12)\n"
    "v_mfma_f32_16x16x32_bf16 v[128:131], v[0:3], a[80:83], v[128:131]\n"
    "ds_read_b64_tr_b16 v[32:33], v97 offset:40960\n"
    "v_mfma_f32_16x16x32_bf16 v[132:135], v[0:3], a[84:87], v[132:135]\n"
    "ds_read_b64_tr_b16 v[34:35], v97 offset:43008\n"
    "buffer_load_dwordx4 a[48:51], v108, s[40:43], s56 offen sc1\n"
    "v_mfma_f32_16x16x32_bf16 v[136:139], v[4:7], a[80:83], v[136:139]\n"
    "ds_read_b64_tr_b16 v[36:37], v100 offset:40960\n"
    "v_mfma_f32_16x16x32_bf16 v[140:143], v[4:7], a[84:87], v[140:143]\n"
    "ds_read_b64_tr_b16 v[38:39], v100 offset:43008\n"
    "s_waitcnt lgkmcnt(12)\n"
    "v_mfma_f32_16x16x32_bf16 v[144:147], v[8:11], a[80:83], v[144:147]\n"
    "ds_read_b64_tr_b16 v[40:41], v103 offset:40960\n"
    "v_mfma_f32_16x16x32_bf16 v[148:151], v[8:11], a[84:87], v[148:151]\n"
    "ds_read_b64_tr_b16 v[42:43], v103 offset:43008\n"
    "buffer_load_dwordx4 a[52:55], v111, s[40:43], s56 offen sc1\n"
    "v_mfma_f32_16x16x32_bf16 v[152:155], v[12:15], a[80:83], v[152:155]\n"
    "ds_read_b64_tr_b16 v[44:45], v106 offset:40960\n"
    "s_add_u32 s56, s56, 131072\n"
    "v_mfma_f32_16x16x32_bf16 v[156:159], v[12:15], a[84:87], v[156:159]\n"
    "ds_read_b64_tr_b16 v[46:47], v106 offset:43008\n"
    "s_waitcnt lgkmcnt(12)\n"
    "v_mfma_f32_16x16x32_bf16 v[160:163], v[16:19], a[80:83], v[160:163]\n"
    "ds_read_b64_tr_b16 v[48:49], v97 offset:45056\n"
    "v_mfma_f32_16x16x32_bf16 v[164:167], v[16:19], a[84:87], v[164:167]\n"
    "ds_read_b64_tr_b16 v[50:51], v97 offset:47104\n"
    "buffer_load_dwordx4 a[56:59], v108, s[40:43], s56 offen sc1\n"
    "v_mfma_f32_16x16x32_bf16 v[168:171], v[20:23], a[80:83], v[168:171]\n"
    "ds_read_b64_tr_b16 v[52:53], v100 offset:45056\n"
    "v_mfma_f32_16x16x32_bf16 v[172:175], v[20:23], a[84:87], v[172:175]\n"
    "ds_read_b64_tr_b16 v[54:55], v100 offset:47104\n"
    "s_waitcnt lgkmcnt(12)\n"
    "v_mfma_f32_16x16x32_bf16 v[176:179], v[24:27], a[80:83], v[176:179]\n"
    "ds_read_b64_tr_b16 v[56:57], v103 offset:45056\n"
    "v_mfma_f32_16x16x32_bf16 v[180:183], v[24:27], a[84:87], v[180:183]\n"
    "ds_read_b64_tr_b16 v[58:59], v103 offset:47104\n"
    "buffer_load_dwordx4 a[60:63], v111, s[40:43], s56 offen sc1\n"
    "v_mfma_f32_16x16x32_bf16 v[184:187], v[28:31], a[80:83], v[184:187]\n"
    "ds_read_b64_tr_b16 v[60:61], v106 offset:45056\n"
    "s_add_u32 s56, s56, 131072\n"
    "v_mfma_f32_16x16x32_bf16 v[188:191], v[28:31], a[84:87], v[188:191]\n"
    "ds_read_b64_tr_b16 v[62:63], v106 offset:47104\n"
    "s_waitcnt vmcnt(40) lgkmcnt(12)\n"
    "s_barrier\n"
    "v_mfma_f32_16x16x32_bf16 v[128:131], v[32:35], a[88:91], v[128:131]\n"
    "ds_read_b64_tr_b16 v[0:1], v98 offset:0\n"
    "s_add_u32 m0, s62, 65536\n"
    "v_mfma_f32_16x16x32_bf16 v[132:135], v[32:35], a[92:95], v[132:135]\n"
    "ds_read_b64_tr_b16 v[2:3], v98 offset:2048\n"
    "buffer_load_dwordx4 v109, s[44:47], s57 offen lds\n"
    "v_mfma_f32_16x16x32_bf16 v[136:139], v[36:39], a[88:91], v[136:139]\n"
    "ds_read_b64_tr_b16 v[4:5], v101 offset:0\n"
    "v_mfma_f32_16x16x32_bf16 v[140:143], v[36:39], a[92:95], v[140:143]\n"
    "ds_read_b64_tr_b16 v[6:7], v101 offset:2048\n"
    "s_waitcnt lgkmcnt(12)\n"
    "v_mfma_f32_16x16x32_bf16 v[144:147], v[40:43], a[88:91], v[144:147]\n"
    "ds_read_b64_tr_b16 v[8:9], v104 offset:0\n"
    "s_add_u32 m0, s62, 69504\n"
    "v_mfma_f32_16x16x32_bf16 v[148:151], v[40:43], a[92:95], v[148:151]\n"
    "ds_read_b64_tr_b16 v[10:11], v104 offset:2048\n"
    "buffer_load_dwordx4 v109, s[44:47], s57 offen offset:128 lds\n"
    "v_mfma_f32_16x16x32_bf16 v[152:155], v[44:47], a[88:91], v[152:155]\n"
    "ds_read_b64_tr_b16 v[12:13], v107 offset:0\n"
    "s_add_u32 s57, s57, 131072\n"
    "v_mfma_f32_16x16x32_bf16 v[156:159], v[44:47], a[92:95], v[156:159]\n"
    "ds_read_b64_tr_b16 v[14:15], v107 offset:2048\n"
    "s_waitcnt lgkmcnt(12)\n"
    "v_mfma_f32_16x16x32_bf16 v[160:163], v[48:51], a[88:91], v[160:163]\n"
    "ds_read_b64_tr_b16 v[16:17], v98 offset:4096\n"
    "s_add_u32 m0, s62, 73728\n"
    "v_mfma_f32_16x16x32_bf16 v[164:167], v[48:51], a[92:95], v[164:167]\n"
    "ds_read_b64_tr_b16 v[18:19], v98 offset:6144\n"
    "buffer_load_dwordx4 v109, s[44:47], s59 offen lds\n"
    "v_mfma_f32_16x16x32_bf16 v[168:171], v[52:55], a[88:91], v[168:171]\n"
    "ds_read_b64_tr_b16 v[20:21], v101 offset:4096\n"
    "v_mfma_f32_16x16x32_bf16 v[172:175], v[52:55], a[92:95], v[172:175]\n"
    "ds_read_b64_tr_b16 v[22:23], v101 offset:6144\n"
    "s_waitcnt lgkmcnt(12)\n"
    "v_mfma_f32_16x16x32_bf16 v[176:179], v[56:59], a[88:91], v[176:179]\n"
    "ds_read_b64_tr_b16 v[24:25], v104 offset:4096\n"
    "s_add_u32 m0, s62, 77696\n"
    "v_mfma_f32_16x16x32_bf16 v[180:183], v[56:59], a[92:95], v[180:183]\n"
    "ds_read_b64_tr_b16 v[26:27], v104 offset:6144\n"
    "buffer_load_dwordx4 v109, s[44:47], s59 offen offset:128 lds\n"
    "v_mfma_f32_16x16x32_bf16 v[184:187], v[60:63], a[88:91], v[184:187]\n"
    "ds_read_b64_tr_b16 v[28:29], v107 offset:4096\n"
    "s_add_u32 s59, s59, 131072\n"
    "v_mfma_f32_16x16x32_bf16 v[188:191], v[60:63], a[92:95], v[188:191]\n"
    "ds_read_b64_tr_b16 v[30:31], v107 offset:6144\n"
    "s_waitcnt lgkmcnt(12)\n"
    "v_mfma_f32_16x16x32_bf16 v[128:131], v[0:3], a[96:99], v[128:131]\n"
    "ds_read_b64_tr_b16 v[32:33], v98 offset:8192\n"
    "v_mfma_f32_16x16x32_bf16 v[132:135], v[0:3], a[100:103], v[132:135]\n"
    "ds_read_b64_tr_b16 v[34:35], v98 offset:10240\n"
    "buffer_load_dwordx4 a[64:67], v108, s[40:43], s56 offen sc1\n"
    "v_mfma_f32_16x16x32_bf16 v[136:139], v[4:7], a[96:99], v[136:139]\n"
    "ds_read_b64_tr_b16 v[36:37], v101 offset:8192\n"
    "v_mfma_f32_16x16x32_bf16 v[140:143], v[4:7], a[100:103], v[140:143]\n"
    "ds_read_b64_tr_b16 v[38:39], v101 offset:10240\n"
    "s_waitcnt lgkmcnt(12)\n"
    "v_mfma_f32_16x16x32_bf16 v[144:147], v[8:11], a[96:99], v[144:147]\n"
    "ds_read_b64_tr_b16 v[40:41], v104 offset:8192\n"
    "v_mfma_f32_16x16x32_bf16 v[148:151], v[8:11], a[100:103], v[148:151]\n"
    "ds_read_b64_tr_b16 v[42:43], v104 offset:10240\n"
    "buffer_load_dwordx4 a[68:71], v111, s[40:43], s56 offen sc1\n"
    "v_mfma_f32_16x16x32_bf16 v[152:155], v[12:15], a[96:99], v[152:155]\n"
    "ds_read_b64_tr_b16 v[44:45], v107 offset:8192\n"
    "s_add_u32 s56, s56, 131072\n"
    "v_mfma_f32_16x16x32_bf16 v[156:159], v[12:15], a[100:103], v[156:159]\n"
    "ds_read_b64_tr_b16 v[46:47], v107 offset:10240\n"
    "s_waitcnt lgkmcnt(12)\n"
    "v_mfma_f32_16x16x32_bf16 v[160:163], v[16:19], a[96:99], v[160:163]\n"
    "ds_read_b64_tr_b16 v[48:49], v98 offset:12288\n"
    "v_mfma_f32_16x16x32_bf16 v[164:167], v[16:19], a[100:103], v[164:167]\n"
    "ds_read_b64_tr_b16 v[50:51], v98 offset:14336\n"
    "buffer_load_dwordx4 a[72:75], v108, s[40:43], s56 offen sc1\n"
    "v_mfma_f32_16x16x32_bf16 v[168:171], v[20:23], a[96:99], v[168:171]\n"
    "ds_read_b64_tr_b16 v[52:53], v101 offset:12288\n"
    "v_mfma_f32_16x16x32_bf16 v[172:175], v[20:23], a[100:103], v[172:175]\n"
    "ds_read_b64_tr_b16 v[54:55], v101 offset:14336\n"
    "s_waitcnt lgkmcnt(12)\n"
    "v_mfma_f32_16x16x32_bf16 v[176:179], v[24:27], a[96:99], v[176:179]\n"
    "ds_read_b64_tr_b16 v[56:57], v104 offset:12288\n"
    "v_mfma_f32_16x16x32_bf16 v[180:183], v[24:27], a[100:103], v[180:183]\n"
    "ds_read_b64_tr_b16 v[58:59], v104 offset:14336\n"
    "buffer_load_dwordx4 a[76:79], v111, s[40:43], s56 offen sc1\n"
    "v_mfma_f32_16x16x32_bf16 v[184:187], v[28:31], a[96:99], v[184:187]\n"
    "ds_read_b64_tr_b16 v[60:61], v107 offset:12288\n"
    "s_add_u32 s56, s56, 131072\n"
    "v_mfma_f32_16x16x32_bf16 v[188:191], v[28:31], a[100:103], v[188:191]\n"
    "ds_read_b64_tr_b16 v[62:63], v107 offset:14336\n"
    "s_waitcnt vmcnt(44) lgkmcnt(12)\n"
    "s_barrier\n"
    "v_mfma_f32_16x16x32_bf16 v[128:131], v[32:35], a[104:107], v[128:131]\n"
    "ds_read_b64_tr_b16 v[0:1], v98 offset:16384\n"
    "s_add_u32 m0, s62, 81920\n"
    "v_mfma_f32_16x16x32_bf16 v[132:135], v[32:35], a[108:111], v[132:135]\n"
    "ds_read_b64_tr_b16 v[2:3], v98 offset:18432\n"
    "buffer_load_dwordx4 v109, s[44:47], s57 offen lds\n"
    "v_mfma_f32_16x16x32_bf16 v[136:139], v[36:39], a[104:107], v[136:139]\n"
    "ds_read_b64_tr_b16 v[4:5], v101 offset:16384\n"
    "v_mfma_f32_16x16x32_bf16 v[140:143], v[36:39], a[108:111], v[140:143]\n"
    "ds_read_b64_tr_b16 v[6:7], v101 offset:18432\n"
    "s_waitcnt lgkmcnt(12)\n"
    "v_mfma_f32_16x16x32_bf16 v[144:147], v[40:43], a[104:107], v[144:147]\n"
    "ds_read_b64_tr_b16 v[8:9], v104 offset:16384\n"
    "s_add_u32 m0, s62, 85888\n"
    "v_mfma_f32_16x16x32_bf16 v[148:151], v[40:43], a[108:111], v[148:151]\n"
    "ds_read_b64_tr_b16 v[10:11], v104 offset:18432\n"
    "buffer_load_dwordx4 v109, s[44:47], s57 offen offset:128 lds\n"
    "v_mfma_f32_16x16x32_bf16 v[152:155], v[44:47], a[104:107], v[152:155]\n"
    "ds_read_b64_tr_b16 v[12:13], v107 offset:16384\n"
    "s_add_u32 s57, s57, 131072\n"
    "v_mfma_f32_16x16x32_bf16 v[156:159], v[44:47], a[108:111], v[156:159]\n"
    "ds_read_b64_tr_b16 v[14:15], v107 offset:18432\n"
    "s_waitcnt lgkmcnt(12)\n"
    "v_mfma_f32_16x16x32_bf16 v[160:163], v[48:51], a[104:107], v[160:163]\n"
    "ds_read_b64_tr_b16 v[16:17], v98 offset:20480\n"
    "s_add_u32 m0, s62, 90112\n"
    "v_mfma_f32_16x16x32_bf16 v[164:167], v[48:51], a[108:111], v[164:167]\n"
    "ds_read_b64_tr_b16 v[18:19], v98 offset:22528\n"
    "buffer_load_dwordx4 v109, s[44:47], s59 offen lds\n"
    "v_mfma_f32_16x16x32_bf16 v[168:171], v[52:55], a[104:107], v[168:171]\n"
    "ds_read_b64_tr_b16 v[20:21], v101 offset:20480\n"
    "v_mfma_f32_16x16x32_bf16 v[172:175], v[52:55], a[108:111], v[172:175]\n"
    "ds_read_b64_tr_b16 v[22:23], v101 offset:22528\n"
    "s_waitcnt lgkmcnt(12)\n"
    "v_mfma_f32_16x16x32_bf16 v[176:179], v[56:59], a[104:107], v[176:179]\n"
    "ds_read_b64_tr_b16 v[24:25], v104 offset:20480\n"
    "s_add_u32 m0, s62, 94080\n"
    "v_mfma_f32_16x16x32_bf16 v[180:183], v[56:59], a[108:111], v[180:183]\n"
    "ds_read_b64_tr_b16 v[26:27], v104 offset:22528\n"
    "buffer_load_dwordx4 v109, s[44:47], s59 offen offset:128 lds\n"
    "v_mfma_f32_16x16x32_bf16 v[184:187], v[60:63], a[104:107], v[184:187]\n"
    "ds_read_b64_tr_b16 v[28:29], v107 offset:20480\n"
    "s_add_u32 s59, s59, 131072\n"
    "v_mfma_f32_16x16x32_bf16 v[188:191], v[60:63], a[108:111], v[188:191]\n"
    "ds_read_b64_tr_b16 v[30:31], v107 offset:22528\n"
    "s_waitcnt lgkmcnt(12)\n"
    "v_mfma_f32_16x16x32_bf16 v[128:131], v[0:3], a[112:115], v[128:131]\n"
    "ds_read_b64_tr_b16 v[32:33], v98 offset:24576\n"
    "v_mfma_f32_16x16x32_bf16 v[132:135], v[0:3], a[116:119], v[132:135]\n"
    "ds_read_b64_tr_b16 v[34:35], v98 offset:26624\n"
    "buffer_load_dwordx4 a[80:83], v108, s[40:43], s56 offen sc1\n"
    "v_mfma_f32_16x16x32_bf16 v[136:139], v[4:7], a[112:115], v[136:139]\n"
    "ds_read_b64_tr_b16 v[36:37], v101 offset:24576\n"
    "v_mfma_f32_16x16x32_bf16 v[140:143], v[4:7], a[116:119], v[140:143]\n"
    "ds_read_b64_tr_b16 v[38:39], v101 offset:26624\n"
    "s_waitcnt lgkmcnt(12)\n"
    "v_mfma_f32_16x16x32_bf16 v[144:147], v[8:11], a[112:115], v[144:147]\n"
    "ds_read_b64_tr_b16 v[40:41], v104 offset:24576\n"
    "v_mfma_f32_16x16x32_bf16 v[148:151], v[8:11], a[116:119], v[148:151]\n"
    "ds_read_b64_tr_b16 v[42:43], v104 offset:26624\n"
    "buffer_load_dwordx4 a[84:87], v111, s[40:43], s56 offen sc1\n"
    "v_mfma_f32_16x16x32_bf16 v[152:155], v[12:15], a[112:115], v[152:155]\n"
    "ds_read_b64_tr_b16 v[44:45], v107 offset:24576\n"
    "s_add_u32 s56, s56, 131072\n"
    "v_mfma_f32_16x16x32_bf16 v[156:159], v[12:15], a[116:119], v[156:159]\n"
    "ds_read_b64_tr_b16 v[46:47], v107 offset:26624\n"
    "s_waitcnt lgkmcnt(12)\n"
    "v_mfma_f32_16x16x32_bf16 v[160:163], v[16:19], a[112:115], v[160:163]\n"
    "ds_read_b64_tr_b16 v[48:49], v98 offset:28672\n"
    "v_mfma_f32_16x16x32_bf16 v[164:167], v[16:19], a[116:119], v[164:167]\n"
    "ds_read_b64_tr_b16 v[50:51], v98 offset:30720\n"
    "buffer_load_dwordx4 a[88:91], v108, s[40:43], s56 offen sc1\n"
    "v_mfma_f32_16x16x32_bf16 v[168:171], v[20:23], a[112:115], v[168:171]\n"
    "ds_read_b64_tr_b16 v[52:53], v101 offset:28672\n"
    "v_mfma_f32_16x16x32_bf16 v[172:175], v[20:23], a[116:119], v[172:175]\n"
    "ds_read_b64_tr_b16 v[54:55], v101 offset:30720\n"
    "s_waitcnt lgkmcnt(12)\n"
    "v_mfma_f32_16x16x32_bf16 v[176:179], v[24:27], a[112:115], v[176:179]\n"
    "ds_read_b64_tr_b16 v[56:57], v104 offset:28672\n"
    "v_mfma_f32_16x16x32_bf16 v[180:183], v[24:27], a[116:119], v[180:183]\n"
    "ds_read_b64_tr_b16 v[58:59], v104 offset:30720\n"
    "buffer_load_dwordx4 a[92:95], v111, s[40:43], s56 offen sc1\n"
    "v_mfma_f32_16x16x32_bf16 v[184:187], v[28:31], a[112:115], v[184:187]\n"
    "ds_read_b64_tr_b16 v[60:61], v107 offset:28672\n"
    "s_add_u32 s56, s56, 131072\n"
    "v_mfma_f32_16x16x32_bf16 v[188:191], v[28:31], a[116:119], v[188:191]\n"
    "ds_read_b64_tr_b16 v[62:63], v107 offset:30720\n"
    "s_waitcnt vmcnt(48) lgkmcnt(12)\n"
    "s_barrier\n"
    "v_mfma_f32_16x16x32_bf16 v[128:131], v[32:35], a[120:123], v[128:131]\n"
    "ds_read_b64_tr_b16 v[0:1], v98 offset:32768\n"
    "s_add_u32 m0, s62, 98304\n"
    "v_mfma_f32_16x16x32_bf16 v[132:135], v[32:35], a[124:127], v[132:135]\n"
    "ds_read_b64_tr_b16 v[2:3], v98 offset:34816\n"
    "buffer_load_dwordx4 v109, s[44:47], s57 offen lds\n"
    "v_mfma_f32_16x16x32_bf16 v[136:139], v[36:39], a[120:123], v[136:139]\n"
    "ds_read_b64_tr_b16 v[4:5], v101 offset:32768\n"
    "v_mfma_f32_16x16x32_bf16 v[140:143], v[36:39], a[124:127], v[140:143]\n"
    "ds_read_b64_tr_b16 v[6:7], v101 offset:34816\n"
    "s_waitcnt lgkmcnt(12)\n"
    "v_mfma_f32_16x16x32_bf16 v[144:147], v[40:43], a[120:123], v[144:147]\n"
    "ds_read_b64_tr_b16 v[8:9], v104 offset:32768\n"
    "s_add_u32 m0, s62, 102272\n"
    "v_mfma_f32_16x16x32_bf16 v[148:151], v[40:43], a[124:127], v[148:151]\n"
    "ds_read_b64_tr_b16 v[10:11], v104 offset:34816\n"
    "buffer_load_dwordx4 v109, s[44:47], s57 offen offset:128 lds\n"
    "v_mfma_f32_16x16x32_bf16 v[152:155], v[44:47], a[120:123], v[152:155]\n"
    "ds_read_b64_tr_b16 v[12:13], v107 offset:32768\n"
    "s_add_u32 s57, s57, 131072\n"
    "v_mfma_f32_16x16x32_bf16 v[156:159], v[44:47], a[124:127], v[156:159]\n"
    "ds_read_b64_tr_b16 v[14:15], v107 offset:34816\n"
    "s_waitcnt lgkmcnt(12)\n"
    "v_mfma_f32_16x16x32_bf16 v[160:163], v[48:51], a[120:123], v[160:163]\n"
    "ds_read_b64_tr_b16 v[16:17], v98 offset:36864\n"
    "s_add_u32 m0, s62, 106496\n"
    "v_mfma_f32_16x16x32_bf16 v[164:167], v[48:51], a[124:127], v[164:167]\n"
    "ds_read_b64_tr_b16 v[18:19], v98 offset:38912\n"
    "buffer_load_dwordx4 v109, s[44:47], s59 offen lds\n"
    "v_mfma_f32_16x16x32_bf16 v[168:171], v[52:55], a[120:123], v[168:171]\n"
    "ds_read_b64_tr_b16 v[20:21], v101 offset:36864\n"
    "v_mfma_f32_16x16x32_bf16 v[172:175], v[52:55], a[124:127], v[172:175]\n"
    "ds_read_b64_tr_b16 v[22:23], v101 offset:38912\n"
    "s_waitcnt lgkmcnt(12)\n"
    "v_mfma_f32_16x16x32_bf16 v[176:179], v[56:59], a[120:123], v[176:179]\n"
    "ds_read_b64_tr_b16 v[24:25], v104 offset:36864\n"
    "s_add_u32 m0, s62, 110464\n"
    "v_mfma_f32_16x16x32_bf16 v[180:183], v[56:59], a[124:127], v[180:183]\n"
    "ds_read_b64_tr_b16 v[26:27], v104 offset:38912\n"
    "buffer_load_dwordx4 v109, s[44:47], s59 offen offset:128 lds\n"
    "v_mfma_f32_16x16x32_bf16 v[184:187], v[60:63], a[120:123], v[184:187]\n"
    "ds_read_b64_tr_b16 v[28:29], v107 offset:36864\n"
    "s_add_u32 s59, s59, 131072\n"
    "v_mfma_f32_16x16x32_bf16 v[188:191], v[60:63], a[124:127], v[188:191]\n"
    "ds_read_b64_tr_b16 v[30:31], v107 offset:38912\n"
    "s_waitcnt lgkmcnt(12)\n"
    "v_mfma_f32_16x16x32_bf16 v[128:131], v[0:3], a[128:131], v[128:131]\n"
    "ds_read_b64_tr_b16 v[32:33], v98 offset:40960\n"
    "v_mfma_f32_16x16x32_bf16 v[132:135], v[0:3], a[132:135], v[132:135]\n"
    "ds_read_b64_tr_b16 v[34:35], v98 offset:43008\n"
    "buffer_load_dwordx4 a[96:99], v108, s[40:43], s56 offen sc1\n"
    "v_mfma_f32_16x16x32_bf16 v[136:139], v[4:7], a[128:131], v[136:139]\n"
    "ds_read_b64_tr_b16 v[36:37], v101 offset:40960\n"
    "v_mfma_f32_16x16x32_bf16 v[140:143], v[4:7], a[132:135], v[140:143]\n"
    "ds_read_b64_tr_b16 v[38:39], v101 offset:43008\n"
    "s_waitcnt lgkmcnt(12)\n"
    "v_mfma_f32_16x16x32_bf16 v[144:147], v[8:11], a[128:131], v[144:147]\n"
    "ds_read_b64_tr_b16 v[40:41], v104 offset:40960\n"
    "v_mfma_f32_16x16x32_bf16 v[148:151], v[8:11], a[132:135], v[148:151]\n"
    "ds_read_b64_tr_b16 v[42:43], v104 offset:43008\n"
    "buffer_load_dwordx4 a[100:103], v111, s[40:43], s56 offen sc1\n"
    "v_mfma_f32_16x16x32_bf16 v[152:155], v[12:15], a[128:131], v[152:155]\n"
    "ds_read_b64_tr_b16 v[44:45], v107 offset:40960\n"
    "s_add_u32 s56, s56, 131072\n"
    "v_mfma_f32_16x16x32_bf16 v[156:159], v[12:15], a[132:135], v[156:159]\n"
    "ds_read_b64_tr_b16 v[46:47], v107 offset:43008\n"
    "s_waitcnt lgkmcnt(12)\n"
    "v_mfma_f32_16x16x32_bf16 v[160:163], v[16:19], a[128:131], v[160:163]\n"
    "ds_read_b64_tr_b16 v[48:49], v98 offset:45056\n"
    "v_mfma_f32_16x16x32_bf16 v[164:167], v[16:19], a[132:135], v[164:167]\n"
    "ds_read_b64_tr_b16 v[50:51], v98 offset:47104\n"
    "buffer_load_dwordx4 a[104:107], v108, s[40:43], s56 offen sc1\n"
    "v_mfma_f32_16x16x32_bf16 v[168:171], v[20:23], a[128:131], v[168:171]\n"
    "ds_read_b64_tr_b16 v[52:53], v101 offset:45056\n"
    "v_mfma_f32_16x16x32_bf16 v[172:175], v[20:23], a[132:135], v[172:175]\n"
    "ds_read_b64_tr_b16 v[54:55], v101 offset:47104\n"
    "s_waitcnt lgkmcnt(12)\n"
    "v_mfma_f32_16x16x32_bf16 v[176:179], v[24:27], a[128:131], v[176:179]\n"
    "ds_read_b64_tr_b16 v[56:57], v104 offset:45056\n"
    "v_mfma_f32_16x16x32_bf16 v[180:183], v[24:27], a[132:135], v[180:183]\n"
    "ds_read_b64_tr_b16 v[58:59], v104 offset:47104\n"
    "buffer_load_dwordx4 a[108:111], v111, s[40:43], s56 offen sc1\n"
    "v_mfma_f32_16x16x32_bf16 v[184:187], v[28:31], a[128:131], v[184:187]\n"
    "ds_read_b64_tr_b16 v[60:61], v107 offset:45056\n"
    "s_add_u32 s56, s56, 131072\n"
    "v_mfma_f32_16x16x32_bf16 v[188:191], v[28:31], a[132:135], v[188:191]\n"
    "ds_read_b64_tr_b16 v[62:63], v107 offset:47104\n"
    "s_waitcnt vmcnt(48) lgkmcnt(12)\n"
    "s_barrier\n"
    "v_mfma_f32_16x16x32_bf16 v[128:131], v[32:35], a[136:139], v[128:131]\n"
    "ds_read_b64_tr_b16 v[0:1], v96 offset:0\n"
    "s_mov_b32 s57, s61\n"
    "s_add_u32 s59, s61, 65536\n"
    "s_mov_b64 s[44:45], s[48:49]\n"
    "s_mov_b32 s46, s50\n"
    "s_add_u32 m0, s62, 114688\n"
    "v_mfma_f32_16x16x32_bf16 v[132:135], v[32:35], a[140:143], v[132:135]\n"
    "ds_read_b64_tr_b16 v[2:3], v96 offset:2048\n"
    "buffer_load_dwordx4 v109, s[44:47], s57 offen lds\n"
    "v_mfma_f32_16x16x32_bf16 v[136:139], v[36:39], a[136:139], v[136:139]\n"
    "ds_read_b64_tr_b16 v[4:5], v99 offset:0\n"
    "v_mfma_f32_16x16x32_bf16 v[140:143], v[36:39], a[140:143], v[140:143]\n"
    "ds_read_b64_tr_b16 v[6:7], v99 offset:2048\n"
    "s_waitcnt lgkmcnt(12)\n"
    "v_mfma_f32_16x16x32_bf16 v[144:147], v[40:43], a[136:139], v[144:147]\n"
    "ds_read_b64_tr_b16 v[8:9], v102 offset:0\n"
    "s_add_u32 m0, s62, 118656\n"
    "v_mfma_f32_16x16x32_bf16 v[148:151], v[40:43], a[140:143], v[148:151]\n"
    "ds_read_b64_tr_b16 v[10:11], v102 offset:2048\n"
    "buffer_load_dwordx4 v109, s[44:47], s57 offen offset:128 lds\n"
    "v_mfma_f32_16x16x32_bf16 v[152:155], v[44:47], a[136:139], v[152:155]\n"
    "ds_read_b64_tr_b16 v[12:13], v105 offset:0\n"
    "s_add_u32 s57, s57, 131072\n"
    "v_mfma_f32_16x16x32_bf16 v[156:159], v[44:47], a[140:143], v[156:159]\n"
    "ds_read_b64_tr_b16 v[14:15], v105 offset:2048\n"
    "s_waitcnt lgkmcnt(12)\n"
    "v_mfma_f32_16x16x32_bf16 v[160:163], v[48:51], a[136:139], v[160:163]\n"
    "ds_read_b64_tr_b16 v[16:17], v96 offset:4096\n"
    "s_add_u32 m0, s62, 122880\n"
    "v_mfma_f32_16x16x32_bf16 v[164:167], v[48:51], a[140:143], v[164:167]\n"
    "ds_read_b64_tr_b16 v[18:19], v96 offset:6144\n"
    "buffer_load_dwordx4 v109, s[44:47], s59 offen lds\n"
    "v_mfma_f32_16x16x32_bf16 v[168:171], v[52:55], a[136:139], v[168:171]\n"
    "ds_read_b64_tr_b16 v[20:21], v99 offset:4096\n"
    "v_mfma_f32_16x16x32_bf16 v[172:175], v[52:55], a[140:143], v[172:175]\n"
    "ds_read_b64_tr_b16 v[22:23], v99 offset:6144\n"
    "s_waitcnt lgkmcnt(12)\n"
    "v_mfma_f32_16x16x32_bf16 v[176:179], v[56:59], a[136:139], v[176:179]\n"
    "ds_read_b64_tr_b16 v[24:25], v102 offset:4096\n"
    "s_add_u32 m0, s62, 126848\n"
    "v_mfma_f32_16x16x32_bf16 v[180:183], v[56:59], a[140:143], v[180:183]\n"
    "ds_read_b64_tr_b16 v[26:27], v102 offset:6144\n"
    "buffer_load_dwordx4 v109, s[44:47], s59 offen offset:128 lds\n"
    "v_mfma_f32_16x16x32_bf16 v[184:187], v[60:63], a[136:139], v[184:187]\n"
    "ds_read_b64_tr_b16 v[28:29], v105 offset:4096\n"
    "s_add_u32 s59, s59, 131072\n"
    "v_mfma_f32_16x16x32_bf16 v[188:191], v[60:63], a[140:143], v[188:191]\n"
    "ds_read_b64_tr_b16 v[30:31], v105 offset:6144\n"
    "s_waitcnt lgkmcnt(12)\n"
    "v_mfma_f32_16x16x32_bf16 v[128:131], v[0:3], a[0:3], v[128:131]\n"
    "ds_read_b64_tr_b16 v[32:33], v96 offset:8192\n"
    "v_mfma_f32_16x16x32_bf16 v[132:135], v[0:3], a[4:7], v[132:135]\n"
    "ds_read_b64_tr_b16 v[34:35], v96 offset:10240\n"
    "buffer_load_dwordx4 a[112:115], v108, s[40:43], s56 offen sc1\n"
    "v_mfma_f32_16x16x32_bf16 v[136:139], v[4:7], a[0:3], v[136:139]\n"
    "ds_read_b64_tr_b16 v[36:37], v99 offset:8192\n"
    "v_mfma_f32_16x16x32_bf16 v[140:143], v[4:7], a[4:7], v[140:143]\n"
    "ds_read_b64_tr_b16 v[38:39], v99 offset:10240\n"
    "s_waitcnt lgkmcnt(12)\n"
    "v_mfma_f32_16x16x32_bf16 v[144:147], v[8:11], a[0:3], v[144:147]\n"
    "ds_read_b64_tr_b16 v[40:41], v102 offset:8192\n"
    "v_mfma_f32_16x16x32_bf16 v[148:151], v[8:11], a[4:7], v[148:151]\n"
    "ds_read_b64_tr_b16 v[42:43], v102 offset:10240\n"
    "buffer_load_dwordx4 a[116:119], v111, s[40:43], s56 offen sc1\n"
    "v_mfma_f32_16x16x32_bf16 v[152:155], v[12:15], a[0:3], v[152:155]\n"
    "ds_read_b64_tr_b16 v[44:45], v105 offset:8192\n"
    "s_add_u32 s56, s56, 131072\n"
    "v_mfma_f32_16x16x32_bf16 v[156:159], v[12:15], a[4:7], v[156:159]\n"
    "ds_read_b64_tr_b16 v[46:47], v105 offset:10240\n"
    "s_waitcnt lgkmcnt(12)\n"
    "v_mfma_f32_16x16x32_bf16 v[160:163], v[16:19], a[0:3], v[160:163]\n"
    "ds_read_b64_tr_b16 v[48:49], v96 offset:12288\n"
    "v_mfma_f32_16x16x32_bf16 v[164:167], v[16:19], a[4:7], v[164:167]\n"
    "ds_read_b64_tr_b16 v[50:51], v96 offset:14336\n"
    "buffer_load_dwordx4 a[120:123], v108, s[40:43], s56 offen sc1\n"
    "v_mfma_f32_16x16x32_bf16 v[168:171], v[20:23], a[0:3], v[168:171]\n"
    "ds_read_b64_tr_b16 v[52:53], v99 offset:12288\n"
    "v_mfma_f32_16x16x32_bf16 v[172:175], v[20:23], a[4:7], v[172:175]\n"
    "ds_read_b64_tr_b16 v[54:55], v99 offset:14336\n"
    "s_waitcnt lgkmcnt(12)\n"
    "v_mfma_f32_16x16x32_bf16 v[176:179], v[24:27], a[0:3], v[176:179]\n"
    "ds_read_b64_tr_b16 v[56:57], v102 offset:12288\n"
    "v_mfma_f32_16x16x32_bf16 v[180:183], v[24:27], a[4:7], v[180:183]\n"
    "ds_read_b64_tr_b16 v[58:59], v102 offset:14336\n"
    "buffer_load_dwordx4 a[124:127], v111, s[40:43], s56 offen sc1\n"
    "v_mfma_f32_16x16x32_bf16 v[184:187], v[28:31], a[0:3], v[184:187]\n"
    "ds_read_b64_tr_b16 v[60:61], v105 offset:12288\n"
    "s_add_u32 s56, s56, 131072\n"
    "v_mfma_f32_16x16x32_bf16 v[188:191], v[28:31], a[4:7], v[188:191]\n"
    "ds_read_b64_tr_b16 v[62:63], v105 offset:14336\n"
    "s_mov_b32 s58, 0\n"
    "LOOPk2_%=:\n"
    "s_waitcnt vmcnt(48) lgkmcnt(12)\n"
    "s_barrier\n"
    "v_mfma_f32_16x16x32_bf16 v[128:131], v[32:35], a[8:11], v[128:131]\n"
    "ds_read_b64_tr_b16 v[0:1], v96 offset:16384\n"
    "s_add_u32 m0, s62, 131072\n"
    "v_mfma_f32_16x16x32_bf16 v[132:135], v[32:35], a[12:15], v[132:135]\n"
    "ds_read_b64_tr_b16 v[2:3], v96 offset:18432\n"
    "buffer_load_dwordx4 v109, s[44:47], s57 offen lds\n"
    "v_mfma_f32_16x16x32_bf16 v[136:139], v[36:39], a[8:11], v[136:139]\n"
    "ds_read_b64_tr_b16 v[4:5], v99 offset:16384\n"
    "v_mfma_f32_16x16x32_bf16 v[140:143], v[36:39], a[12:15], v[140:143]\n"
    "ds_read_b64_tr_b16 v[6:7], v99 offset:18432\n"
    "s_waitcnt lgkmcnt(12)\n"
    "v_mfma_f32_16x16x32_bf16 v[144:147], v[40:43], a[8:11], v[144:147]\n"
    "ds_read_b64_tr_b16 v[8:9], v102 offset:16384\n"
    "s_add_u32 m0, s62, 135040\n"
    "v_mfma_f32_16x16x32_bf16 v[148:151], v[40:43], a[12:15], v[148:151]\n"
    "ds_read_b64_tr_b16 v[10:11], v102 offset:18432\n"
    "buffer_load_dwordx4 v109, s[44:47], s57 offen offset:128 lds\n"
    "v_mfma_f32_16x16x32_bf16 v[152:155], v[44:47], a[8:11], v[152:155]\n"
    "ds_read_b64_tr_b16 v[12:13], v105 offset:16384\n"
    "s_add_u32 s57, s57, 131072\n"
    "v_mfma_f32_16x16x32_bf16 v[156:159], v[44:47], a[12:15], v[156:159]\n"
    "ds_read_b64_tr_b16 v[14:15], v105 offset:18432\n"
    "s_waitcnt lgkmcnt(12)\n"
    "v_mfma_f32_16x16x32_bf16 v[160:163], v[48:51], a[8:11], v[160:163]\n"
    "ds_read_b64_tr_b16 v[16:17], v96 offset:20480\n"
    "s_add_u32 m0, s62, 139264\n"
    "v_mfma_f32_16x16x32_bf16 v[164:167], v[48:51], a[12:15], v[164:167]\n"
    "ds_read_b64_tr_b16 v[18:19], v96 offset:22528\n"
    "buffer_load_dwordx4 v109, s[44:47], s59 offen lds\n"
    "v_mfma_f32_16x16x32_bf16 v[168:171], v[52:55], a[8:11], v[168:171]\n"
    "ds_read_b64_tr_b16 v[20:21], v99 offset:20480\n"
    "v_mfma_f32_16x16x32_bf16 v[172:175], v[52:55], a[12:15], v[172:175]\n"
    "ds_read_b64_tr_b16 v[22:23], v99 offset:22528\n"
    "s_waitcnt lgkmcnt(12)\n"
    "v_mfma_f32_16x16x32_bf16 v[176:179], v[56:59], a[8:11], v[176:179]\n"
    "ds_read_b64_tr_b16 v[24:25], v102 offset:20480\n"
    "s_add_u32 m0, s62, 143232\n"
    "v_mfma_f32_16x16x32_bf16 v[180:183], v[56:59], a[12:15], v[180:183]\n"
    "ds_read_b64_tr_b16 v[26:27], v102 offset:22528\n"
    "buffer_load_dwordx4 v109, s[44:47], s59 offen offset:128 lds\n"
    "v_mfma_f32_16x16x32_bf16 v[184:187], v[60:63], a[8:11], v[184:187]\n"
    "ds_read_b64_tr_b16 v[28:29], v105 offset:20480\n"
    "s_add_u32 s59, s59, 131072\n"
    "v_mfma_f32_16x16x32_bf16 v[188:191], v[60:63], a[12:15], v[188:191]\n"
    "ds_read_b64_tr_b16 v[30:31], v105 offset:22528\n"
    "s_waitcnt lgkmcnt(12)\n"
    "v_mfma_f32_16x16x32_bf16 v[128:131], v[0:3], a[16:19], v[128:131]\n"
    "ds_read_b64_tr_b16 v[32:33], v96 offset:24576\n"
    "v_mfma_f32_16x16x32_bf16 v[132:135], v[0:3], a[20:23], v[132:135]\n"
    "ds_read_b64_tr_b16 v[34:35], v96 offset:26624\n"
    "buffer_load_dwordx4 a[128:131], v108, s[40:43], s56 offen sc1\n"
    "v_mfma_f32_16x16x32_bf16 v[136:139], v[4:7], a[16:19], v[136:139]\n"
    "ds_read_b64_tr_b16 v[36:37], v99 offset:24576\n"
    "v_mfma_f32_16x16x32_bf16 v[140:143], v[4:7], a[20:23], v[140:143]\n"
    "ds_read_b64_tr_b16 v[38:39], v99 offset:26624\n"
    "s_waitcnt lgkmcnt(12)\n"
    "v_mfma_f32_16x16x32_bf16 v[144:147], v[8:11], a[16:19], v[144:147]\n"
    "ds_read_b64_tr_b16 v[40:41], v102 offset:24576\n"
    "v_mfma_f32_16x16x32_bf16 v[148:151], v[8:11], a[20:23], v[148:151]\n"
    "ds_read_b64_tr_b16 v[42:43], v102 offset:26624\n"
    "buffer_load_dwordx4 a[132:135], v111, s[40:43], s56 offen sc1\n"
    "v_mfma_f32_16x16x32_bf16 v[152:155], v[12:15], a[16:19], v[152:155]\n"
    "ds_read_b64_tr_b16 v[44:45], v105 offset:24576\n"
    "s_add_u32 s56, s56, 131072\n"
    "v_mfma_f32_16x16x32_bf16 v[156:159], v[12:15], a[20:23], v[156:159]\n"
    "ds_read_b64_tr_b16 v[46:47], v105 offset:26624\n"
    "s_waitcnt lgkmcnt(12)\n"
    "v_mfma_f32_16x16x32_bf16 v[160:163], v[16:19], a[16:19], v[160:163]\n"
    "ds_read_b64_tr_b16 v[48:49], v96 offset:28672\n"
    "v_mfma_f32_16x16x32_bf16 v[164:167], v[16:19], a[20:23], v[164:167]\n"
    "ds_read_b64_tr_b16 v[50:51], v96 offset:30720\n"
    "buffer_load_dwordx4 a[136:139], v108, s[40:43], s56 offen sc1\n"
    "v_mfma_f32_16x16x32_bf16 v[168:171], v[20:23], a[16:19], v[168:171]\n"
    "ds_read_b64_tr_b16 v[52:53], v99 offset:28672\n"
    "v_mfma_f32_16x16x32_bf16 v[172:175], v[20:23], a[20:23], v[172:175]\n"
    "ds_read_b64_tr_b16 v[54:55], v99 offset:30720\n"
    "s_waitcnt lgkmcnt(12)\n"
    "v_mfma_f32_16x16x32_bf16 v[176:179], v[24:27], a[16:19], v[176:179]\n"
    "ds_read_b64_tr_b16 v[56:57], v102 offset:28672\n"
    "v_mfma_f32_16x16x32_bf16 v[180:183], v[24:27], a[20:23], v[180:183]\n"
    "ds_read_b64_tr_b16 v[58:59], v102 offset:30720\n"
    "buffer_load_dwordx4 a[140:143], v111, s[40:43], s56 offen sc1\n"
    "v_mfma_f32_16x16x32_bf16 v[184:187], v[28:31], a[16:19], v[184:187]\n"
    "ds_read_b64_tr_b16 v[60:61], v105 offset:28672\n"
    "s_add_u32 s56, s56, 131072\n"
    "v_mfma_f32_16x16x32_bf16 v[188:191], v[28:31], a[20:23], v[188:191]\n"
    "ds_read_b64_tr_b16 v[62:63], v105 offset:30720\n"
    "s_waitcnt vmcnt(48) lgkmcnt(12)\n"
    "s_barrier\n"
    "v_mfma_f32_16x16x32_bf16 v[128:131], v[32:35], a[24:27], v[128:131]\n"
    "ds_read_b64_tr_b16 v[0:1], v96 offset:32768\n"
    "s_add_u32 m0, s62, 0\n"
    "v_mfma_f32_16x16x32_bf16 v[132:135], v[32:35], a[28:31], v[132:135]\n"
    "ds_read_b64_tr_b16 v[2:3], v96 offset:34816\n"
    "buffer_load_dwordx4 v109, s[44:47], s57 offen lds\n"
    "v_mfma_f32_16x16x32_bf16 v[136:139], v[36:39], a[24:27], v[136:139]\n"
    "ds_read_b64_tr_b16 v[4:5], v99 offset:32768\n"
    "v_mfma_f32_16x16x32_bf16 v[140:143], v[36:39], a[28:31], v[140:143]\n"
    "ds_read_b64_tr_b16 v[6:7], v99 offset:34816\n"
    "s_waitcnt lgkmcnt(12)\n"
    "v_mfma_f32_16x16x32_bf16 v[144:147], v[40:43], a[24:27], v[144:147]\n"
    "ds_read_b64_tr_b16 v[8:9], v102 offset:32768\n"
    "s_add_u32 m0, s62, 3968\n"
    "v_mfma_f32_16x16x32_bf16 v[148:151], v[40:43], a[28:31], v[148:151]\n"
    "ds_read_b64_tr_b16 v[10:11], v102 offset:34816\n"
    "buffer_load_dwordx4 v109, s[44:47], s57 offen offset:128 lds\n"
    "v_mfma_f32_16x16x32_bf16 v[152:155], v[44:47], a[24:27], v[152:155]\n"
    "ds_read_b64_tr_b16 v[12:13], v105 offset:32768\n"
    "s_add_u32 s57, s57, 131072\n"
    "v_mfma_f32_16x16x32_bf16 v[156:159], v[44:47], a[28:31], v[156:159]\n"
    "ds_read_b64_tr_b16 v[14:15], v105 offset:34816\n"
    "s_waitcnt lgkmcnt(12)\n"
    "v_mfma_f32_16x16x32_bf16 v[160:163], v[48:51], a[24:27], v[160:163]\n"
    "ds_read_b64_tr_b16 v[16:17], v96 offset:36864\n"
    "s_add_u32 m0, s62, 8192\n"
    "v_mfma_f32_16x16x32_bf16 v[164:167], v[48:51], a[28:31], v[164:167]\n"
    "ds_read_b64_tr_b16 v[18:19], v96 offset:38912\n"
    "buffer_load_dwordx4 v109, s[44:47], s59 offen lds\n"
    "v_mfma_f32_16x16x32_bf16 v[168:171], v[52:55], a[24:27], v[168:171]\n"
    "ds_read_b64_tr_b16 v[20:21], v99 offset:36864\n"
    "v_mfma_f32_16x16x32_bf16 v[172:175], v[52:55], a[28:31], v[172:175]\n"
    "ds_read_b64_tr_b16 v[22:23], v99 offset:38912\n"
    "s_waitcnt lgkmcnt(12)\n"
    "v_mfma_f32_16x16x32_bf16 v[176:179], v[56:59], a[24:27], v[176:179]\n"
    "ds_read_b64_tr_b16 v[24:25], v102 offset:36864\n"
    "s_add_u32 m0, s62, 12160\n"
    "v_mfma_f32_16x16x32_bf16 v[180:183], v[56:59], a[28:31], v[180:183]\n"
    "ds_read_b64_tr_b16 v[26:27], v102 offset:38912\n"
    "buffer_load_dwordx4 v109, s[44:47], s59 offen offset:128 lds\n"
    "v_mfma_f32_16x16x32_bf16 v[184:187], v[60:63], a[24:27], v[184:187]\n"
    "ds_read_b64_tr_b16 v[28:29], v105 offset:36864\n"
    "s_add_u32 s59, s59, 131072\n"
    "v_mfma_f32_16x16x32_bf16 v[188:191], v[60:63], a[28:31], v[188:191]\n"
    "ds_read_b64_tr_b16 v[30:31], v105 offset:38912\n"
    "s_waitcnt lgkmcnt(12)\n"
    "v_mfma_f32_16x16x32_bf16 v[128:131], v[0:3], a[32:35], v[128:131]\n"
    "ds_read_b64_tr_b16 v[32:33], v96 offset:40960\n"
    "v_mfma_f32_16x16x32_bf16 v[132:135], v[0:3], a[36:39], v[132:135]\n"
    "ds_read_b64_tr_b16 v[34:35], v96 offset:43008\n"
    "buffer_load_dwordx4 a[0:3], v108, s[40:43], s56 offen sc1\n"
    "v_mfma_f32_16x16x32_bf16 v[136:139], v[4:7], a[32:35], v[136:139]\n"
    "ds_read_b64_tr_b16 v[36:37], v99 offset:40960\n"
    "v_mfma_f32_16x16x32_bf16 v[140:143], v[4:7], a[36:39], v[140:143]\n"
    "ds_read_b64_tr_b16 v[38:39], v99 offset:43008\n"
    "s_waitcnt lgkmcnt(12)\n"
    "v_mfma_f32_16x16x32_bf16 v[144:147], v[8:11], a[32:35], v[144:147]\n"
    "ds_read_b64_tr_b16 v[40:41], v102 offset:40960\n"
    "v_mfma_f32_16x16x32_bf16 v[148:151], v[8:11], a[36:39], v[148:151]\n"
    "ds_read_b64_tr_b16 v[42:43], v102 offset:43008\n"
    "buffer_load_dwordx4 a[4:7], v111, s[40:43], s56 offen sc1\n"
    "v_mfma_f32_16x16x32_bf16 v[152:155], v[12:15], a[32:35], v[152:155]\n"
    "ds_read_b64_tr_b16 v[44:45], v105 offset:40960\n"
    "s_add_u32 s56, s56, 131072\n"
    "v_mfma_f32_16x16x32_bf16 v[156:159], v[12:15], a[36:39], v[156:159]\n"
    "ds_read_b64_tr_b16 v[46:47], v105 offset:43008\n"
    "s_waitcnt lgkmcnt(12)\n"
    "v_mfma_f32_16x16x32_bf16 v[160:163], v[16:19], a[32:35], v[160:163]\n"
    "ds_read_b64_tr_b16 v[48:49], v96 offset:45056\n"
    "v_mfma_f32_16x16x32_bf16 v[164:167], v[16:19], a[36:39], v[164:167]\n"
    "ds_read_b64_tr_b16 v[50:51], v96 offset:47104\n"
    "buffer_load_dwordx4 a[8:11], v108, s[40:43], s56 offen sc1\n"
    "v_mfma_f32_16x16x32_bf16 v[168:171], v[20:23], a[32:35], v[168:171]\n"
    "ds_read_b64_tr_b16 v[52:53], v99 offset:45056\n"
    "v_mfma_f32_16x16x32_bf16 v[172:175], v[20:23], a[36:39], v[172:175]\n"
    "ds_read_b64_tr_b16 v[54:55], v99 offset:47104\n"
    "s_waitcnt lgkmcnt(12)\n"
    "v_mfma_f32_16x16x32_bf16 v[176:179], v[24:27], a[32:35], v[176:179]\n"
    "ds_read_b64_tr_b16 v[56:57], v102 offset:45056\n"
    "v_mfma_f32_16x16x32_bf16 v[180:183], v[24:27], a[36:39], v[180:183]\n"
    "ds_read_b64_tr_b16 v[58:59], v102 offset:47104\n"
    "buffer_load_dwordx4 a[12:15], v111, s[40:43], s56 offen sc1\n"
    "v_mfma_f32_16x16x32_bf16 v[184:187], v[28:31], a[32:35], v[184:187]\n"
    "ds_read_b64_tr_b16 v[60:61], v105 offset:45056\n"
    "s_add_u32 s56, s56, 131072\n"
    "v_mfma_f32_16x16x32_bf16 v[188:191], v[28:31], a[36:39], v[188:191]\n"
    "ds_read_b64_tr_b16 v[62:63], v105 offset:47104\n"
    "s_waitcnt vmcnt(48) lgkmcnt(12)\n"
    "s_barrier\n"
    "v_mfma_f32_16x16x32_bf16 v[128:131], v[32:35], a[40:43], v[128:131]\n"
    "ds_read_b64_tr_b16 v[0:1], v97 offset:0\n"
    "s_add_u32 m0, s62, 16384\n"
    "v_mfma_f32_16x16x32_bf16 v[132:135], v[32:35], a[44:47], v[132:135]\n"
    "ds_read_b64_tr_b16 v[2:3], v97 offset:2048\n"
    "buffer_load_dwordx4 v109, s[44:47], s57 offen lds\n"
    "v_mfma_f32_16x16x32_bf16 v[136:139], v[36:39], a[40:43], v[136:139]\n"
    "ds_read_b64_tr_b16 v[4:5], v100 offset:0\n"
    "v_mfma_f32_16x16x32_bf16 v[140:143], v[36:39], a[44:47], v[140:143]\n"
    "ds_read_b64_tr_b16 v[6:7], v100 offset:2048\n"
    "s_waitcnt lgkmcnt(12)\n"
    "v_mfma_f32_16x16x32_bf16 v[144:147], v[40:43], a[40:43], v[144:147]\n"
    "ds_read_b64_tr_b16 v[8:9], v103 offset:0\n"
    "s_add_u32 m0, s62, 20352\n"
    "v_mfma_f32_16x16x32_bf16 v[148:151], v[40:43], a[44:47], v[148:151]\n"
    "ds_read_b64_tr_b16 v[10:11], v103 offset:2048\n"
    "buffer_load_dwordx4 v109, s[44:47], s57 offen offset:128 lds\n"
    "v_mfma_f32_16x16x32_bf16 v[152:155], v[44:47], a[40:43], v[152:155]\n"
    "ds_read_b64_tr_b16 v[12:13], v106 offset:0\n"
    "s_add_u32 s57, s57, 131072\n"
    "v_mfma_f32_16x16x32_bf16 v[156:159], v[44:47], a[44:47], v[156:159]\n"
    "ds_read_b64_tr_b16 v[14:15], v106 offset:2048\n"
    "s_waitcnt lgkmcnt(12)\n"
    "v_mfma_f32_16x16x32_bf16 v[160:163], v[48:51], a[40:43], v[160:163]\n"
    "ds_read_b64_tr_b16 v[16:17], v97 offset:4096\n"
    "s_add_u32 m0, s62, 24576\n"
    "v_mfma_f32_16x16x32_bf16 v[164:167], v[48:51], a[44:47], v[164:167]\n"
    "ds_read_b64_tr_b16 v[18:19], v97 offset:6144\n"
    "buffer_load_dwordx4 v109, s[44:47], s59 offen lds\n"
    "v_mfma_f32_16x16x32_bf16 v[168:171], v[52:55], a[40:43], v[168:171]\n"
    "ds_read_b64_tr_b16 v[20:21], v100 offset:4096\n"
    "v_mfma_f32_16x16x32_bf16 v[172:175], v[52:55], a[44:47], v[172:175]\n"
    "ds_read_b64_tr_b16 v[22:23], v100 offset:6144\n"
    "s_waitcnt lgkmcnt(12)\n"
    "v_mfma_f32_16x16x32_bf16 v[176:179], v[56:59], a[40:43], v[176:179]\n"
    "ds_read_b64_tr_b16 v[24:25], v103 offset:4096\n"
    "s_add_u32 m0, s62, 28544\n"
    "v_mfma_f32_16x16x32_bf16 v[180:183], v[56:59], a[44:47], v[180:183]\n"
    "ds_read_b64_tr_b16 v[26:27], v103 offset:6144\n"
    "buffer_load_dwordx4 v109, s[44:47], s59 offen offset:128 lds\n"
    "v_mfma_f32_16x16x32_bf16 v[184:187], v[60:63], a[40:43], v[184:187]\n"
    "ds_read_b64_tr_b16 v[28:29], v106 offset:4096\n"
    "s_add_u32 s59, s59, 131072\n"
    "v_mfma_f32_16x16x32_bf16 v[188:191], v[60:63], a[44:47], v[188:191]\n"
    "ds_read_b64_tr_b16 v[30:31], v106 offset:6144\n"
    "s_waitcnt lgkmcnt(12)\n"
    "v_mfma_f32_16x16x32_bf16 v[128:131], v[0:3], a[48:51], v[128:131]\n"
    "ds_read_b64_tr_b16 v[32:33], v97 offset:8192\n"
    "v_mfma_f32_16x16x32_bf16 v[132:135], v[0:3], a[52:55], v[132:135]\n"
    "ds_read_b64_tr_b16 v[34:35], v97 offset:10240\n"
    "buffer_load_dwordx4 a[16:19], v108, s[40:43], s56 offen sc1\n"
    "v_mfma_f32_16x16x32_bf16 v[136:139], v[4:7], a[48:51], v[136:139]\n"
    "ds_read_b64_tr_b16 v[36:37], v100 offset:8192\n"
    "v_mfma_f32_16x16x32_bf16 v[140:143], v[4:7], a[52:55], v[140:143]\n"
    "ds_read_b64_tr_b16 v[38:39], v100 offset:10240\n"
    "s_waitcnt lgkmcnt(12)\n"
    "v_mfma_f32_16x16x32_bf16 v[144:147], v[8:11], a[48:51], v[144:147]\n"
    "ds_read_b64_tr_b16 v[40:41], v103 offset:8192\n"
    "v_mfma_f32_16x16x32_bf16 v[148:151], v[8:11], a[52:55], v[148:151]\n"
    "ds_read_b64_tr_b16 v[42:43], v103 offset:10240\n"
    "buffer_load_dwordx4 a[20:23], v111, s[40:43], s56 offen sc1\n"
    "v_mfma_f32_16x16x32_bf16 v[152:155], v[12:15], a[48:51], v[152:155]\n"
    "ds_read_b64_tr_b16 v[44:45], v106 offset:8192\n"
    "s_add_u32 s56, s56, 131072\n"
    "v_mfma_f32_16x16x32_bf16 v[156:159], v[12:15], a[52:55], v[156:159]\n"
    "ds_read_b64_tr_b16 v[46:47], v106 offset:10240\n"
    "s_waitcnt lgkmcnt(12)\n"
    "v_mfma_f32_16x16x32_bf16 v[160:163], v[16:19], a[48:51], v[160:163]\n"
    "ds_read_b64_tr_b16 v[48:49], v97 offset:12288\n"
    "v_mfma_f32_16x16x32_bf16 v[164:167], v[16:19], a[52:55], v[164:167]\n"
    "ds_read_b64_tr_b16 v[50:51], v97 offset:14336\n"
    "buffer_load_dwordx4 a[24:27], v108, s[40:43], s56 offen sc1\n"
    "v_mfma_f32_16x16x32_bf16 v[168:171], v[20:23], a[48:51], v[168:171]\n"
    "ds_read_b64_tr_b16 v[52:53], v100 offset:12288\n"
    "v_mfma_f32_16x16x32_bf16 v[172:175], v[20:23], a[52:55], v[172:175]\n"
    "ds_read_b64_tr_b16 v[54:55], v100 offset:14336\n"
    "s_waitcnt lgkmcnt(12)\n"
    "v_mfma_f32_16x16x32_bf16 v[176:179], v[24:27], a[48:51], v[176:179]\n"
    "ds_read_b64_tr_b16 v[56:57], v103 offset:12288\n"
    "v_mfma_f32_16x16x32_bf16 v[180:183], v[24:27], a[52:55], v[180:183]\n"
    "ds_read_b64_tr_b16 v[58:59], v103 offset:14336\n"
    "buffer_load_dwordx4 a[28:31], v111, s[40:43], s56 offen sc1\n"
    "v_mfma_f32_16x16x32_bf16 v[184:187], v[28:31], a[48:51], v[184:187]\n"
    "ds_read_b64_tr_b16 v[60:61], v106 offset:12288\n"
    "s_add_u32 s56, s56, 131072\n"
    "v_mfma_f32_16x16x32_bf16 v[188:191], v[28:31], a[52:55], v[188:191]\n"
    "ds_read_b64_tr_b16 v[62:63], v106 offset:14336\n"
    "s_waitcnt vmcnt(48) lgkmcnt(12)\n"
    "s_barrier\n"
    "v_mfma_f32_16x16x32_bf16 v[128:131], v[32:35], a[56:59], v[128:131]\n"
    "ds_read_b64_tr_b16 v[0:1], v97 offset:16384\n"
    "s_add_u32 m0, s62, 32768\n"
    "v_mfma_f32_16x16x32_bf16 v[132:135], v[32:35], a[60:63], v[132:135]\n"
    "ds_read_b64_tr_b16 v[2:3], v97 offset:18432\n"
    "buffer_load_dwordx4 v109, s[44:47], s57 offen lds\n"
    "v_mfma_f32_16x16x32_bf16 v[136:139], v[36:39], a[56:59], v[136:139]\n"
    "ds_read_b64_tr_b16 v[4:5], v100 offset:16384\n"
    "v_mfma_f32_16x16x32_bf16 v[140:143], v[36:39], a[60:63], v[140:143]\n"
    "ds_read_b64_tr_b16 v[6:7], v100 offset:18432\n"
    "s_waitcnt lgkmcnt(12)\n"
    "v_mfma_f32_16x16x32_bf16 v[144:147], v[40:43], a[56:59], v[144:147]\n"
    "ds_read_b64_tr_b16 v[8:9], v103 offset:16384\n"
    "s_add_u32 m0, s62, 36736\n"
    "v_mfma_f32_16x16x32_bf16 v[148:151], v[40:43], a[60:63], v[148:151]\n"
    "ds_read_b64_tr_b16 v[10:11], v103 offset:18432\n"
    "buffer_load_dwordx4 v109, s[44:47], s57 offen offset:128 lds\n"
    "v_mfma_f32_16x16x32_bf16 v[152:155], v[44:47], a[56:59], v[152:155]\n"
    "ds_read_b64_tr_b16 v[12:13], v106 offset:16384\n"
    "s_add_u32 s57, s57, 131072\n"
    "v_mfma_f32_16x16x32_bf16 v[156:159], v[44:47], a[60:63], v[156:159]\n"
    "ds_read_b64_tr_b16 v[14:15], v106 offset:18432\n"
    "s_waitcnt lgkmcnt(12)\n"
    "v_mfma_f32_16x16x32_bf16 v[160:163], v[48:51], a[56:59], v[160:163]\n"
    "ds_read_b64_tr_b16 v[16:17], v97 offset:20480\n"
    "s_add_u32 m0, s62, 40960\n"
    "v_mfma_f32_16x16x32_bf16 v[164:167], v[48:51], a[60:63], v[164:167]\n"
    "ds_read_b64_tr_b16 v[18:19], v97 offset:22528\n"
    "buffer_load_dwordx4 v109, s[44:47], s59 offen lds\n"
    "v_mfma_f32_16x16x32_bf16 v[168:171], v[52:55], a[56:59], v[168:171]\n"
    "ds_read_b64_tr_b16 v[20:21], v100 offset:20480\n"
    "v_mfma_f32_16x16x32_bf16 v[172:175], v[52:55], a[60:63], v[172:175]\n"
    "ds_read_b64_tr_b16 v[22:23], v100 offset:22528\n"
    "s_waitcnt lgkmcnt(12)\n"
    "v_mfma_f32_16x16x32_bf16 v[176:179], v[56:59], a[56:59], v[176:179]\n"
    "ds_read_b64_tr_b16 v[24:25], v103 offset:20480\n"
    "s_add_u32 m0, s62, 44928\n"
    "v_mfma_f32_16x16x32_bf16 v[180:183], v[56:59], a[60:63], v[180:183]\n"
    "ds_read_b64_tr_b16 v[26:27], v103 offset:22528\n"
    "buffer_load_dwordx4 v109, s[44:47], s59 offen offset:128 lds\n"
    "v_mfma_f32_16x16x32_bf16 v[184:187], v[60:63], a[56:59], v[184:187]\n"
    "ds_read_b64_tr_b16 v[28:29], v106 offset:20480\n"
    "s_add_u32 s59, s59, 131072\n"
    "v_mfma_f32_16x16x32_bf16 v[188:191], v[60:63], a[60:63], v[188:191]\n"
    "ds_read_b64_tr_b16 v[30:31], v106 offset:22528\n"
    "s_waitcnt lgkmcnt(12)\n"
    "v_mfma_f32_16x16x32_bf16 v[128:131], v[0:3], a[64:67], v[128:131]\n"
    "ds_read_b64_tr_b16 v[32:33], v97 offset:24576\n"
    "v_mfma_f32_16x16x32_bf16 v[132:135], v[0:3], a[68:71], v[132:135]\n"
    "ds_read_b64_tr_b16 v[34:35], v97 offset:26624\n"
    "buffer_load_dwordx4 a[32:35], v108, s[40:43], s56 offen sc1\n"
    "v_mfma_f32_16x16x32_bf16 v[136:139], v[4:7], a[64:67], v[136:139]\n"
    "ds_read_b64_tr_b16 v[36:37], v100 offset:24576\n"
    "v_mfma_f32_16x16x32_bf16 v[140:143], v[4:7], a[68:71], v[140:143]\n"
    "ds_read_b64_tr_b16 v[38:39], v100 offset:26624\n"
    "s_waitcnt lgkmcnt(12)\n"
    "v_mfma_f32_16x16x32_bf16 v[144:147], v[8:11], a[64:67], v[144:147]\n"
    "ds_read_b64_tr_b16 v[40:41], v103 offset:24576\n"
    "v_mfma_f32_16x16x32_bf16 v[148:151], v[8:11], a[68:71], v[148:151]\n"
    "ds_read_b64_tr_b16 v[42:43], v103 offset:26624\n"
    "buffer_load_dwordx4 a[36:39], v111, s[40:43], s56 offen sc1\n"
    "v_mfma_f32_16x16x32_bf16 v[152:155], v[12:15], a[64:67], v[152:155]\n"
    "ds_read_b64_tr_b16 v[44:45], v106 offset:24576\n"
    "s_add_u32 s56, s56, 131072\n"
    "v_mfma_f32_16x16x32_bf16 v[156:159], v[12:15], a[68:71], v[156:159]\n"
    "ds_read_b64_tr_b16 v[46:47], v106 offset:26624\n"
    "s_waitcnt lgkmcnt(12)\n"
    "v_mfma_f32_16x16x32_bf16 v[160:163], v[16:19], a[64:67], v[160:163]\n"
    "ds_read_b64_tr_b16 v[48:49], v97 offset:28672\n"
    "v_mfma_f32_16x16x32_bf16 v[164:167], v[16:19], a[68:71], v[164:167]\n"
    "ds_read_b64_tr_b16 v[50:51], v97 offset:30720\n"
    "buffer_load_dwordx4 a[40:43], v108, s[40:43], s56 offen sc1\n"
    "v_mfma_f32_16x16x32_bf16 v[168:171], v[20:23], a[64:67], v[168:171]\n"
    "ds_read_b64_tr_b16 v[52:53], v100 offset:28672\n"
    "v_mfma_f32_16x16x32_bf16 v[172:175], v[20:23], a[68:71], v[172:175]\n"
    "ds_read_b64_tr_b16 v[54:55], v100 offset:30720\n"
    "s_waitcnt lgkmcnt(12)\n"
    "v_mfma_f32_16x16x32_bf16 v[176:179], v[24:27], a[64:67], v[176:179]\n"
    "ds_read_b64_tr_b16 v[56:57], v103 offset:28672\n"
    "v_mfma_f32_16x16x32_bf16 v[180:183], v[24:27], a[68:71], v[180:183]\n"
    "ds_read_b64_tr_b16 v[58:59], v103 offset:30720\n"
    "buffer_load_dwordx4 a[44:47], v111, s[40:43], s56 offen sc1\n"
    "v_mfma_f32_16x16x32_bf16 v[184:187], v[28:31], a[64:67], v[184:187]\n"
    "ds_read_b64_tr_b16 v[60:61], v106 offset:28672\n"
    "s_add_u32 s56, s56, 131072\n"
    "v_mfma_f32_16x16x32_bf16 v[188:191], v[28:31], a[68:71], v[188:191]\n"
    "ds_read_b64_tr_b16 v[62:63], v106 offset:30720\n"
    "s_waitcnt vmcnt(48) lgkmcnt(12)\n"
    "s_barrier\n"
    "v_mfma_f32_16x16x32_bf16 v[128:131], v[32:35], a[72:75], v[128:131]\n"
    "ds_read_b64_tr_b16 v[0:1], v97 offset:32768\n"
    "s_add_u32 m0, s62, 49152\n"
    "v_mfma_f32_16x16x32_bf16 v[132:135], v[32:35], a[76:79], v[132:135]\n"
    "ds_read_b64_tr_b16 v[2:3], v97 offset:34816\n"
    "buffer_load_dwordx4 v109, s[44:47], s57 offen lds\n"
    "v_mfma_f32_16x16x32_bf16 v[136:139], v[36:39], a[72:75], v[136:139]\n"
    "ds_read_b64_tr_b16 v[4:5], v100 offset:32768\n"
    "v_mfma_f32_16x16x32_bf16 v[140:143], v[36:39], a[76:79], v[140:143]\n"
    "ds_read_b64_tr_b16 v[6:7], v100 offset:34816\n"
    "s_waitcnt lgkmcnt(12)\n"
    "v_mfma_f32_16x16x32_bf16 v[144:147], v[40:43], a[72:75], v[144:147]\n"
    "ds_read_b64_tr_b16 v[8:9], v103 offset:32768\n"
    "s_add_u32 m0, s62, 53120\n"
    "v_mfma_f32_16x16x32_bf16 v[148:151], v[40:43], a[76:79], v[148:151]\n"
    "ds_read_b64_tr_b16 v[10:11], v103 offset:34816\n"
    "buffer_load_dwordx4 v109, s[44:47], s57 offen offset:128 lds\n"
    "v_mfma_f32_16x16x32_bf16 v[152:155], v[44:47], a[72:75], v[152:155]\n"
    "ds_read_b64_tr_b16 v[12:13], v106 offset:32768\n"
    "s_add_u32 s57, s57, 131072\n"
    "v_mfma_f32_16x16x32_bf16 v[156:159], v[44:47], a[76:79], v[156:159]\n"
    "ds_read_b64_tr_b16 v[14:15], v106 offset:34816\n"
    "s_waitcnt lgkmcnt(12)\n"
    "v_mfma_f32_16x16x32_bf16 v[160:163], v[48:51], a[72:75], v[160:163]\n"
    "ds_read_b64_tr_b16 v[16:17], v97 offset:36864\n"
    "s_add_u32 m0, s62, 57344\n"
    "v_mfma_f32_16x16x32_bf16 v[164:167], v[48:51], a[76:79], v[164:167]\n"
    "ds_read_b64_tr_b16 v[18:19], v97 offset:38912\n"
    "buffer_load_dwordx4 v109, s[44:47], s59 offen lds\n"
    "v_mfma_f32_16x16x32_bf16 v[168:171], v[52:55], a[72:75], v[168:171]\n"
    "ds_read_b64_tr_b16 v[20:21], v100 offset:36864\n"
    "v_mfma_f32_16x16x32_bf16 v[172:175], v[52:55], a[76:79], v[172:175]\n"
    "ds_read_b64_tr_b16 v[22:23], v100 offset:38912\n"
    "s_waitcnt lgkmcnt(12)\n"
    "v_mfma_f32_16x16x32_bf16 v[176:179], v[56:59], a[72:75], v[176:179]\n"
    "ds_read_b64_tr_b16 v[24:25], v103 offset:36864\n"
    "s_add_u32 m0, s62, 61312\n"
    "v_mfma_f32_16x16x32_bf16 v[180:183], v[56:59], a[76:79], v[180:183]\n"
    "ds_read_b64_tr_b16 v[26:27], v103 offset:38912\n"
    "buffer_load_dwordx4 v109, s[44:47], s59 offen offset:128 lds\n"
    "v_mfma_f32_16x16x32_bf16 v[184:187], v[60:63], a[72:75], v[184:187]\n"
    "ds_read_b64_tr_b16 v[28:29], v106 offset:36864\n"
    "s_add_u32 s59, s59, 131072\n"
    "v_mfma_f32_16x16x32_bf16 v[188:191], v[60:63], a[76:79], v[188:191]\n"
    "ds_read_b64_tr_b16 v[30:31], v106 offset:38912\n"
    "s_waitcnt lgkmcnt(12)\n"
    "v_mfma_f32_16x16x32_bf16 v[128:131], v[0:3], a[80:83], v[128:131]\n"
    "ds_read_b64_tr_b16 v[32:33], v97 offset:40960\n"
    "v_mfma_f32_16x16x32_bf16 v[132:135], v[0:3], a[84:87], v[132:135]\n"
    "ds_read_b64_tr_b16 v[34:35], v97 offset:43008\n"
    "buffer_load_dwordx4 a[48:51], v108, s[40:43], s56 offen sc1\n"
    "v_mfma_f32_16x16x32_bf16 v[136:139], v[4:7], a[80:83], v[136:139]\n"
    "ds_read_b64_tr_b16 v[36:37], v100 offset:40960\n"
    "v_mfma_f32_16x16x32_bf16 v[140:143], v[4:7], a[84:87], v[140:143]\n"
    "ds_read_b64_tr_b16 v[38:39], v100 offset:43008\n"
    "s_waitcnt lgkmcnt(12)\n"
    "v_mfma_f32_16x16x32_bf16 v[144:147], v[8:11], a[80:83], v[144:147]\n"
    "ds_read_b64_tr_b16 v[40:41], v103 offset:40960\n"
    "v_mfma_f32_16x16x32_bf16 v[148:151], v[8:11], a[84:87], v[148:151]\n"
    "ds_read_b64_tr_b16 v[42:43], v103 offset:43008\n"
    "buffer_load_dwordx4 a[52:55], v111, s[40:43], s56 offen sc1\n"
    "v_mfma_f32_16x16x32_bf16 v[152:155], v[12:15], a[80:83], v[152:155]\n"
    "ds_read_b64_tr_b16 v[44:45], v106 offset:40960\n"
    "s_add_u32 s56, s56, 131072\n"
    "v_mfma_f32_16x16x32_bf16 v[156:159], v[12:15], a[84:87], v[156:159]\n"
    "ds_read_b64_tr_b16 v[46:47], v106 offset:43008\n"
    "s_waitcnt lgkmcnt(12)\n"
    "v_mfma_f32_16x16x32_bf16 v[160:163], v[16:19], a[80:83], v[160:163]\n"
    "ds_read_b64_tr_b16 v[48:49], v97 offset:45056\n"
    "v_mfma_f32_16x16x32_bf16 v[164:167], v[16:19], a[84:87], v[164:167]\n"
    "ds_read_b64_tr_b16 v[50:51], v97 offset:47104\n"
    "buffer_load_dwordx4 a[56:59], v108, s[40:43], s56 offen sc1\n"
    "v_mfma_f32_16x16x32_bf16 v[168:171], v[20:23], a[80:83], v[168:171]\n"
    "ds_read_b64_tr_b16 v[52:53], v100 offset:45056\n"
    "v_mfma_f32_16x16x32_bf16 v[172:175], v[20:23], a[84:87], v[172:175]\n"
    "ds_read_b64_tr_b16 v[54:55], v100 offset:47104\n"
    "s_waitcnt lgkmcnt(12)\n"
    "v_mfma_f32_16x16x32_bf16 v[176:179], v[24:27], a[80:83], v[176:179]\n"
    "ds_read_b64_tr_b16 v[56:57], v103 offset:45056\n"
    "v_mfma_f32_16x16x32_bf16 v[180:183], v[24:27], a[84:87], v[180:183]\n"
    "ds_read_b64_tr_b16 v[58:59], v103 offset:47104\n"
    "buffer_load_dwordx4 a[60:63], v111, s[40:43], s56 offen sc1\n"
    "v_mfma_f32_16x16x32_bf16 v[184:187], v[28:31], a[80:83], v[184:187]\n"
    "ds_read_b64_tr_b16 v[60:61], v106 offset:45056\n"
    "s_add_u32 s56, s56, 131072\n"
    "v_mfma_f32_16x16x32_bf16 v[188:191], v[28:31], a[84:87], v[188:191]\n"
    "ds_read_b64_tr_b16 v[62:63], v106 offset:47104\n"
    "s_waitcnt vmcnt(48) lgkmcnt(12)\n"
    "s_barrier\n"
    "v_mfma_f32_16x16x32_bf16 v[128:131], v[32:35], a[88:91], v[128:131]\n"
    "ds_read_b64_tr_b16 v[0:1], v98 offset:0\n"
    "s_add_u32 m0, s62, 65536\n"
    "v_mfma_f32_16x16x32_bf16 v[132:135], v[32:35], a[92:95], v[132:135]\n"
    "ds_read_b64_tr_b16 v[2:3], v98 offset:2048\n"
    "buffer_load_dwordx4 v109, s[44:47], s57 offen lds\n"
    "v_mfma_f32_16x16x32_bf16 v[136:139], v[36:39], a[88:91], v[136:139]\n"
    "ds_read_b64_tr_b16 v[4:5], v101 offset:0\n"
    "v_mfma_f32_16x16x32_bf16 v[140:143], v[36:39], a[92:95], v[140:143]\n"
    "ds_read_b64_tr_b16 v[6:7], v101 offset:2048\n"
    "s_waitcnt lgkmcnt(12)\n"
    "v_mfma_f32_16x16x32_bf16 v[144:147], v[40:43], a[88:91], v[144:147]\n"
    "ds_read_b64_tr_b16 v[8:9], v104 offset:0\n"
    "s_add_u32 m0, s62, 69504\n"
    "v_mfma_f32_16x16x32_bf16 v[148:151], v[40:43], a[92:95], v[148:151]\n"
    "ds_read_b64_tr_b16 v[10:11], v104 offset:2048\n"
    "buffer_load_dwordx4 v109, s[44:47], s57 offen offset:128 lds\n"
    "v_mfma_f32_16x16x32_bf16 v[152:155], v[44:47], a[88:91], v[152:155]\n"
    "ds_read_b64_tr_b16 v[12:13], v107 offset:0\n"
    "s_add_u32 s57, s57, 131072\n"
    "v_mfma_f32_16x16x32_bf16 v[156:159], v[44:47], a[92:95], v[156:159]\n"
    "ds_read_b64_tr_b16 v[14:15], v107 offset:2048\n"
    "s_waitcnt lgkmcnt(12)\n"
    "v_mfma_f32_16x16x32_bf16 v[160:163], v[48:51], a[88:91], v[160:163]\n"
    "ds_read_b64_tr_b16 v[16:17], v98 offset:4096\n"
    "s_add_u32 m0, s62, 73728\n"
    "v_mfma_f32_16x16x32_bf16 v[164:167], v[48:51], a[92:95], v[164:167]\n"
    "ds_read_b64_tr_b16 v[18:19], v98 offset:6144\n"
    "buffer_load_dwordx4 v109, s[44:47], s59 offen lds\n"
    "v_mfma_f32_16x16x32_bf16 v[168:171], v[52:55], a[88:91], v[168:171]\n"
    "ds_read_b64_tr_b16 v[20:21], v101 offset:4096\n"
    "v_mfma_f32_16x16x32_bf16 v[172:175], v[52:55], a[92:95], v[172:175]\n"
    "ds_read_b64_tr_b16 v[22:23], v101 offset:6144\n"
    "s_waitcnt lgkmcnt(12)\n"
    "v_mfma_f32_16x16x32_bf16 v[176:179], v[56:59], a[88:91], v[176:179]\n"
    "ds_read_b64_tr_b16 v[24:25], v104 offset:4096\n"
    "s_add_u32 m0, s62, 77696\n"
    "v_mfma_f32_16x16x32_bf16 v[180:183], v[56:59], a[92:95], v[180:183]\n"
    "ds_read_b64_tr_b16 v[26:27], v104 offset:6144\n"
    "buffer_load_dwordx4 v109, s[44:47], s59 offen offset:128 lds\n"
    "v_mfma_f32_16x16x32_bf16 v[184:187], v[60:63], a[88:91], v[184:187]\n"
    "ds_read_b64_tr_b16 v[28:29], v107 offset:4096\n"
    "s_add_u32 s59, s59, 131072\n"
    "v_mfma_f32_16x16x32_bf16 v[188:191], v[60:63], a[92:95], v[188:191]\n"
    "ds_read_b64_tr_b16 v[30:31], v107 offset:6144\n"
    "s_waitcnt lgkmcnt(12)\n"
    "v_mfma_f32_16x16x32_bf16 v[128:131], v[0:3], a[96:99], v[128:131]\n"
    "ds_read_b64_tr_b16 v[32:33], v98 offset:8192\n"
    "v_mfma_f32_16x16x32_bf16 v[132:135], v[0:3], a[100:103], v[132:135]\n"
    "ds_read_b64_tr_b16 v[34:35], v98 offset:10240\n"
    "buffer_load_dwordx4 a[64:67], v108, s[40:43], s56 offen sc1\n"
    "v_mfma_f32_16x16x32_bf16 v[136:139], v[4:7], a[96:99], v[136:139]\n"
    "ds_read_b64_tr_b16 v[36:37], v101 offset:8192\n"
    "v_mfma_f32_16x16x32_bf16 v[140:143], v[4:7], a[100:103], v[140:143]\n"
    "ds_read_b64_tr_b16 v[38:39], v101 offset:10240\n"
    "s_waitcnt lgkmcnt(12)\n"
    "v_mfma_f32_16x16x32_bf16 v[144:147], v[8:11], a[96:99], v[144:147]\n"
    "ds_read_b64_tr_b16 v[40:41], v104 offset:8192\n"
    "v_mfma_f32_16x16x32_bf16 v[148:151], v[8:11], a[100:103], v[148:151]\n"
    "ds_read_b64_tr_b16 v[42:43], v104 offset:10240\n"
    "buffer_load_dwordx4 a[68:71], v111, s[40:43], s56 offen sc1\n"
    "v_mfma_f32_16x16x32_bf16 v[152:155], v[12:15], a[96:99], v[152:155]\n"
    "ds_read_b64_tr_b16 v[44:45], v107 offset:8192\n"
    "s_add_u32 s56, s56, 131072\n"
    "v_mfma_f32_16x16x32_bf16 v[156:159], v[12:15], a[100:103], v[156:159]\n"
    "ds_read_b64_tr_b16 v[46:47], v107 offset:10240\n"
    "s_waitcnt lgkmcnt(12)\n"
    "v_mfma_f32_16x16x32_bf16 v[160:163], v[16:19], a[96:99], v[160:163]\n"
    "ds_read_b64_tr_b16 v[48:49], v98 offset:12288\n"
    "v_mfma_f32_16x16x32_bf16 v[164:167], v[16:19], a[100:103], v[164:167]\n"
    "ds_read_b64_tr_b16 v[50:51], v98 offset:14336\n"
    "buffer_load_dwordx4 a[72:75], v108, s[40:43], s56 offen sc1\n"
    "v_mfma_f32_16x16x32_bf16 v[168:171], v[20:23], a[96:99], v[168:171]\n"
    "ds_read_b64_tr_b16 v[52:53], v101 offset:12288\n"
    "v_mfma_f32_16x16x32_bf16 v[172:175], v[20:23], a[100:103], v[172:175]\n"
    "ds_read_b64_tr_b16 v[54:55], v101 offset:14336\n"
    "s_waitcnt lgkmcnt(12)\n"
    "v_mfma_f32_16x16x32_bf16 v[176:179], v[24:27], a[96:99], v[176:179]\n"
    "ds_read_b64_tr_b16 v[56:57], v104 offset:12288\n"
    "v_mfma_f32_16x16x32_bf16 v[180:183], v[24:27], a[100:103], v[180:183]\n"
    "ds_read_b64_tr_b16 v[58:59], v104 offset:14336\n"
    "buffer_load_dwordx4 a[76:79], v111, s[40:43], s56 offen sc1\n"
    "v_mfma_f32_16x16x32_bf16 v[184:187], v[28:31], a[96:99], v[184:187]\n"
    "ds_read_b64_tr_b16 v[60:61], v107 offset:12288\n"
    "s_add_u32 s56, s56, 131072\n"
    "v_mfma_f32_16x16x32_bf16 v[188:191], v[28:31], a[100:103], v[188:191]\n"
    "ds_read_b64_tr_b16 v[62:63], v107 offset:14336\n"
    "s_waitcnt vmcnt(48) lgkmcnt(12)\n"
    "s_barrier\n"
    "v_mfma_f32_16x16x32_bf16 v[128:131], v[32:35], a[104:107], v[128:131]\n"
    "ds_read_b64_tr_b16 v[0:1], v98 offset:16384\n"
    "s_add_u32 m0, s62, 81920\n"
    "v_mfma_f32_16x16x32_bf16 v[132:135], v[32:35], a[108:111], v[132:135]\n"
    "ds_read_b64_tr_b16 v[2:3], v98 offset:18432\n"
    "buffer_load_dwordx4 v109, s[44:47], s57 offen lds\n"
    "v_mfma_f32_16x16x32_bf16 v[136:139], v[36:39], a[104:107], v[136:139]\n"
    "ds_read_b64_tr_b16 v[4:5], v101 offset:16384\n"
    "v_mfma_f32_16x16x32_bf16 v[140:143], v[36:39], a[108:111], v[140:143]\n"
    "ds_read_b64_tr_b16 v[6:7], v101 offset:18432\n"
    "s_waitcnt lgkmcnt(12)\n"
    "v_mfma_f32_16x16x32_bf16 v[144:147], v[40:43], a[104:107], v[144:147]\n"
    "ds_read_b64_tr_b16 v[8:9], v104 offset:16384\n"
    "s_add_u32 m0, s62, 85888\n"
    "v_mfma_f32_16x16x32_bf16 v[148:151], v[40:43], a[108:111], v[148:151]\n"
    "ds_read_b64_tr_b16 v[10:11], v104 offset:18432\n"
    "buffer_load_dwordx4 v109, s[44:47], s57 offen offset:128 lds\n"
    "v_mfma_f32_16x16x32_bf16 v[152:155], v[44:47], a[104:107], v[152:155]\n"
    "ds_read_b64_tr_b16 v[12:13], v107 offset:16384\n"
    "s_add_u32 s57, s57, 131072\n"
    "v_mfma_f32_16x16x32_bf16 v[156:159], v[44:47], a[108:111], v[156:159]\n"
    "ds_read_b64_tr_b16 v[14:15], v107 offset:18432\n"
    "s_waitcnt lgkmcnt(12)\n"
    "v_mfma_f32_16x16x32_bf16 v[160:163], v[48:51], a[104:107], v[160:163]\n"
    "ds_read_b64_tr_b16 v[16:17], v98 offset:20480\n"
    "s_add_u32 m0, s62, 90112\n"
    "v_mfma_f32_16x16x32_bf16 v[164:167], v[48:51], a[108:111], v[164:167]\n"
    "ds_read_b64_tr_b16 v[18:19], v98 offset:22528\n"
    "buffer_load_dwordx4 v109, s[44:47], s59 offen lds\n"
    "v_mfma_f32_16x16x32_bf16 v[168:171], v[52:55], a[104:107], v[168:171]\n"
    "ds_read_b64_tr_b16 v[20:21], v101 offset:20480\n"
    "v_mfma_f32_16x16x32_bf16 v[172:175], v[52:55], a[108:111], v[172:175]\n"
    "ds_read_b64_tr_b16 v[22:23], v101 offset:22528\n"
    "s_waitcnt lgkmcnt(12)\n"
    "v_mfma_f32_16x16x32_bf16 v[176:179], v[56:59], a[104:107], v[176:179]\n"
    "ds_read_b64_tr_b16 v[24:25], v104 offset:20480\n"
    "s_add_u32 m0, s62, 94080\n"
    "v_mfma_f32_16x16x32_bf16 v[180:183], v[56:59], a[108:111], v[180:183]\n"
    "ds_read_b64_tr_b16 v[26:27], v104 offset:22528\n"
    "buffer_load_dwordx4 v109, s[44:47], s59 offen offset:128 lds\n"
    "v_mfma_f32_16x16x32_bf16 v[184:187], v[60:63], a[104:107], v[184:187]\n"
    "ds_read_b64_tr_b16 v[28:29], v107 offset:20480\n"
    "s_add_u32 s59, s59, 131072\n"
    "v_mfma_f32_16x16x32_bf16 v[188:191], v[60:63], a[108:111], v[188:191]\n"
    "ds_read_b64_tr_b16 v[30:31], v107 offset:22528\n"
    "s_waitcnt lgkmcnt(12)\n"
    "v_mfma_f32_16x16x32_bf16 v[128:131], v[0:3], a[112:115], v[128:131]\n"
    "ds_read_b64_tr_b16 v[32:33], v98 offset:24576\n"
    "v_mfma_f32_16x16x32_bf16 v[132:135], v[0:3], a[116:119], v[132:135]\n"
    "ds_read_b64_tr_b16 v[34:35], v98 offset:26624\n"
    "buffer_load_dwordx4 a[80:83], v108, s[40:43], s56 offen sc1\n"
    "v_mfma_f32_16x16x32_bf16 v[136:139], v[4:7], a[112:115], v[136:139]\n"
    "ds_read_b64_tr_b16 v[36:37], v101 offset:24576\n"
    "v_mfma_f32_16x16x32_bf16 v[140:143], v[4:7], a[116:119], v[140:143]\n"
    "ds_read_b64_tr_b16 v[38:39], v101 offset:26624\n"
    "s_waitcnt lgkmcnt(12)\n"
    "v_mfma_f32_16x16x32_bf16 v[144:147], v[8:11], a[112:115], v[144:147]\n"
    "ds_read_b64_tr_b16 v[40:41], v104 offset:24576\n"
    "v_mfma_f32_16x16x32_bf16 v[148:151], v[8:11], a[116:119], v[148:151]\n"
    "ds_read_b64_tr_b16 v[42:43], v104 offset:26624\n"
    "buffer_load_dwordx4 a[84:87], v111, s[40:43], s56 offen sc1\n"
    "v_mfma_f32_16x16x32_bf16 v[152:155], v[12:15], a[112:115], v[152:155]\n"
    "ds_read_b64_tr_b16 v[44:45], v107 offset:24576\n"
    "s_add_u32 s56, s56, 131072\n"
    "v_mfma_f32_16x16x32_bf16 v[156:159], v[12:15], a[116:119], v[156:159]\n"
    "ds_read_b64_tr_b16 v[46:47], v107 offset:26624\n"
    "s_waitcnt lgkmcnt(12)\n"
    "v_mfma_f32_16x16x32_bf16 v[160:163], v[16:19], a[112:115], v[160:163]\n"
    "ds_read_b64_tr_b16 v[48:49], v98 offset:28672\n"
    "v_mfma_f32_16x16x32_bf16 v[164:167], v[16:19], a[116:119], v[164:167]\n"
    "ds_read_b64_tr_b16 v[50:51], v98 offset:30720\n"
    "buffer_load_dwordx4 a[88:91], v108, s[40:43], s56 offen sc1\n"
    "v_mfma_f32_16x16x32_bf16 v[168:171], v[20:23], a[112:115], v[168:171]\n"
    "ds_read_b64_tr_b16 v[52:53], v101 offset:28672\n"
    "v_mfma_f32_16x16x32_bf16 v[172:175], v[20:23], a[116:119], v[172:175]\n"
    "ds_read_b64_tr_b16 v[54:55], v101 offset:30720\n"
    "s_waitcnt lgkmcnt(12)\n"
    "v_mfma_f32_16x16x32_bf16 v[176:179], v[24:27], a[112:115], v[176:179]\n"
    "ds_read_b64_tr_b16 v[56:57], v104 offset:28672\n"
    "v_mfma_f32_16x16x32_bf16 v[180:183], v[24:27], a[116:119], v[180:183]\n"
    "ds_read_b64_tr_b16 v[58:59], v104 offset:30720\n"
    "buffer_load_dwordx4 a[92:95], v111, s[40:43], s56 offen sc1\n"
    "v_mfma_f32_16x16x32_bf16 v[184:187], v[28:31], a[112:115], v[184:187]\n"
    "ds_read_b64_tr_b16 v[60:61], v107 offset:28672\n"
    "s_add_u32 s56, s56, 131072\n"
    "v_mfma_f32_16x16x32_bf16 v[188:191], v[28:31], a[116:119], v[188:191]\n"
    "ds_read_b64_tr_b16 v[62:63], v107 offset:30720\n"
    "s_waitcnt vmcnt(48) lgkmcnt(12)\n"
    "s_barrier\n"
    "v_mfma_f32_16x16x32_bf16 v[128:131], v[32:35], a[120:123], v[128:131]\n"
    "ds_read_b64_tr_b16 v[0:1], v98 offset:32768\n"
    "s_add_u32 m0, s62, 98304\n"
    "v_mfma_f32_16x16x32_bf16 v[132:135], v[32:35], a[124:127], v[132:135]\n"
    "ds_read_b64_tr_b16 v[2:3], v98 offset:34816\n"
    "buffer_load_dwordx4 v109, s[44:47], s57 offen lds\n"
    "v_mfma_f32_16x16x32_bf16 v[136:139], v[36:39], a[120:123], v[136:139]\n"
    "ds_read_b64_tr_b16 v[4:5], v101 offset:32768\n"
    "v_mfma_f32_16x16x32_bf16 v[140:143], v[36:39], a[124:127], v[140:143]\n"
    "ds_read_b64_tr_b16 v[6:7], v101 offset:34816\n"
    "s_waitcnt lgkmcnt(12)\n"
    "v_mfma_f32_16x16x32_bf16 v[144:147], v[40:43], a[120:123], v[144:147]\n"
    "ds_read_b64_tr_b16 v[8:9], v104 offset:32768\n"
    "s_add_u32 m0, s62, 102272\n"
    "v_mfma_f32_16x16x32_bf16 v[148:151], v[40:43], a[124:127], v[148:151]\n"
    "ds_read_b64_tr_b16 v[10:11], v104 offset:34816\n"
    "buffer_load_dwordx4 v109, s[44:47], s57 offen offset:128 lds\n"
    "v_mfma_f32_16x16x32_bf16 v[152:155], v[44:47], a[120:123], v[152:155]\n"
    "ds_read_b64_tr_b16 v[12:13], v107 offset:32768\n"
    "s_add_u32 s57, s57, 131072\n"
    "v_mfma_f32_16x16x32_bf16 v[156:159], v[44:47], a[124:127], v[156:159]\n"
    "ds_read_b64_tr_b16 v[14:15], v107 offset:34816\n"
    "s_waitcnt lgkmcnt(12)\n"
    "v_mfma_f32_16x16x32_bf16 v[160:163], v[48:51], a[120:123], v[160:163]\n"
    "ds_read_b64_tr_b16 v[16:17], v98 offset:36864\n"
    "s_add_u32 m0, s62, 106496\n"
    "v_mfma_f32_16x16x32_bf16 v[164:167], v[48:51], a[124:127], v[164:167]\n"
    "ds_read_b64_tr_b16 v[18:19], v98 offset:38912\n"
    "buffer_load_dwordx4 v109, s[44:47], s59 offen lds\n"
    "v_mfma_f32_16x16x32_bf16 v[168:171], v[52:55], a[120:123], v[168:171]\n"
    "ds_read_b64_tr_b16 v[20:21], v101 offset:36864\n"
    "v_mfma_f32_16x16x32_bf16 v[172:175], v[52:55], a[124:127], v[172:175]\n"
    "ds_read_b64_tr_b16 v[22:23], v101 offset:38912\n"
    "s_waitcnt lgkmcnt(12)\n"
    "v_mfma_f32_16x16x32_bf16 v[176:179], v[56:59], a[120:123], v[176:179]\n"
    "ds_read_b64_tr_b16 v[24:25], v104 offset:36864\n"
    "s_add_u32 m0, s62, 110464\n"
    "v_mfma_f32_16x16x32_bf16 v[180:183], v[56:59], a[124:127], v[180:183]\n"
    "ds_read_b64_tr_b16 v[26:27], v104 offset:38912\n"
    "buffer_load_dwordx4 v109, s[44:47], s59 offen offset:128 lds\n"
    "v_mfma_f32_16x16x32_bf16 v[184:187], v[60:63], a[120:123], v[184:187]\n"
    "ds_read_b64_tr_b16 v[28:29], v107 offset:36864\n"
    "s_add_u32 s59, s59, 131072\n"
    "v_mfma_f32_16x16x32_bf16 v[188:191], v[60:63], a[124:127], v[188:191]\n"
    "ds_read_b64_tr_b16 v[30:31], v107 offset:38912\n"
    "s_waitcnt lgkmcnt(12)\n"
    "v_mfma_f32_16x16x32_bf16 v[128:131], v[0:3], a[128:131], v[128:131]\n"
    "ds_read_b64_tr_b16 v[32:33], v98 offset:40960\n"
    "v_mfma_f32_16x16x32_bf16 v[132:135], v[0:3], a[132:135], v[132:135]\n"
    "ds_read_b64_tr_b16 v[34:35], v98 offset:43008\n"
    "buffer_load_dwordx4 a[96:99], v108, s[40:43], s56 offen sc1\n"
    "v_mfma_f32_16x16x32_bf16 v[136:139], v[4:7], a[128:131], v[136:139]\n"
    "ds_read_b64_tr_b16 v[36:37], v101 offset:40960\n"
    "v_mfma_f32_16x16x32_bf16 v[140:143], v[4:7], a[132:135], v[140:143]\n"
    "ds_read_b64_tr_b16 v[38:39], v101 offset:43008\n"
    "s_waitcnt lgkmcnt(12)\n"
    "v_mfma_f32_16x16x32_bf16 v[144:147], v[8:11], a[128:131], v[144:147]\n"
    "ds_read_b64_tr_b16 v[40:41], v104 offset:40960\n"
    "v_mfma_f32_16x16x32_bf16 v[148:151], v[8:11], a[132:135], v[148:151]\n"
    "ds_read_b64_tr_b16 v[42:43], v104 offset:43008\n"
    "buffer_load_dwordx4 a[100:103], v111, s[40:43], s56 offen sc1\n"
    "v_mfma_f32_16x16x32_bf16 v[152:155], v[12:15], a[128:131], v[152:155]\n"
    "ds_read_b64_tr_b16 v[44:45], v107 offset:40960\n"
    "s_add_u32 s56, s56, 131072\n"
    "v_mfma_f32_16x16x32_bf16 v[156:159], v[12:15], a[132:135], v[156:159]\n"
    "ds_read_b64_tr_b16 v[46:47], v107 offset:43008\n"
    "s_waitcnt lgkmcnt(12)\n"
    "v_mfma_f32_16x16x32_bf16 v[160:163], v[16:19], a[128:131], v[160:163]\n"
    "ds_read_b64_tr_b16 v[48:49], v98 offset:45056\n"
    "v_mfma_f32_16x16x32_bf16 v[164:167], v[16:19], a[132:135], v[164:167]\n"
    "ds_read_b64_tr_b16 v[50:51], v98 offset:47104\n"
    "buffer_load_dwordx4 a[104:107], v108, s[40:43], s56 offen sc1\n"
    "v_mfma_f32_16x16x32_bf16 v[168:171], v[20:23], a[128:131], v[168:171]\n"
    "ds_read_b64_tr_b16 v[52:53], v101 offset:45056\n"
    "v_mfma_f32_16x16x32_bf16 v[172:175], v[20:23], a[132:135], v[172:175]\n"
    "ds_read_b64_tr_b16 v[54:55], v101 offset:47104\n"
    "s_waitcnt lgkmcnt(12)\n"
    "v_mfma_f32_16x16x32_bf16 v[176:179], v[24:27], a[128:131], v[176:179]\n"
    "ds_read_b64_tr_b16 v[56:57], v104 offset:45056\n"
    "v_mfma_f32_16x16x32_bf16 v[180:183], v[24:27], a[132:135], v[180:183]\n"
    "ds_read_b64_tr_b16 v[58:59], v104 offset:47104\n"
    "buffer_load_dwordx4 a[108:111], v111, s[40:43], s56 offen sc1\n"
    "v_mfma_f32_16x16x32_bf16 v[184:187], v[28:31], a[128:131], v[184:187]\n"
    "ds_read_b64_tr_b16 v[60:61], v107 offset:45056\n"
    "s_add_u32 s56, s56, 131072\n"
    "v_mfma_f32_16x16x32_bf16 v[188:191], v[28:31], a[132:135], v[188:191]\n"
    "ds_read_b64_tr_b16 v[62:63], v107 offset:47104\n"
    "s_waitcnt vmcnt(48) lgkmcnt(12)\n"
    "s_barrier\n"
    "v_mfma_f32_16x16x32_bf16 v[128:131], v[32:35], a[136:139], v[128:131]\n"
    "ds_read_b64_tr_b16 v[0:1], v96 offset:0\n"
    "s_add_u32 m0, s62, 114688\n"
    "v_mfma_f32_16x16x32_bf16 v[132:135], v[32:35], a[140:143], v[132:135]\n"
    "ds_read_b64_tr_b16 v[2:3], v96 offset:2048\n"
    "buffer_load_dwordx4 v109, s[44:47], s57 offen lds\n"
    "v_mfma_f32_16x16x32_bf16 v[136:139], v[36:39], a[136:139], v[136:139]\n"
    "ds_read_b64_tr_b16 v[4:5], v99 offset:0\n"
    "v_mfma_f32_16x16x32_bf16 v[140:143], v[36:39], a[140:143], v[140:143]\n"
    "ds_read_b64_tr_b16 v[6:7], v99 offset:2048\n"
    "s_waitcnt lgkmcnt(12)\n"
    "v_mfma_f32_16x16x32_bf16 v[144:147], v[40:43], a[136:139], v[144:147]\n"
    "ds_read_b64_tr_b16 v[8:9], v102 offset:0\n"
    "s_add_u32 m0, s62, 118656\n"
    "v_mfma_f32_16x16x32_bf16 v[148:151], v[40:43], a[140:143], v[148:151]\n"
    "ds_read_b64_tr_b16 v[10:11], v102 offset:2048\n"
    "buffer_load_dwordx4 v109, s[44:47], s57 offen offset:128 lds\n"
    "v_mfma_f32_16x16x32_bf16 v[152:155], v[44:47], a[136:139], v[152:155]\n"
    "ds_read_b64_tr_b16 v[12:13], v105 offset:0\n"
    "s_add_u32 s57, s57, 131072\n"
    "v_mfma_f32_16x16x32_bf16 v[156:159], v[44:47], a[140:143], v[156:159]\n"
    "ds_read_b64_tr_b16 v[14:15], v105 offset:2048\n"
    "s_waitcnt lgkmcnt(12)\n"
    "v_mfma_f32_16x16x32_bf16 v[160:163], v[48:51], a[136:139], v[160:163]\n"
    "ds_read_b64_tr_b16 v[16:17], v96 offset:4096\n"
    "s_add_u32 m0, s62, 122880\n"
    "v_mfma_f32_16x16x32_bf16 v[164:167], v[48:51], a[140:143], v[164:167]\n"
    "ds_read_b64_tr_b16 v[18:19], v96 offset:6144\n"
    "buffer_load_dwordx4 v109, s[44:47], s59 offen lds\n"
    "v_mfma_f32_16x16x32_bf16 v[168:171], v[52:55], a[136:139], v[168:171]\n"
    "ds_read_b64_tr_b16 v[20:21], v99 offset:4096\n"
    "v_mfma_f32_16x16x32_bf16 v[172:175], v[52:55], a[140:143], v[172:175]\n"
    "ds_read_b64_tr_b16 v[22:23], v99 offset:6144\n"
    "s_waitcnt lgkmcnt(12)\n"
    "v_mfma_f32_16x16x32_bf16 v[176:179], v[56:59], a[136:139], v[176:179]\n"
    "ds_read_b64_tr_b16 v[24:25], v102 offset:4096\n"
    "s_add_u32 m0, s62, 126848\n"
    "v_mfma_f32_16x16x32_bf16 v[180:183], v[56:59], a[140:143], v[180:183]\n"
    "ds_read_b64_tr_b16 v[26:27], v102 offset:6144\n"
    "buffer_load_dwordx4 v109, s[44:47], s59 offen offset:128 lds\n"
    "v_mfma_f32_16x16x32_bf16 v[184:187], v[60:63], a[136:139], v[184:187]\n"
    "ds_read_b64_tr_b16 v[28:29], v105 offset:4096\n"
    "s_add_u32 s59, s59, 131072\n"
    "v_mfma_f32_16x16x32_bf16 v[188:191], v[60:63], a[140:143], v[188:191]\n"
    "ds_read_b64_tr_b16 v[30:31], v105 offset:6144\n"
    "s_waitcnt lgkmcnt(12)\n"
    "v_mfma_f32_16x16x32_bf16 v[128:131], v[0:3], a[0:3], v[128:131]\n"
    "ds_read_b64_tr_b16 v[32:33], v96 offset:8192\n"
    "v_mfma_f32_16x16x32_bf16 v[132:135], v[0:3], a[4:7], v[132:135]\n"
    "ds_read_b64_tr_b16 v[34:35], v96 offset:10240\n"
    "buffer_load_dwordx4 a[112:115], v108, s[40:43], s56 offen sc1\n"
    "v_mfma_f32_16x16x32_bf16 v[136:139], v[4:7], a[0:3], v[136:139]\n"
    "ds_read_b64_tr_b16 v[36:37], v99 offset:8192\n"
    "v_mfma_f32_16x16x32_bf16 v[140:143], v[4:7], a[4:7], v[140:143]\n"
    "ds_read_b64_tr_b16 v[38:39], v99 offset:10240\n"
    "s_waitcnt lgkmcnt(12)\n"
    "v_mfma_f32_16x16x32_bf16 v[144:147], v[8:11], a[0:3], v[144:147]\n"
    "ds_read_b64_tr_b16 v[40:41], v102 offset:8192\n"
    "v_mfma_f32_16x16x32_bf16 v[148:151], v[8:11], a[4:7], v[148:151]\n"
    "ds_read_b64_tr_b16 v[42:43], v102 offset:10240\n"
    "buffer_load_dwordx4 a[116:119], v111, s[40:43], s56 offen sc1\n"
    "v_mfma_f32_16x16x32_bf16 v[152:155], v[12:15], a[0:3], v[152:155]\n"
    "ds_read_b64_tr_b16 v[44:45], v105 offset:8192\n"
    "s_add_u32 s56, s56, 131072\n"
    "v_mfma_f32_16x16x32_bf16 v[156:159], v[12:15], a[4:7], v[156:159]\n"
    "ds_read_b64_tr_b16 v[46:47], v105 offset:10240\n"
    "s_waitcnt lgkmcnt(12)\n"
    "v_mfma_f32_16x16x32_bf16 v[160:163], v[16:19], a[0:3], v[160:163]\n"
    "ds_read_b64_tr_b16 v[48:49], v96 offset:12288\n"
    "v_mfma_f32_16x16x32_bf16 v[164:167], v[16:19], a[4:7], v[164:167]\n"
    "ds_read_b64_tr_b16 v[50:51], v96 offset:14336\n"
    "buffer_load_dwordx4 a[120:123], v108, s[40:43], s56 offen sc1\n"
    "v_mfma_f32_16x16x32_bf16 v[168:171], v[20:23], a[0:3], v[168:171]\n"
    "ds_read_b64_tr_b16 v[52:53], v99 offset:12288\n"
    "v_mfma_f32_16x16x32_bf16 v[172:175], v[20:23], a[4:7], v[172:175]\n"
    "ds_read_b64_tr_b16 v[54:55], v99 offset:14336\n"
    "s_waitcnt lgkmcnt(12)\n"
    "v_mfma_f32_16x16x32_bf16 v[176:179], v[24:27], a[0:3], v[176:179]\n"
    "ds_read_b64_tr_b16 v[56:57], v102 offset:12288\n"
    "v_mfma_f32_16x16x32_bf16 v[180:183], v[24:27], a[4:7], v[180:183]\n"
    "ds_read_b64_tr_b16 v[58:59], v102 offset:14336\n"
    "buffer_load_dwordx4 a[124:127], v111, s[40:43], s56 offen sc1\n"
    "v_mfma_f32_16x16x32_bf16 v[184:187], v[28:31], a[0:3], v[184:187]\n"
    "ds_read_b64_tr_b16 v[60:61], v105 offset:12288\n"
    "s_add_u32 s56, s56, 131072\n"
    "v_mfma_f32_16x16x32_bf16 v[188:191], v[28:31], a[4:7], v[188:191]\n"
    "ds_read_b64_tr_b16 v[62:63], v105 offset:14336\n"
    "s_add_u32 s58, s58, 1\n"
    "s_cmp_lg_u32 s58, 2\n"
    "s_cbranch_scc1 LOOPk2_%=\n"
    "s_waitcnt vmcnt(48) lgkmcnt(12)\n"
    "s_barrier\n"
    "v_mfma_f32_16x16x32_bf16 v[128:131], v[32:35], a[8:11], v[128:131]\n"
    "ds_read_b64_tr_b16 v[0:1], v96 offset:16384\n"
    "s_add_u32 m0, s62, 131072\n"
    "v_mfma_f32_16x16x32_bf16 v[132:135], v[32:35], a[12:15], v[132:135]\n"
    "ds_read_b64_tr_b16 v[2:3], v96 offset:18432\n"
    "buffer_load_dwordx4 v109, s[44:47], s57 offen lds\n"
    "v_mfma_f32_16x16x32_bf16 v[136:139], v[36:39], a[8:11], v[136:139]\n"
    "ds_read_b64_tr_b16 v[4:5], v99 offset:16384\n"
    "v_mfma_f32_16x16x32_bf16 v[140:143], v[36:39], a[12:15], v[140:143]\n"
    "ds_read_b64_tr_b16 v[6:7], v99 offset:18432\n"
    "s_waitcnt lgkmcnt(12)\n"
    "v_mfma_f32_16x16x32_bf16 v[144:147], v[40:43], a[8:11], v[144:147]\n"
    "ds_read_b64_tr_b16 v[8:9], v102 offset:16384\n"
    "s_add_u32 m0, s62, 135040\n"
    "v_mfma_f32_16x16x32_bf16 v[148:151], v[40:43], a[12:15], v[148:151]\n"
    "ds_read_b64_tr_b16 v[10:11], v102 offset:18432\n"
    "buffer_load_dwordx4 v109, s[44:47], s57 offen offset:128 lds\n"
    "v_mfma_f32_16x16x32_bf16 v[152:155], v[44:47], a[8:11], v[152:155]\n"
    "ds_read_b64_tr_b16 v[12:13], v105 offset:16384\n"
    "s_add_u32 s57, s57, 131072\n"
    "v_mfma_f32_16x16x32_bf16 v[156:159], v[44:47], a[12:15], v[156:159]\n"
    "ds_read_b64_tr_b16 v[14:15], v105 offset:18432\n"
    "s_waitcnt lgkmcnt(12)\n"
    "v_mfma_f32_16x16x32_bf16 v[160:163], v[48:51], a[8:11], v[160:163]\n"
    "ds_read_b64_tr_b16 v[16:17], v96 offset:20480\n"
    "s_add_u32 m0, s62, 139264\n"
    "v_mfma_f32_16x16x32_bf16 v[164:167], v[48:51], a[12:15], v[164:167]\n"
    "ds_read_b64_tr_b16 v[18:19], v96 offset:22528\n"
    "buffer_load_dwordx4 v109, s[44:47], s59 offen lds\n"
    "v_mfma_f32_16x16x32_bf16 v[168:171], v[52:55], a[8:11], v[168:171]\n"
    "ds_read_b64_tr_b16 v[20:21], v99 offset:20480\n"
    "v_mfma_f32_16x16x32_bf16 v[172:175], v[52:55], a[12:15], v[172:175]\n"
    "ds_read_b64_tr_b16 v[22:23], v99 offset:22528\n"
    "s_waitcnt lgkmcnt(12)\n"
    "v_mfma_f32_16x16x32_bf16 v[176:179], v[56:59], a[8:11], v[176:179]\n"
    "ds_read_b64_tr_b16 v[24:25], v102 offset:20480\n"
    "s_add_u32 m0, s62, 143232\n"
    "v_mfma_f32_16x16x32_bf16 v[180:183], v[56:59], a[12:15], v[180:183]\n"
    "ds_read_b64_tr_b16 v[26:27], v102 offset:22528\n"
    "buffer_load_dwordx4 v109, s[44:47], s59 offen offset:128 lds\n"
    "v_mfma_f32_16x16x32_bf16 v[184:187], v[60:63], a[8:11], v[184:187]\n"
    "ds_read_b64_tr_b16 v[28:29], v105 offset:20480\n"
    "s_add_u32 s59, s59, 131072\n"
    "v_mfma_f32_16x16x32_bf16 v[188:191], v[60:63], a[12:15], v[188:191]\n"
    "ds_read_b64_tr_b16 v[30:31], v105 offset:22528\n"
    "s_waitcnt lgkmcnt(12)\n"
    "v_mfma_f32_16x16x32_bf16 v[128:131], v[0:3], a[16:19], v[128:131]\n"
    "ds_read_b64_tr_b16 v[32:33], v96 offset:24576\n"
    "v_mfma_f32_16x16x32_bf16 v[132:135], v[0:3], a[20:23], v[132:135]\n"
    "ds_read_b64_tr_b16 v[34:35], v96 offset:26624\n"
    "buffer_load_dwordx4 a[128:131], v108, s[40:43], s56 offen sc1\n"
    "v_mfma_f32_16x16x32_bf16 v[136:139], v[4:7], a[16:19], v[136:139]\n"
    "ds_read_b64_tr_b16 v[36:37], v99 offset:24576\n"
    "v_mfma_f32_16x16x32_bf16 v[140:143], v[4:7], a[20:23], v[140:143]\n"
    "ds_read_b64_tr_b16 v[38:39], v99 offset:26624\n"
    "s_waitcnt lgkmcnt(12)\n"
    "v_mfma_f32_16x16x32_bf16 v[144:147], v[8:11], a[16:19], v[144:147]\n"
    "ds_read_b64_tr_b16 v[40:41], v102 offset:24576\n"
    "v_mfma_f32_16x16x32_bf16 v[148:151], v[8:11], a[20:23], v[148:151]\n"
    "ds_read_b64_tr_b16 v[42:43], v102 offset:26624\n"
    "buffer_load_dwordx4 a[132:135], v111, s[40:43], s56 offen sc1\n"
    "v_mfma_f32_16x16x32_bf16 v[152:155], v[12:15], a[16:19], v[152:155]\n"
    "ds_read_b64_tr_b16 v[44:45], v105 offset:24576\n"
    "s_add_u32 s56, s56, 131072\n"
    "v_mfma_f32_16x16x32_bf16 v[156:159], v[12:15], a[20:23], v[156:159]\n"
    "ds_read_b64_tr_b16 v[46:47], v105 offset:26624\n"
    "s_waitcnt lgkmcnt(12)\n"
    "v_mfma_f32_16x16x32_bf16 v[160:163], v[16:19], a[16:19], v[160:163]\n"
    "ds_read_b64_tr_b16 v[48:49], v96 offset:28672\n"
    "v_mfma_f32_16x16x32_bf16 v[164:167], v[16:19], a[20:23], v[164:167]\n"
    "ds_read_b64_tr_b16 v[50:51], v96 offset:30720\n"
    "buffer_load_dwordx4 a[136:139], v108, s[40:43], s56 offen sc1\n"
    "v_mfma_f32_16x16x32_bf16 v[168:171], v[20:23], a[16:19], v[168:171]\n"
    "ds_read_b64_tr_b16 v[52:53], v99 offset:28672\n"
    "v_mfma_f32_16x16x32_bf16 v[172:175], v[20:23], a[20:23], v[172:175]\n"
    "ds_read_b64_tr_b16 v[54:55], v99 offset:30720\n"
    "s_waitcnt lgkmcnt(12)\n"
    "v_mfma_f32_16x16x32_bf16 v[176:179], v[24:27], a[16:19], v[176:179]\n"
    "ds_read_b64_tr_b16 v[56:57], v102 offset:28672\n"
    "v_mfma_f32_16x16x32_bf16 v[180:183], v[24:27], a[20:23], v[180:183]\n"
    "ds_read_b64_tr_b16 v[58:59], v102 offset:30720\n"
    "buffer_load_dwordx4 a[140:143], v111, s[40:43], s56 offen sc1\n"
    "v_mfma_f32_16x16x32_bf16 v[184:187], v[28:31], a[16:19], v[184:187]\n"
    "ds_read_b64_tr_b16 v[60:61], v105 offset:28672\n"
    "s_add_u32 s56, s56, 131072\n"
    "v_mfma_f32_16x16x32_bf16 v[188:191], v[28:31], a[20:23], v[188:191]\n"
    "ds_read_b64_tr_b16 v[62:63], v105 offset:30720\n"
    "s_waitcnt vmcnt(48) lgkmcnt(12)\n"
    "s_barrier\n"
    "v_mfma_f32_16x16x32_bf16 v[128:131], v[32:35], a[24:27], v[128:131]\n"
    "ds_read_b64_tr_b16 v[0:1], v96 offset:32768\n"
    "v_mfma_f32_16x16x32_bf16 v[132:135], v[32:35], a[28:31], v[132:135]\n"
    "ds_read_b64_tr_b16 v[2:3], v96 offset:34816\n"
    "buffer_load_dwordx4 v[64:67], v110, s[52:55], 0 offen\n"
    "v_mfma_f32_16x16x32_bf16 v[136:139], v[36:39], a[24:27], v[136:139]\n"
    "ds_read_b64_tr_b16 v[4:5], v99 offset:32768\n"
    "v_mfma_f32_16x16x32_bf16 v[140:143], v[36:39], a[28:31], v[140:143]\n"
    "ds_read_b64_tr_b16 v[6:7], v99 offset:34816\n"
    "s_waitcnt lgkmcnt(12)\n"
    "v_mfma_f32_16x16x32_bf16 v[144:147], v[40:43], a[24:27], v[144:147]\n"
    "ds_read_b64_tr_b16 v[8:9], v102 offset:32768\n"
    "v_mfma_f32_16x16x32_bf16 v[148:151], v[40:43], a[28:31], v[148:151]\n"
    "ds_read_b64_tr_b16 v[10:11], v102 offset:34816\n"
    "buffer_load_dwordx4 v[68:71], v110, s[52:55], 0 offen offset:64\n"
    "v_mfma_f32_16x16x32_bf16 v[152:155], v[44:47], a[24:27], v[152:155]\n"
    "ds_read_b64_tr_b16 v[12:13], v105 offset:32768\n"
    "v_mfma_f32_16x16x32_bf16 v[156:159], v[44:47], a[28:31], v[156:159]\n"
    "ds_read_b64_tr_b16 v[14:15], v105 offset:34816\n"
    "s_waitcnt lgkmcnt(12)\n"
    "v_mfma_f32_16x16x32_bf16 v[160:163], v[48:51], a[24:27], v[160:163]\n"
    "ds_read_b64_tr_b16 v[16:17], v96 offset:36864\n"
    "v_mfma_f32_16x16x32_bf16 v[164:167], v[48:51], a[28:31], v[164:167]\n"
    "ds_read_b64_tr_b16 v[18:19], v96 offset:38912\n"
    "buffer_load_dwordx4 v[72:75], v110, s[52:55], 0 offen offset:128\n"
    "v_mfma_f32_16x16x32_bf16 v[168:171], v[52:55], a[24:27], v[168:171]\n"
    "ds_read_b64_tr_b16 v[20:21], v99 offset:36864\n"
    "v_mfma_f32_16x16x32_bf16 v[172:175], v[52:55], a[28:31], v[172:175]\n"
    "ds_read_b64_tr_b16 v[22:23], v99 offset:38912\n"
    "s_waitcnt lgkmcnt(12)\n"
    "v_mfma_f32_16x16x32_bf16 v[176:179], v[56:59], a[24:27], v[176:179]\n"
    "ds_read_b64_tr_b16 v[24:25], v102 offset:36864\n"
    "v_mfma_f32_16x16x32_bf16 v[180:183], v[56:59], a[28:31], v[180:183]\n"
    "ds_read_b64_tr_b16 v[26:27], v102 offset:38912\n"
    "buffer_load_dwordx4 v[76:79], v110, s[52:55], 0 offen offset:192\n"
    "v_mfma_f32_16x16x32_bf16 v[184:187], v[60:63], a[24:27], v[184:187]\n"
    "ds_read_b64_tr_b16 v[28:29], v105 offset:36864\n"
    "v_mfma_f32_16x16x32_bf16 v[188:191], v[60:63], a[28:31], v[188:191]\n"
    "ds_read_b64_tr_b16 v[30:31], v105 offset:38912\n"
    "s_waitcnt lgkmcnt(12)\n"
    "v_mfma_f32_16x16x32_bf16 v[128:131], v[0:3], a[32:35], v[128:131]\n"
    "ds_read_b64_tr_b16 v[32:33], v96 offset:40960\n"
    "v_mfma_f32_16x16x32_bf16 v[132:135], v[0:3], a[36:39], v[132:135]\n"
    "ds_read_b64_tr_b16 v[34:35], v96 offset:43008\n"
    "v_mfma_f32_16x16x32_bf16 v[136:139], v[4:7], a[32:35], v[136:139]\n"
    "ds_read_b64_tr_b16 v[36:37], v99 offset:40960\n"
    "v_mfma_f32_16x16x32_bf16 v[140:143], v[4:7], a[36:39], v[140:143]\n"
    "ds_read_b64_tr_b16 v[38:39], v99 offset:43008\n"
    "s_waitcnt lgkmcnt(12)\n"
    "v_mfma_f32_16x16x32_bf16 v[144:147], v[8:11], a[32:35], v[144:147]\n"
    "ds_read_b64_tr_b16 v[40:41], v102 offset:40960\n"
    "v_mfma_f32_16x16x32_bf16 v[148:151], v[8:11], a[36:39], v[148:151]\n"
    "ds_read_b64_tr_b16 v[42:43], v102 offset:43008\n"
    "v_mfma_f32_16x16x32_bf16 v[152:155], v[12:15], a[32:35], v[152:155]\n"
    "ds_read_b64_tr_b16 v[44:45], v105 offset:40960\n"
    "v_mfma_f32_16x16x32_bf16 v[156:159], v[12:15], a[36:39], v[156:159]\n"
    "ds_read_b64_tr_b16 v[46:47], v105 offset:43008\n"
    "s_waitcnt lgkmcnt(12)\n"
    "v_mfma_f32_16x16x32_bf16 v[160:163], v[16:19], a[32:35], v[160:163]\n"
    "ds_read_b64_tr_b16 v[48:49], v96 offset:45056\n"
    "v_mfma_f32_16x16x32_bf16 v[164:167], v[16:19], a[36:39], v[164:167]\n"
    "ds_read_b64_tr_b16 v[50:51], v96 offset:47104\n"
    "v_mfma_f32_16x16x32_bf16 v[168:171], v[20:23], a[32:35], v[168:171]\n"
    "ds_read_b64_tr_b16 v[52:53], v99 offset:45056\n"
    "v_mfma_f32_16x16x32_bf16 v[172:175], v[20:23], a[36:39], v[172:175]\n"
    "ds_read_b64_tr_b16 v[54:55], v99 offset:47104\n"
    "s_waitcnt lgkmcnt(12)\n"
    "v_mfma_f32_16x16x32_bf16 v[176:179], v[24:27], a[32:35], v[176:179]\n"
    "ds_read_b64_tr_b16 v[56:57], v102 offset:45056\n"
    "v_mfma_f32_16x16x32_bf16 v[180:183], v[24:27], a[36:39], v[180:183]\n"
    "ds_read_b64_tr_b16 v[58:59], v102 offset:47104\n"
    "v_mfma_f32_16x16x32_bf16 v[184:187], v[28:31], a[32:35], v[184:187]\n"
    "ds_read_b64_tr_b16 v[60:61], v105 offset:45056\n"
    "v_mfma_f32_16x16x32_bf16 v[188:191], v[28:31], a[36:39], v[188:191]\n"
    "ds_read_b64_tr_b16 v[62:63], v105 offset:47104\n"
    "s_waitcnt vmcnt(44) lgkmcnt(12)\n"
    "s_barrier\n"
    "v_mfma_f32_16x16x32_bf16 v[128:131], v[32:35], a[40:43], v[128:131]\n"
    "ds_read_b64_tr_b16 v[0:1], v97 offset:0\n"
    "v_mfma_f32_16x16x32_bf16 v[132:135], v[32:35], a[44:47], v[132:135]\n"
    "ds_read_b64_tr_b16 v[2:3], v97 offset:2048\n"
    "v_mfma_f32_16x16x32_bf16 v[136:139], v[36:39], a[40:43], v[136:139]\n"
    "ds_read_b64_tr_b16 v[4:5], v100 offset:0\n"
    "v_mfma_f32_16x16x32_bf16 v[140:143], v[36:39], a[44:47], v[140:143]\n"
    "ds_read_b64_tr_b16 v[6:7], v100 offset:2048\n"
    "s_waitcnt lgkmcnt(12)\n"
    "v_mfma_f32_16x16x32_bf16 v[144:147], v[40:43], a[40:43], v[144:147]\n"
    "ds_read_b64_tr_b16 v[8:9], v103 offset:0\n"
    "v_mfma_f32_16x16x32_bf16 v[148:151], v[40:43], a[44:47], v[148:151]\n"
    "ds_read_b64_tr_b16 v[10:11], v103 offset:2048\n"
    "v_mfma_f32_16x16x32_bf16 v[152:155], v[44:47], a[40:43], v[152:155]\n"
    "ds_read_b64_tr_b16 v[12:13], v106 offset:0\n"
    "v_mfma_f32_16x16x32_bf16 v[156:159], v[44:47], a[44:47], v[156:159]\n"
    "ds_read_b64_tr_b16 v[14:15], v106 offset:2048\n"
    "s_waitcnt lgkmcnt(12)\n"
    "v_mfma_f32_16x16x32_bf16 v[160:163], v[48:51], a[40:43], v[160:163]\n"
    "ds_read_b64_tr_b16 v[16:17], v97 offset:4096\n"
    "v_mfma_f32_16x16x32_bf16 v[164:167], v[48:51], a[44:47], v[164:167]\n"
    "ds_read_b64_tr_b16 v[18:19], v97 offset:6144\n"
    "v_mfma_f32_16x16x32_bf16 v[168:171], v[52:55], a[40:43], v[168:171]\n"
    "ds_read_b64_tr_b16 v[20:21], v100 offset:4096\n"
    "v_mfma_f32_16x16x32_bf16 v[172:175], v[52:55], a[44:47], v[172:175]\n"
    "ds_read_b64_tr_b16 v[22:23], v100 offset:6144\n"
    "s_waitcnt lgkmcnt(12)\n"
    "v_mfma_f32_16x16x32_bf16 v[176:179], v[56:59], a[40:43], v[176:179]\n"
    "ds_read_b64_tr_b16 v[24:25], v103 offset:4096\n"
    "v_mfma_f32_16x16x32_bf16 v[180:183], v[56:59], a[44:47], v[180:183]\n"
    "ds_read_b64_tr_b16 v[26:27], v103 offset:6144\n"
    "v_mfma_f32_16x16x32_bf16 v[184:187], v[60:63], a[40:43], v[184:187]\n"
    "ds_read_b64_tr_b16 v[28:29], v106 offset:4096\n"
    "v_mfma_f32_16x16x32_bf16 v[188:191], v[60:63], a[44:47], v[188:191]\n"
    "ds_read_b64_tr_b16 v[30:31], v106 offset:6144\n"
    "s_waitcnt lgkmcnt(12)\n"
    "v_mfma_f32_16x16x32_bf16 v[128:131], v[0:3], a[48:51], v[128:131]\n"
    "ds_read_b64_tr_b16 v[32:33], v97 offset:8192\n"
    "v_mfma_f32_16x16x32_bf16 v[132:135], v[0:3], a[52:55], v[132:135]\n"
    "ds_read_b64_tr_b16 v[34:35], v97 offset:10240\n"
    "v_mfma_f32_16x16x32_bf16 v[136:139], v[4:7], a[48:51], v[136:139]\n"
    "ds_read_b64_tr_b16 v[36:37], v100 offset:8192\n"
    "v_mfma_f32_16x16x32_bf16 v[140:143], v[4:7], a[52:55], v[140:143]\n"
    "ds_read_b64_tr_b16 v[38:39], v100 offset:10240\n"
    "s_waitcnt lgkmcnt(12)\n"
    "v_mfma_f32_16x16x32_bf16 v[144:147], v[8:11], a[48:51], v[144:147]\n"
    "ds_read_b64_tr_b16 v[40:41], v103 offset:8192\n"
    "v_mfma_f32_16x16x32_bf16 v[148:151], v[8:11], a[52:55], v[148:151]\n"
    "ds_read_b64_tr_b16 v[42:43], v103 offset:10240\n"
    "v_mfma_f32_16x16x32_bf16 v[152:155], v[12:15], a[48:51], v[152:155]\n"
    "ds_read_b64_tr_b16 v[44:45], v106 offset:8192\n"
    "v_mfma_f32_16x16x32_bf16 v[156:159], v[12:15], a[52:55], v[156:159]\n"
    "ds_read_b64_tr_b16 v[46:47], v106 offset:10240\n"
    "s_waitcnt lgkmcnt(12)\n"
    "v_mfma_f32_16x16x32_bf16 v[160:163], v[16:19], a[48:51], v[160:163]\n"
    "ds_read_b64_tr_b16 v[48:49], v97 offset:12288\n"
    "v_mfma_f32_16x16x32_bf16 v[164:167], v[16:19], a[52:55], v[164:167]\n"
    "ds_read_b64_tr_b16 v[50:51], v97 offset:14336\n"
    "v_mfma_f32_16x16x32_bf16 v[168:171], v[20:23], a[48:51], v[168:171]\n"
    "ds_read_b64_tr_b16 v[52:53], v100 offset:12288\n"
    "v_mfma_f32_16x16x32_bf16 v[172:175], v[20:23], a[52:55], v[172:175]\n"
    "ds_read_b64_tr_b16 v[54:55], v100 offset:14336\n"
    "s_waitcnt lgkmcnt(12)\n"
    "v_mfma_f32_16x16x32_bf16 v[176:179], v[24:27], a[48:51], v[176:179]\n"
    "ds_read_b64_tr_b16 v[56:57], v103 offset:12288\n"
    "v_mfma_f32_16x16x32_bf16 v[180:183], v[24:27], a[52:55], v[180:183]\n"
    "ds_read_b64_tr_b16 v[58:59], v103 offset:14336\n"
    "v_mfma_f32_16x16x32_bf16 v[184:187], v[28:31], a[48:51], v[184:187]\n"
    "ds_read_b64_tr_b16 v[60:61], v106 offset:12288\n"
    "v_mfma_f32_16x16x32_bf16 v[188:191], v[28:31], a[52:55], v[188:191]\n"
    "ds_read_b64_tr_b16 v[62:63], v106 offset:14336\n"
    "s_waitcnt vmcnt(20) lgkmcnt(14)\n"
    "s_barrier\n"
    "v_mfma_f32_16x16x32_bf16 v[128:131], v[32:35], a[56:59], v[128:131]\n"
    "ds_read_b64_tr_b16 v[0:1], v97 offset:16384\n"
    "ds_read_b64_tr_b16 v[192:193], v98 offset:8192\n"
    "v_mfma_f32_16x16x32_bf16 v[132:135], v[32:35], a[60:63], v[132:135]\n"
    "ds_read_b64_tr_b16 v[2:3], v97 offset:18432\n"
    "s_waitcnt lgkmcnt(15)\n"
    "v_mfma_f32_16x16x32_bf16 v[136:139], v[36:39], a[56:59], v[136:139]\n"
    "ds_read_b64_tr_b16 v[4:5], v100 offset:16384\n"
    "ds_read_b64_tr_b16 v[194:195], v98 offset:10240\n"
    "v_mfma_f32_16x16x32_bf16 v[140:143], v[36:39], a[60:63], v[140:143]\n"
    "ds_read_b64_tr_b16 v[6:7], v100 offset:18432\n"
    "s_waitcnt lgkmcnt(15)\n"
    "v_mfma_f32_16x16x32_bf16 v[144:147], v[40:43], a[56:59], v[144:147]\n"
    "ds_read_b64_tr_b16 v[8:9], v103 offset:16384\n"
    "ds_read_b64_tr_b16 v[196:197], v101 offset:8192\n"
    "v_mfma_f32_16x16x32_bf16 v[148:151], v[40:43], a[60:63], v[148:151]\n"
    "ds_read_b64_tr_b16 v[10:11], v103 offset:18432\n"
    "s_waitcnt lgkmcnt(15)\n"
    "v_mfma_f32_16x16x32_bf16 v[152:155], v[44:47], a[56:59], v[152:155]\n"
    "ds_read_b64_tr_b16 v[12:13], v106 offset:16384\n"
    "ds_read_b64_tr_b16 v[198:199], v101 offset:10240\n"
    "v_mfma_f32_16x16x32_bf16 v[156:159], v[44:47], a[60:63], v[156:159]\n"
    "ds_read_b64_tr_b16 v[14:15], v106 offset:18432\n"
    "v_mfma_f32_16x16x32_bf16 v[160:163], v[48:51], a[56:59], v[160:163]\n"
    "ds_read_b64_tr_b16 v[16:17], v97 offset:20480\n"
    "ds_read_b64_tr_b16 v[200:201], v104 offset:8192\n"
    "v_mfma_f32_16x16x32_bf16 v[164:167], v[48:51], a[60:63], v[164:167]\n"
    "ds_read_b64_tr_b16 v[18:19], v97 offset:22528\n"
    "s_waitcnt lgkmcnt(15)\n"
    "v_mfma_f32_16x16x32_bf16 v[168:171], v[52:55], a[56:59], v[168:171]\n"
    "ds_read_b64_tr_b16 v[20:21], v100 offset:20480\n"
    "ds_read_b64_tr_b16 v[202:203], v104 offset:10240\n"
    "v_mfma_f32_16x16x32_bf16 v[172:175], v[52:55], a[60:63], v[172:175]\n"
    "ds_read_b64_tr_b16 v[22:23], v100 offset:22528\n"
    "v_mfma_f32_16x16x32_bf16 v[176:179], v[56:59], a[56:59], v[176:179]\n"
    "ds_read_b64_tr_b16 v[24:25], v103 offset:20480\n"
    "ds_read_b64_tr_b16 v[204:205], v107 offset:8192\n"
    "v_mfma_f32_16x16x32_bf16 v[180:183], v[56:59], a[60:63], v[180:183]\n"
    "ds_read_b64_tr_b16 v[26:27], v103 offset:22528\n"
    "v_mfma_f32_16x16x32_bf16 v[184:187], v[60:63], a[56:59], v[184:187]\n"
    "ds_read_b64_tr_b16 v[28:29], v106 offset:20480\n"
    "ds_read_b64_tr_b16 v[206:207], v107 offset:10240\n"
    "v_mfma_f32_16x16x32_bf16 v[188:191], v[60:63], a[60:63], v[188:191]\n"
    "ds_read_b64_tr_b16 v[30:31], v106 offset:22528\n"
    "s_waitcnt lgkmcnt(15)\n"
    "v_mfma_f32_16x16x32_bf16 v[128:131], v[0:3], a[64:67], v[128:131]\n"
    "ds_read_b64_tr_b16 v[32:33], v97 offset:24576\n"
    "ds_read_b64_tr_b16 v[208:209], v98 offset:12288\n"
    "v_mfma_f32_16x16x32_bf16 v[132:135], v[0:3], a[68:71], v[132:135]\n"
    "ds_read_b64_tr_b16 v[34:35], v97 offset:26624\n"
    "v_mfma_f32_16x16x32_bf16 v[136:139], v[4:7], a[64:67], v[136:139]\n"
    "ds_read_b64_tr_b16 v[36:37], v100 offset:24576\n"
    "ds_read_b64_tr_b16 v[210:211], v98 offset:14336\n"
    "v_mfma_f32_16x16x32_bf16 v[140:143], v[4:7], a[68:71], v[140:143]\n"
    "ds_read_b64_tr_b16 v[38:39], v100 offset:26624\n"
    "v_mfma_f32_16x16x32_bf16 v[144:147], v[8:11], a[64:67], v[144:147]\n"
    "ds_read_b64_tr_b16 v[40:41], v103 offset:24576\n"
    "ds_read_b64_tr_b16 v[212:213], v101 offset:12288\n"
    "v_mfma_f32_16x16x32_bf16 v[148:151], v[8:11], a[68:71], v[148:151]\n"
    "ds_read_b64_tr_b16 v[42:43], v103 offset:26624\n"
    "s_waitcnt lgkmcnt(15)\n"
    "v_mfma_f32_16x16x32_bf16 v[152:155], v[12:15], a[64:67], v[152:155]\n"
    "ds_read_b64_tr_b16 v[44:45], v106 offset:24576\n"
    "ds_read_b64_tr_b16 v[214:215], v101 offset:14336\n"
    "v_mfma_f32_16x16x32_bf16 v[156:159], v[12:15], a[68:71], v[156:159]\n"
    "ds_read_b64_tr_b16 v[46:47], v106 offset:26624\n"
    "v_mfma_f32_16x16x32_bf16 v[160:163], v[16:19], a[64:67], v[160:163]\n"
    "ds_read_b64_tr_b16 v[48:49], v97 offset:28672\n"
    "ds_read_b64_tr_b16 v[216:217], v104 offset:12288\n"
    "v_mfma_f32_16x16x32_bf16 v[164:167], v[16:19], a[68:71], v[164:167]\n"
    "ds_read_b64_tr_b16 v[50:51], v97 offset:30720\n"
    "v_mfma_f32_16x16x32_bf16 v[168:171], v[20:23], a[64:67], v[168:171]\n"
    "ds_read_b64_tr_b16 v[52:53], v100 offset:28672\n"
    "ds_read_b64_tr_b16 v[218:219], v104 offset:14336\n"
    "v_mfma_f32_16x16x32_bf16 v[172:175], v[20:23], a[68:71], v[172:175]\n"
    "ds_read_b64_tr_b16 v[54:55], v100 offset:30720\n"
    "s_waitcnt lgkmcnt(15)\n"
    "v_mfma_f32_16x16x32_bf16 v[176:179], v[24:27], a[64:67], v[176:179]\n"
    "ds_read_b64_tr_b16 v[56:57], v103 offset:28672\n"
    "ds_read_b64_tr_b16 v[220:221], v107 offset:12288\n"
    "v_mfma_f32_16x16x32_bf16 v[180:183], v[24:27], a[68:71], v[180:183]\n"
    "ds_read_b64_tr_b16 v[58:59], v103 offset:30720\n"
    "v_mfma_f32_16x16x32_bf16 v[184:187], v[28:31], a[64:67], v[184:187]\n"
    "ds_read_b64_tr_b16 v[60:61], v106 offset:28672\n"
    "ds_read_b64_tr_b16 v[222:223], v107 offset:14336\n"
    "v_mfma_f32_16x16x32_bf16 v[188:191], v[28:31], a[68:71], v[188:191]\n"
    "ds_read_b64_tr_b16 v[62:63], v106 offset:30720\n"
    "s_waitcnt vmcnt(12)\n"
    "s_barrier\n"
    "v_mfma_f32_16x16x32_bf16 v[128:131], v[32:35], a[72:75], v[128:131]\n"
    "ds_read_b64_tr_b16 v[0:1], v97 offset:32768\n"
    "ds_read_b64_tr_b16 v[224:225], v98 offset:16384\n"
    "v_mfma_f32_16x16x32_bf16 v[132:135], v[32:35], a[76:79], v[132:135]\n"
    "ds_read_b64_tr_b16 v[2:3], v97 offset:34816\n"
    "s_waitcnt lgkmcnt(15)\n"
    "v_mfma_f32_16x16x32_bf16 v[136:139], v[36:39], a[72:75], v[136:139]\n"
    "ds_read_b64_tr_b16 v[4:5], v100 offset:32768\n"
    "ds_read_b64_tr_b16 v[226:227], v98 offset:18432\n"
    "v_mfma_f32_16x16x32_bf16 v[140:143], v[36:39], a[76:79], v[140:143]\n"
    "ds_read_b64_tr_b16 v[6:7], v100 offset:34816\n"
    "v_mfma_f32_16x16x32_bf16 v[144:147], v[40:43], a[72:75], v[144:147]\n"
    "ds_read_b64_tr_b16 v[8:9], v103 offset:32768\n"
    "ds_read_b64_tr_b16 v[228:229], v101 offset:16384\n"
    "v_mfma_f32_16x16x32_bf16 v[148:151], v[40:43], a[76:79], v[148:151]\n"
    "ds_read_b64_tr_b16 v[10:11], v103 offset:34816\n"
    "v_mfma_f32_16x16x32_bf16 v[152:155], v[44:47], a[72:75], v[152:155]\n"
    "ds_read_b64_tr_b16 v[12:13], v106 offset:32768\n"
    "ds_read_b64_tr_b16 v[230:231], v101 offset:18432\n"
    "v_mfma_f32_16x16x32_bf16 v[156:159], v[44:47], a[76:79], v[156:159]\n"
    "ds_read_b64_tr_b16 v[14:15], v106 offset:34816\n"
    "s_waitcnt lgkmcnt(15)\n"
    "v_mfma_f32_16x16x32_bf16 v[160:163], v[48:51], a[72:75], v[160:163]\n"
    "ds_read_b64_tr_b16 v[16:17], v97 offset:36864\n"
    "ds_read_b64_tr_b16 v[232:233], v104 offset:16384\n"
    "v_mfma_f32_16x16x32_bf16 v[164:167], v[48:51], a[76:79], v[164:167]\n"
    "ds_read_b64_tr_b16 v[18:19], v97 offset:38912\n"
    "v_mfma_f32_16x16x32_bf16 v[168:171], v[52:55], a[72:75], v[168:171]\n"
    "ds_read_b64_tr_b16 v[20:21], v100 offset:36864\n"
    "ds_read_b64_tr_b16 v[234:235], v104 offset:18432\n"
    "v_mfma_f32_16x16x32_bf16 v[172:175], v[52:55], a[76:79], v[172:175]\n"
    "ds_read_b64_tr_b16 v[22:23], v100 offset:38912\n"
    "v_mfma_f32_16x16x32_bf16 v[176:179], v[56:59], a[72:75], v[176:179]\n"
    "ds_read_b64_tr_b16 v[24:25], v103 offset:36864\n"
    "ds_read_b64_tr_b16 v[236:237], v107 offset:16384\n"
    "v_mfma_f32_16x16x32_bf16 v[180:183], v[56:59], a[76:79], v[180:183]\n"
    "ds_read_b64_tr_b16 v[26:27], v103 offset:38912\n"
    "s_waitcnt lgkmcnt(15)\n"
    "v_mfma_f32_16x16x32_bf16 v[184:187], v[60:63], a[72:75], v[184:187]\n"
    "ds_read_b64_tr_b16 v[28:29], v106 offset:36864\n"
    "ds_read_b64_tr_b16 v[238:239], v107 offset:18432\n"
    "v_mfma_f32_16x16x32_bf16 v[188:191], v[60:63], a[76:79], v[188:191]\n"
    "ds_read_b64_tr_b16 v[30:31], v106 offset:38912\n"
    "v_mfma_f32_16x16x32_bf16 v[128:131], v[0:3], a[80:83], v[128:131]\n"
    "ds_read_b64_tr_b16 v[32:33], v97 offset:40960\n"
    "ds_read_b64_tr_b16 v[240:241], v98 offset:20480\n"
    "v_mfma_f32_16x16x32_bf16 v[132:135], v[0:3], a[84:87], v[132:135]\n"
    "ds_read_b64_tr_b16 v[34:35], v97 offset:43008\n"
    "v_mfma_f32_16x16x32_bf16 v[136:139], v[4:7], a[80:83], v[136:139]\n"
    "ds_read_b64_tr_b16 v[36:37], v100 offset:40960\n"
    "ds_read_b64_tr_b16 v[242:243], v98 offset:22528\n"
    "v_mfma_f32_16x16x32_bf16 v[140:143], v[4:7], a[84:87], v[140:143]\n"
    "ds_read_b64_tr_b16 v[38:39], v100 offset:43008\n"
    "s_waitcnt lgkmcnt(15)\n"
    "v_mfma_f32_16x16x32_bf16 v[144:147], v[8:11], a[80:83], v[144:147]\n"
    "ds_read_b64_tr_b16 v[40:41], v103 offset:40960\n"
    "ds_read_b64_tr_b16 v[244:245], v101 offset:20480\n"
    "v_mfma_f32_16x16x32_bf16 v[148:151], v[8:11], a[84:87], v[148:151]\n"
    "ds_read_b64_tr_b16 v[42:43], v103 offset:43008\n"
    "v_mfma_f32_16x16x32_bf16 v[152:155], v[12:15], a[80:83], v[152:155]\n"
    "ds_read_b64_tr_b16 v[44:45], v106 offset:40960\n"
    "ds_read_b64_tr_b16 v[246:247], v101 offset:22528\n"
    "v_mfma_f32_16x16x32_bf16 v[156:159], v[12:15], a[84:87], v[156:159]\n"
    "ds_read_b64_tr_b16 v[46:47], v106 offset:43008\n"
    "v_mfma_f32_16x16x32_bf16 v[160:163], v[16:19], a[80:83], v[160:163]\n"
    "ds_read_b64_tr_b16 v[48:49], v97 offset:45056\n"
    "ds_read_b64_tr_b16 v[248:249], v104 offset:20480\n"
    "v_mfma_f32_16x16x32_bf16 v[164:167], v[16:19], a[84:87], v[164:167]\n"
    "ds_read_b64_tr_b16 v[50:51], v97 offset:47104\n"
    "s_waitcnt lgkmcnt(15)\n"
    "v_mfma_f32_16x16x32_bf16 v[168:171], v[20:23], a[80:83], v[168:171]\n"
    "ds_read_b64_tr_b16 v[52:53], v100 offset:45056\n"
    "ds_read_b64_tr_b16 v[250:251], v104 offset:22528\n"
    "v_mfma_f32_16x16x32_bf16 v[172:175], v[20:23], a[84:87], v[172:175]\n"
    "ds_read_b64_tr_b16 v[54:55], v100 offset:47104\n"
    "v_mfma_f32_16x16x32_bf16 v[176:179], v[24:27], a[80:83], v[176:179]\n"
    "ds_read_b64_tr_b16 v[56:57], v103 offset:45056\n"
    "ds_read_b64_tr_b16 v[252:253], v107 offset:20480\n"
    "v_mfma_f32_16x16x32_bf16 v[180:183], v[24:27], a[84:87], v[180:183]\n"
    "ds_read_b64_tr_b16 v[58:59], v103 offset:47104\n"
    "v_mfma_f32_16x16x32_bf16 v[184:187], v[28:31], a[80:83], v[184:187]\n"
    "ds_read_b64_tr_b16 v[60:61], v106 offset:45056\n"
    "ds_read_b64_tr_b16 v[254:255], v107 offset:22528\n"
    "v_mfma_f32_16x16x32_bf16 v[188:191], v[28:31], a[84:87], v[188:191]\n"
    "ds_read_b64_tr_b16 v[62:63], v106 offset:47104\n"
    "s_waitcnt vmcnt(0) lgkmcnt(15)\n"
    "s_barrier\n"
    "v_mfma_f32_16x16x32_bf16 v[128:131], v[32:35], a[88:91], v[128:131]\n"
    "ds_read_b64_tr_b16 v[0:1], v98 offset:0\n"
    "ds_read_b64_tr_b16 a[144:145], v98 offset:24576\n"
    "v_mfma_f32_16x16x32_bf16 v[132:135], v[32:35], a[92:95], v[132:135]\n"
    "ds_read_b64_tr_b16 v[2:3], v98 offset:2048\n"
    "v_mfma_f32_16x16x32_bf16 v[136:139], v[36:39], a[88:91], v[136:139]\n"
    "ds_read_b64_tr_b16 v[4:5], v101 offset:0\n"
    "ds_read_b64_tr_b16 a[146:147], v98 offset:26624\n"
    "v_mfma_f32_16x16x32_bf16 v[140:143], v[36:39], a[92:95], v[140:143]\n"
    "ds_read_b64_tr_b16 v[6:7], v101 offset:2048\n"
    "v_mfma_f32_16x16x32_bf16 v[144:147], v[40:43], a[88:91], v[144:147]\n"
    "ds_read_b64_tr_b16 v[8:9], v104 offset:0\n"
    "ds_read_b64_tr_b16 a[148:149], v101 offset:24576\n"
    "v_mfma_f32_16x16x32_bf16 v[148:151], v[40:43], a[92:95], v[148:151]\n"
    "ds_read_b64_tr_b16 v[10:11], v104 offset:2048\n"
    "s_waitcnt lgkmcnt(15)\n"
    "v_mfma_f32_16x16x32_bf16 v[152:155], v[44:47], a[88:91], v[152:155]\n"
    "ds_read_b64_tr_b16 v[12:13], v107 offset:0\n"
    "ds_read_b64_tr_b16 a[150:151], v101 offset:26624\n"
    "v_mfma_f32_16x16x32_bf16 v[156:159], v[44:47], a[92:95], v[156:159]\n"
    "ds_read_b64_tr_b16 v[14:15], v107 offset:2048\n"
    "v_mfma_f32_16x16x32_bf16 v[160:163], v[48:51], a[88:91], v[160:163]\n"
    "ds_read_b64_tr_b16 v[16:17], v98 offset:4096\n"
    "ds_read_b64_tr_b16 a[152:153], v104 offset:24576\n"
    "v_mfma_f32_16x16x32_bf16 v[164:167], v[48:51], a[92:95], v[164:167]\n"
    "ds_read_b64_tr_b16 v[18:19], v98 offset:6144\n"
    "v_mfma_f32_16x16x32_bf16 v[168:171], v[52:55], a[88:91], v[168:171]\n"
    "ds_read_b64_tr_b16 v[20:21], v101 offset:4096\n"
    "ds_read_b64_tr_b16 a[154:155], v104 offset:26624\n"
    "v_mfma_f32_16x16x32_bf16 v[172:175], v[52:55], a[92:95], v[172:175]\n"
    "ds_read_b64_tr_b16 v[22:23], v101 offset:6144\n"
    "s_waitcnt lgkmcnt(15)\n"
    "v_mfma_f32_16x16x32_bf16 v[176:179], v[56:59], a[88:91], v[176:179]\n"
    "ds_read_b64_tr_b16 v[24:25], v104 offset:4096\n"
    "ds_read_b64_tr_b16 a[156:157], v107 offset:24576\n"
    "v_mfma_f32_16x16x32_bf16 v[180:183], v[56:59], a[92:95], v[180:183]\n"
    "ds_read_b64_tr_b16 v[26:27], v104 offset:6144\n"
    "v_mfma_f32_16x16x32_bf16 v[184:187], v[60:63], a[88:91], v[184:187]\n"
    "ds_read_b64_tr_b16 v[28:29], v107 offset:4096\n"
    "ds_read_b64_tr_b16 a[158:159], v107 offset:26624\n"
    "v_mfma_f32_16x16x32_bf16 v[188:191], v[60:63], a[92:95], v[188:191]\n"
    "ds_read_b64_tr_b16 v[30:31], v107 offset:6144\n"
    "v_mfma_f32_16x16x32_bf16 v[128:131], v[0:3], a[96:99], v[128:131]\n"
    "ds_read_b64_tr_b16 a[160:161], v98 offset:28672\n"
    "s_waitcnt lgkmcnt(15)\n"
    "v_mfma_f32_16x16x32_bf16 v[136:139], v[4:7], a[96:99], v[136:139]\n"
    "ds_read_b64_tr_b16 a[162:163], v98 offset:30720\n"
    "v_mfma_f32_16x16x32_bf16 v[144:147], v[8:11], a[96:99], v[144:147]\n"
    "ds_read_b64_tr_b16 a[164:165], v101 offset:28672\n"
    "s_waitcnt lgkmcnt(15)\n"
    "v_mfma_f32_16x16x32_bf16 v[152:155], v[12:15], a[96:99], v[152:155]\n"
    "ds_read_b64_tr_b16 a[166:167], v101 offset:30720\n"
    "s_waitcnt lgkmcnt(13)\n"
    "v_mfma_f32_16x16x32_bf16 v[160:163], v[16:19], a[96:99], v[160:163]\n"
    "ds_read_b64_tr_b16 a[168:169], v104 offset:28672\n"
    "s_waitcnt lgkmcnt(11)\n"
    "v_mfma_f32_16x16x32_bf16 v[168:171], v[20:23], a[96:99], v[168:171]\n"
    "ds_read_b64_tr_b16 a[170:171], v104 offset:30720\n"
    "s_waitcnt lgkmcnt(9)\n"
    "v_mfma_f32_16x16x32_bf16 v[176:179], v[24:27], a[96:99], v[176:179]\n"
    "ds_read_b64_tr_b16 a[172:173], v107 offset:28672\n"
    "s_waitcnt lgkmcnt(7)\n"
    "v_mfma_f32_16x16x32_bf16 v[184:187], v[28:31], a[96:99], v[184:187]\n"
    "ds_read_b64_tr_b16 a[174:175], v107 offset:30720\n"
    "v_mfma_f32_16x16x32_bf16 v[128:131], v[192:195], a[104:107], v[128:131]\n"
    "ds_read_b64_tr_b16 v[32:33], v98 offset:32768\n"
    "v_mfma_f32_16x16x32_bf16 v[136:139], v[196:199], a[104:107], v[136:139]\n"
    "ds_read_b64_tr_b16 v[34:35], v98 offset:34816\n"
    "v_mfma_f32_16x16x32_bf16 v[144:147], v[200:203], a[104:107], v[144:147]\n"
    "ds_read_b64_tr_b16 v[36:37], v101 offset:32768\n"
    "v_mfma_f32_16x16x32_bf16 v[152:155], v[204:207], a[104:107], v[152:155]\n"
    "ds_read_b64_tr_b16 v[38:39], v101 offset:34816\n"
    "v_mfma_f32_16x16x32_bf16 v[160:163], v[208:211], a[104:107], v[160:163]\n"
    "ds_read_b64_tr_b16 v[40:41], v104 offset:32768\n"
    "v_mfma_f32_16x16x32_bf16 v[168:171], v[212:215], a[104:107], v[168:171]\n"
    "ds_read_b64_tr_b16 v[42:43], v104 offset:34816\n"
    "v_mfma_f32_16x16x32_bf16 v[176:179], v[216:219], a[104:107], v[176:179]\n"
    "ds_read_b64_tr_b16 v[44:45], v107 offset:32768\n"
    "v_mfma_f32_16x16x32_bf16 v[184:187], v[220:223], a[104:107], v[184:187]\n"
    "ds_read_b64_tr_b16 v[46:47], v107 offset:34816\n"
    "v_mfma_f32_16x16x32_bf16 v[128:131], v[224:227], a[112:115], v[128:131]\n"
    "ds_read_b64_tr_b16 v[48:49], v98 offset:36864\n"
    "v_mfma_f32_16x16x32_bf16 v[136:139], v[228:231], a[112:115], v[136:139]\n"
    "ds_read_b64_tr_b16 v[50:51], v98 offset:38912\n"
    "v_mfma_f32_16x16x32_bf16 v[144:147], v[232:235], a[112:115], v[144:147]\n"
    "ds_read_b64_tr_b16 v[52:53], v101 offset:36864\n"
    "v_mfma_f32_16x16x32_bf16 v[152:155], v[236:239], a[112:115], v[152:155]\n"
    "ds_read_b64_tr_b16 v[54:55], v101 offset:38912\n"
    "v_mfma_f32_16x16x32_bf16 v[160:163], v[240:243], a[112:115], v[160:163]\n"
    "ds_read_b64_tr_b16 v[56:57], v104 offset:36864\n"
    "v_mfma_f32_16x16x32_bf16 v[168:171], v[244:247], a[112:115], v[168:171]\n"
    "ds_read_b64_tr_b16 v[58:59], v104 offset:38912\n"
    "v_mfma_f32_16x16x32_bf16 v[176:179], v[248:251], a[112:115], v[176:179]\n"
    "ds_read_b64_tr_b16 v[60:61], v107 offset:36864\n"
    "v_mfma_f32_16x16x32_bf16 v[184:187], v[252:255], a[112:115], v[184:187]\n"
    "ds_read_b64_tr_b16 v[62:63], v107 offset:38912\n"
    "v_mfma_f32_16x16x32_bf16 v[128:131], a[144:147], a[120:123], v[128:131]\n"
    "ds_read_b64_tr_b16 a[176:177], v98 offset:40960\n"
    "v_mfma_f32_16x16x32_bf16 v[136:139], a[148:151], a[120:123], v[136:139]\n"
    "ds_read_b64_tr_b16 a[178:179], v98 offset:43008\n"
    "v_mfma_f32_16x16x32_bf16 v[144:147], a[152:155], a[120:123], v[144:147]\n"
    "ds_read_b64_tr_b16 a[180:181], v101 offset:40960\n"
    "v_mfma_f32_16x16x32_bf16 v[152:155], a[156:159], a[120:123], v[152:155]\n"
    "ds_read_b64_tr_b16 a[182:183], v101 offset:43008\n"
    "s_waitcnt lgkmcnt(15)\n"
    "v_mfma_f32_16x16x32_bf16 v[160:163], a[160:163], a[120:123], v[160:163]\n"
    "ds_read_b64_tr_b16 a[184:185], v104 offset:40960\n"
    "v_mfma_f32_16x16x32_bf16 v[168:171], a[164:167], a[120:123], v[168:171]\n"
    "ds_read_b64_tr_b16 a[186:187], v104 offset:43008\n"
    "v_mfma_f32_16x16x32_bf16 v[176:179], a[168:171], a[120:123], v[176:179]\n"
    "ds_read_b64_tr_b16 a[188:189], v107 offset:40960\n"
    "v_mfma_f32_16x16x32_bf16 v[184:187], a[172:175], a[120:123], v[184:187]\n"
    "ds_read_b64_tr_b16 a[190:191], v107 offset:43008\n"
    "v_mfma_f32_16x16x32_bf16 v[128:131], v[32:35], a[128:131], v[128:131]\n"
    "ds_read_b64_tr_b16 a[192:193], v98 offset:45056\n"
    "v_mfma_f32_16x16x32_bf16 v[136:139], v[36:39], a[128:131], v[136:139]\n"
    "ds_read_b64_tr_b16 a[194:195], v98 offset:47104\n"
    "s_waitcnt lgkmcnt(15)\n"
    "v_mfma_f32_16x16x32_bf16 v[144:147], v[40:43], a[128:131], v[144:147]\n"
    "ds_read_b64_tr_b16 a[196:197], v101 offset:45056\n"
    "v_mfma_f32_16x16x32_bf16 v[152:155], v[44:47], a[128:131], v[152:155]\n"
    "ds_read_b64_tr_b16 a[198:199], v101 offset:47104\n"
    "v_mfma_f32_16x16x32_bf16 v[160:163], v[48:51], a[128:131], v[160:163]\n"
    "ds_read_b64_tr_b16 a[200:201], v104 offset:45056\n"
    "s_waitcnt lgkmcnt(15)\n"
    "v_mfma_f32_16x16x32_bf16 v[168:171], v[52:55], a[128:131], v[168:171]\n"
    "ds_read_b64_tr_b16 a[202:203], v104 offset:47104\n"
    "v_mfma_f32_16x16x32_bf16 v[176:179], v[56:59], a[128:131], v[176:179]\n"
    "ds_read_b64_tr_b16 a[204:205], v107 offset:45056\n"
    "s_waitcnt lgkmcnt(15)\n"
    "v_mfma_f32_16x16x32_bf16 v[184:187], v[60:63], a[128:131], v[184:187]\n"
    "ds_read_b64_tr_b16 a[206:207], v107 offset:47104\n"
    "s_waitcnt lgkmcnt(14)\n"
    "v_mfma_f32_16x16x32_bf16 v[128:131], a[176:179], a[136:139], v[128:131]\n"
    "s_waitcnt lgkmcnt(12)\n"
    "v_mfma_f32_16x16x32_bf16 v[136:139], a[180:183], a[136:139], v[136:139]\n"
    "s_waitcnt lgkmcnt(10)\n"
    "v_mfma_f32_16x16x32_bf16 v[144:147], a[184:187], a[136:139], v[144:147]\n"
    "s_waitcnt lgkmcnt(8)\n"
    "v_mfma_f32_16x16x32_bf16 v[152:155], a[188:191], a[136:139], v[152:155]\n"
    "s_waitcnt lgkmcnt(6)\n"
    "v_mfma_f32_16x16x32_bf16 v[160:163], a[192:195], a[136:139], v[160:163]\n"
    "s_waitcnt lgkmcnt(4)\n"
    "v_mfma_f32_16x16x32_bf16 v[168:171], a[196:199], a[136:139], v[168:171]\n"
    "s_waitcnt lgkmcnt(2)\n"
    "v_mfma_f32_16x16x32_bf16 v[176:179], a[200:203], a[136:139], v[176:179]\n"
    "s_waitcnt lgkmcnt(0)\n"
    "v_mfma_f32_16x16x32_bf16 v[184:187], a[204:207], a[136:139], v[184:187]\n"
    "v_mfma_f32_16x16x32_bf16 v[132:135], v[0:3], a[100:103], v[132:135]\n"
    "v_mfma_f32_16x16x32_bf16 v[140:143], v[4:7], a[100:103], v[140:143]\n"
    "v_cvt_pk_bf16_f32 v80, v128, v129\n"
    "v_cvt_pk_bf16_f32 v81, v130, v131\n"
    "v_mfma_f32_16x16x32_bf16 v[148:151], v[8:11], a[100:103], v[148:151]\n"
    "v_cvt_pk_bf16_f32 v82, v136, v137\n"
    "v_cvt_pk_bf16_f32 v83, v138, v139\n"
    "v_mfma_f32_16x16x32_bf16 v[156:159], v[12:15], a[100:103], v[156:159]\n"
    "v_min_u32 v80, -2, v80\n"
    "v_min_u32 v81, -2, v81\n"
    "v_mfma_f32_16x16x32_bf16 v[164:167], v[16:19], a[100:103], v[164:167]\n"
    "v_min_u32 v82, -2, v82\n"
    "v_min_u32 v83, -2, v83\n"
    "v_mfma_f32_16x16x32_bf16 v[172:175], v[20:23], a[100:103], v[172:175]\n"
    "buffer_store_dwordx4 v[80:83], v112, s[64:67], s68 offen sc1\n"
    "v_cvt_pk_bf16_f32 v84, v144, v145\n"
    "v_mfma_f32_16x16x32_bf16 v[180:183], v[24:27], a[100:103], v[180:183]\n"
    "v_cvt_pk_bf16_f32 v85, v146, v147\n"
    "v_cvt_pk_bf16_f32 v86, v152, v153\n"
    "v_mfma_f32_16x16x32_bf16 v[188:191], v[28:31], a[100:103], v[188:191]\n"
    "v_cvt_pk_bf16_f32 v87, v154, v155\n"
    "v_min_u32 v84, -2, v84\n"
    "v_mfma_f32_16x16x32_bf16 v[132:135], v[192:195], a[108:111], v[132:135]\n"
    "v_min_u32 v85, -2, v85\n"
    "v_min_u32 v86, -2, v86\n"
    "v_mfma_f32_16x16x32_bf16 v[140:143], v[196:199], a[108:111], v[140:143]\n"
    "v_min_u32 v87, -2, v87\n"
    "buffer_store_dwordx4 v[84:87], v112, s[64:67], s68 offen offset:1024 sc1\n"
    "v_mfma_f32_16x16x32_bf16 v[148:151], v[200:203], a[108:111], v[148:151]\n"
    "v_cvt_pk_bf16_f32 v88, v160, v161\n"
    "v_cvt_pk_bf16_f32 v89, v162, v163\n"
    "v_mfma_f32_16x16x32_bf16 v[156:159], v[204:207], a[108:111], v[156:159]\n"
    "v_cvt_pk_bf16_f32 v90, v168, v169\n"
    "v_cvt_pk_bf16_f32 v91, v170, v171\n"
    "v_mfma_f32_16x16x32_bf16 v[164:167], v[208:211], a[108:111], v[164:167]\n"
    "v_min_u32 v88, -2, v88\n"
    "v_min_u32 v89, -2, v89\n"
    "v_mfma_f32_16x16x32_bf16 v[172:175], v[212:215], a[108:111], v[172:175]\n"
    "v_min_u32 v90, -2, v90\n"
    "v_min_u32 v91, -2, v91\n"
    "v_mfma_f32_16x16x32_bf16 v[180:183], v[216:219], a[108:111], v[180:183]\n"
    "buffer_store_dwordx4 v[88:91], v112, s[64:67], s68 offen offset:2048 sc1\n"
    "v_cvt_pk_bf16_f32 v92, v176, v177\n"
    "v_mfma_f32_16x16x32_bf16 v[188:191], v[220:223], a[108:111], v[188:191]\n"
    "v_cvt_pk_bf16_f32 v93, v178, v179\n"
    "v_cvt_pk_bf16_f32 v94, v184, v185\n"
    "v_mfma_f32_16x16x32_bf16 v[132:135], v[224:227], a[116:119], v[132:135]\n"
    "v_cvt_pk_bf16_f32 v95, v186, v187\n"
    "v_min_u32 v92, -2, v92\n"
    "v_mfma_f32_16x16x32_bf16 v[140:143], v[228:231], a[116:119], v[140:143]\n"
    "v_min_u32 v93, -2, v93\n"
    "v_min_u32 v94, -2, v94\n"
    "v_mfma_f32_16x16x32_bf16 v[148:151], v[232:235], a[116:119], v[148:151]\n"
    "v_min_u32 v95, -2, v95\n"
    "buffer_store_dwordx4 v[92:95], v112, s[64:67], s68 offen offset:3072 sc1\n"
    "v_mfma_f32_16x16x32_bf16 v[156:159], v[236:239], a[116:119], v[156:159]\n"
    "v_mfma_f32_16x16x32_bf16 v[164:167], v[240:243], a[116:119], v[164:167]\n"
    "v_mfma_f32_16x16x32_bf16 v[172:175], v[244:247], a[116:119], v[172:175]\n"
    "v_mfma_f32_16x16x32_bf16 v[180:183], v[248:251], a[116:119], v[180:183]\n"
    "v_mfma_f32_16x16x32_bf16 v[188:191], v[252:255], a[116:119], v[188:191]\n"
    "v_mfma_f32_16x16x32_bf16 v[132:135], a[144:147], a[124:127], v[132:135]\n"
    "v_mfma_f32_16x16x32_bf16 v[140:143], a[148:151], a[124:127], v[140:143]\n"
    "v_mfma_f32_16x16x32_bf16 v[148:151], a[152:155], a[124:127], v[148:151]\n"
    "v_mfma_f32_16x16x32_bf16 v[156:159], a[156:159], a[124:127], v[156:159]\n"
    "v_mfma_f32_16x16x32_bf16 v[164:167], a[160:163], a[124:127], v[164:167]\n"
    "v_mfma_f32_16x16x32_bf16 v[172:175], a[164:167], a[124:127], v[172:175]\n"
    "v_mfma_f32_16x16x32_bf16 v[180:183], a[168:171], a[124:127], v[180:183]\n"
    "v_mfma_f32_16x16x32_bf16 v[188:191], a[172:175], a[124:127], v[188:191]\n"
    "v_mfma_f32_16x16x32_bf16 v[132:135], v[32:35], a[132:135], v[132:135]\n"
    "v_mfma_f32_16x16x32_bf16 v[140:143], v[36:39], a[132:135], v[140:143]\n"
    "v_mfma_f32_16x16x32_bf16 v[148:151], v[40:43], a[132:135], v[148:151]\n"
    "v_mfma_f32_16x16x32_bf16 v[156:159], v[44:47], a[132:135], v[156:159]\n"
    "v_mfma_f32_16x16x32_bf16 v[164:167], v[48:51], a[132:135], v[164:167]\n"
    "v_mfma_f32_16x16x32_bf16 v[172:175], v[52:55], a[132:135], v[172:175]\n"
    "v_mfma_f32_16x16x32_bf16 v[180:183], v[56:59], a[132:135], v[180:183]\n"
    "v_mfma_f32_16x16x32_bf16 v[188:191], v[60:63], a[132:135], v[188:191]\n"
    "v_mfma_f32_16x16x32_bf16 v[132:135], a[176:179], a[140:143], v[132:135]\n"
    "v_mfma_f32_16x16x32_bf16 v[140:143], a[180:183], a[140:143], v[140:143]\n"
    "v_mfma_f32_16x16x32_bf16 v[148:151], a[184:187], a[140:143], v[148:151]\n"
    "v_mfma_f32_16x16x32_bf16 v[156:159], a[188:191], a[140:143], v[156:159]\n"
    "v_mfma_f32_16x16x32_bf16 v[164:167], a[192:195], a[140:143], v[164:167]\n"
    "v_mfma_f32_16x16x32_bf16 v[172:175], a[196:199], a[140:143], v[172:175]\n"
    "v_mfma_f32_16x16x32_bf16 v[180:183], a[200:203], a[140:143], v[180:183]\n"
    "v_mfma_f32_16x16x32_bf16 v[188:191], a[204:207], a[140:143], v[188:191]\n"
    "s_nop 7\n"
    "s_nop 7\n"
    : "={v[128:131]}"(acc[0]), "={v[132:135]}"(acc[1]), "={v[136:139]}"(acc[2]), "={v[140:143]}"(acc[3]), "={v[144:147]}"(acc[4]), "={v[148:151]}"(acc[5]), "={v[152:155]}"(acc[6]), "={v[156:159]}"(acc[7]), "={v[160:163]}"(acc[8]), "={v[164:167]}"(acc[9]), "={v[168:171]}"(acc[10]), "={v[172:175]}"(acc[11]), "={v[176:179]}"(acc[12]), "={v[180:183]}"(acc[13]), "={v[184:187]}"(acc[14]), "={v[188:191]}"(acc[15]), "={v[64:67]}"(xr[0]), "={v[68:71]}"(xr[1]), "={v[72:75]}"(xr[2]), "={v[76:79]}"(xr[3]), "+{s[56:63]}"(soff), "+{s[44:47]}"(srdW)
    : "{v[96:111]}"(vin), "{s[40:43]}"(srdX), "{s[48:51]}"(srdW2), "{s[52:55]}"(srdR2), "{v112}"(vsl), "{s[64:67]}"(srdSl), "{s68}"(ssl)
    : "memory", "scc", "m0", "v0", "v1", "v2", "v3", "v4", "v5", "v6", "v7", "v8", "v9", "v10", "v11", "v12", "v13", "v14", "v15", "v16", "v17", "v18", "v19", "v20", "v21", "v22", "v23", "v24", "v25", "v26", "v27", "v28", "v29", "v30", "v31", "v32", "v33", "v34", "v35", "v36", "v37", "v38", "v39", "v40", "v41", "v42", "v43", "v44", "v45", "v46", "v47", "v48", "v49", "v50", "v51", "v52", "v53", "v54", "v55", "v56", "v57", "v58", "v59", "v60", "v61", "v62", "v63", "a0", "a1", "a2", "a3", "a4", "a5", "a6", "a7", "a8", "a9", "a10", "a11", "a12", "a13", "a14", "a15", "a16", "a17", "a18", "a19", "a20", "a21", "a22", "a23", "a24", "a25", "a26", "a27", "a28", "a29", "a30", "a31", "a32", "a33", "a34", "a35", "a36", "a37", "a38", "a39", "a40", "a41", "a42", "a43", "a44", "a45", "a46", "a47", "a48", "a49", "a50", "a51", "a52", "a53", "a54", "a55", "a56", "a57", "a58", "a59", "a60", "a61", "a62", "a63", "a64", "a65", "a66", "a67", "a68", "a69", "a70", "a71", "a72", "a73", "a74", "a75", "a76", "a77", "a78", "a79", "a80", "a81", "a82", "a83", "a84", "a85", "a86", "a87", "a88", "a89", "a90", "a91", "a92", "a93", "a94", "a95", "a96", "a97", "a98", "a99", "a100", "a101", "a102", "a103", "a104", "a105", "a106", "a107", "a108", "a109", "a110", "a111", "a112", "a113", "a114", "a115", "a116", "a117", "a118", "a119", "a120", "a121", "a122", "a123", "a124", "a125", "a126", "a127", "a128", "a129", "a130", "a131", "a132", "a133", "a134", "a135", "a136", "a137", "a138", "a139", "a140", "a141", "a142", "a143", "v80", "v81", "v82", "v83", "v84", "v85", "v86", "v87", "v88", "v89", "v90", "v91", "v92", "v93", "v94", "v95", "v192", "v193", "v194", "v195", "v196", "v197", "v198", "v199", "v200", "v201", "v202", "v203", "v204", "v205", "v206", "v207", "v208", "v209", "v210", "v211", "v212", "v213", "v214", "v215", "v216", "v217", "v218", "v219", "v220", "v221", "v222", "v223", "v224", "v225", "v226", "v227", "v228", "v229", "v230", "v231", "v232", "v233", "v234", "v235", "v236", "v237", "v238", "v239", "v240", "v241", "v242", "v243", "v244", "v245", "v246", "v247", "v248", "v249", "v250", "v251", "v252", "v253", "v254", "v255", "a144", "a145", "a146", "a147", "a148", "a149", "a150", "a151", "a152", "a153", "a154", "a155", "a156", "a157", "a158", "a159", "a160", "a161", "a162", "a163", "a164", "a165", "a166", "a167", "a168", "a169", "a170", "a171", "a172", "a173", "a174", "a175", "a176", "a177", "a178", "a179", "a180", "a181", "a182", "a183", "a184", "a185", "a186", "a187", "a188", "a189", "a190", "a191", "a192", "a193", "a194", "a195", "a196", "a197", "a198", "a199", "a200", "a201", "a202", "a203", "a204", "a205", "a206", "a207");

  TS(1)
  const int grp = rb * 8 + cb;
  const int ln = fresh_lane();
  const u32 ob = vin[14];
  k2_epi_t(acc, xr, ws, out, (u32)(grp * 2 + j) * 16384u, w, ln, ob TS_ARG);
  TS(5)
  TS_END(1)
}
__global__ __launch_bounds__(256, 1) void k12_gemm(const u16* __restrict__ x, const u16* __restrict__ wqkv, const u16* __restrict__ wg, const u16* __restrict__ wu, const u16* __restrict__ wo, const u16* __restrict__ wd, char* __restrict__ ws, u16* __restrict__ out) {
  extern __shared__ __attribute__((aligned(16))) char smem[];
  const int w = __builtin_amdgcn_readfirstlane(threadIdx.x >> 6);
  unsigned long long tag;
  if ((blockIdx.x >> 6) < 2) k1_body<true>(x, wqkv, wg, wu, wo, wd, ws, smem, w, tag);
  else k1_body<false>(x, wqkv, wg, wu, wo, wd, ws, smem, w, tag);
  k2_body(ws, smem, w, tag);
  asm volatile("k2code_end:");
}

extern "C" void kernel_launch(void* const* d_in, const int* in_sizes, int n_in, void* d_out, int out_size, void* d_ws, size_t ws_size, hipStream_t stream) {
  const u16* x = (const u16*)d_in[0]; const u16* wqkv = (const u16*)d_in[1]; const u16* wo = (const u16*)d_in[2];
  const u16* wg = (const u16*)d_in[4]; const u16* wu = (const u16*)d_in[5]; const u16* wd = (const u16*)d_in[6];
  char* ws = (char*)d_ws;
  (void)hipFuncSetAttribute((const void*)k12_gemm, hipFuncAttributeMaxDynamicSharedMemorySize, K12_LDS);
  k12_gemm<<<256, 256, K12_LDS, stream>>>(x, wqkv, wg, wu, wo, wd, ws, (u16*)d_out);
}
```
